```python
import torch

def _softmax(x, axis=-1):
    return torch.softmax(x, dim=axis)
def _gelu(x):
    return torch.nn.functional.gelu(x, approximate="tanh")
def _silu(x):
    return torch.nn.functional.silu(x)
def _sigmoid(x):
    return torch.sigmoid(x)
def _layernorm(x, axis=-1, eps=1e-5):
    m = x.mean(dim=axis, keepdim=True)
    v = x.var(dim=axis, keepdim=True, unbiased=False)
    return (x - m) / torch.sqrt(v + eps)
def _scatter_add(tgt, idx, src, axis=0):
    return tgt.clone().index_add_(axis, idx, src)
def _segment_sum(src, seg, n):
    out = torch.zeros((n,) + src.shape[1:], dtype=src.dtype)
    return out.index_add_(0, seg, src)
def _pad(x, pw):
    flat = tuple(v for pair in reversed(pw) for v in pair)
    return torch.nn.functional.pad(x, flat)

kernel_name = 'recurrent_scan__ssm_conv_h3__mixer_channel_mlp__prompt'

def setup_inputs():

    _w = (lambda a: (lambda t: t.to(torch.bfloat16) if t.is_floating_point() else t)(torch.as_tensor(a)))
    def _A_setup():
        B, S, D = 1, 2048, 512
        ks = 4
        return {
            "q": _w((lambda *s: torch.randn(*s, dtype=torch.bfloat16) * 0.05)(B, S, D)),
            "k": _w((lambda *s: torch.randn(*s, dtype=torch.bfloat16) * 0.8)(B, S, D)),
            "v": _w((lambda *s: torch.randn(*s, dtype=torch.bfloat16) * 0.8)(B, S, D)),
            "shift_k": _w((lambda *s: torch.randn(*s, dtype=torch.bfloat16) * 0.8)(D, ks) * 0.5),
            "ssm_k": _w((lambda *s: torch.randn(*s, dtype=torch.bfloat16) * 0.8)(D, S) * (1.0 / S ** 0.5)),
            "ssm_D": _w((lambda *s: torch.randn(*s, dtype=torch.bfloat16) * 0.8)(D)),
        }
    _A_in = _A_setup()

    def _B_setup():
        B, S, D, F = 1, 2048, 512, 1792
        return {
            "x": _w((lambda *s: torch.randn(*s, dtype=torch.bfloat16) * 0.05)(B, S, D)),
            "w1": _w((lambda *s: torch.randn(*s, dtype=torch.bfloat16) * 0.8)(D, F) * (D ** -0.5)),
            "b1": _w((lambda *s: torch.randn(*s, dtype=torch.bfloat16) * 0.4)(F)),
            "w2": _w((lambda *s: torch.randn(*s, dtype=torch.bfloat16) * 0.2)(F, D) * (F ** -0.5)),
            "b2": _w((lambda *s: torch.randn(*s, dtype=torch.bfloat16) * 0.2)(D)),
        }
    _B_in = _B_setup()

    return {
        "A_q": _A_in["q"],
        "A_k": _A_in["k"],
        "A_v": _A_in["v"],
        "A_shift_k": _A_in["shift_k"],
        "A_ssm_k": _A_in["ssm_k"],
        "A_ssm_D": _A_in["ssm_D"],
        "B_x": _B_in["x"],
        "B_w1": _B_in["w1"],
        "B_b1": _B_in["b1"],
        "B_w2": _B_in["w2"],
        "B_b2": _B_in["b2"],
    }

def reference(A_q, A_k, A_v, A_shift_k, A_ssm_k, A_ssm_D, B_x, B_w1, B_b1, B_w2, B_b2):
    def _A_ref(q, k, v, shift_k, ssm_k, ssm_D):
        B, S, D = q.shape
        ks = shift_k.shape[-1]
        k_pad = _pad(k, ((0, 0), (ks - 1, 0), (0, 0)))
        k_shift = torch.zeros_like(k)
        for t in range(ks):
            k_shift = k_shift + k_pad[:, t:t + S, :] * shift_k[:, ks - 1 - t][None, None, :]
        kv = k_shift * v
        L = 2 * S
        kv_pad = _pad(kv, ((0, 0), (0, S), (0, 0)))
        kf_pad = _pad(ssm_k, ((0, 0), (0, S)))
        kv_f = (lambda x, axes=(-2,-1): torch.fft.rfft2(x.float(), dim=axes))(kv_pad[:, None, :, :], axes=(1, 2))
        kf_f = (lambda x, axes=(-2,-1): torch.fft.rfft2(x.float(), dim=axes))(kf_pad[:, None, :], axes=(1, 2))
        kf_f = (lambda x, *d: x.permute(*d))(kf_f, 1, 2, 0)
        y_f = kv_f * kf_f[None, :, :, :]
        y = (lambda x, s, axes=(-2,-1): torch.fft.irfft2(x, s=s, dim=axes).to(torch.bfloat16))(y_f, (1, L), axes=(1, 2))[:, 0, :S, :]
        y = y + kv * ssm_D[None, None, :]
        return q * y

    def _B_ref(x, w1, b1, w2, b2):
        y = _layernorm(x, axis=-1)
        h = _gelu(torch.einsum("bsd,df->bsf", y, w1) + b1)
        return torch.einsum("bsf,fd->bsd", h, w2) + b2

    x = A_q
    xn = _layernorm(x, axis=-1)
    s = _A_ref(xn, A_k, A_v, A_shift_k, A_ssm_k, A_ssm_D)
    g = _silu(_B_ref(xn, B_w1, B_b1, B_w2, B_b2))
    return x + s * g
```

```cpp
#include <hip/hip_runtime.h>
#include <hip/hip_bf16.h>
typedef unsigned short u16;
constexpr int S=2048, D=512, F=1792;
__device__ __forceinline__ float bf2f(u16 x){ return __uint_as_float(((unsigned)x)<<16); }
__device__ __forceinline__ u16 f2bf(float f){ __hip_bfloat16 b = __float2bfloat16(f); return *(u16*)&b; }
__device__ __forceinline__ float rb(float f){ return bf2f(f2bf(f)); }

__device__ float wsum(float v){ for(int o=32;o>0;o>>=1) v+=__shfl_xor(v,o); return v; }

__global__ void k_ln(const u16* q, u16* xn, u16* y2){
  int r=blockIdx.x; int l=threadIdx.x;
  float x[8]; float s=0;
  for(int j=0;j<8;j++){ x[j]=bf2f(q[r*D+l*8+j]); s+=x[j]; }
  for(int pass=0;pass<2;pass++){
    s=0; for(int j=0;j<8;j++) s+=x[j];
    float mean=wsum(s)/D; float v=0; for(int j=0;j<8;j++){ float d=x[j]-mean; v+=d*d; }
    v=wsum(v)/D; float m=rb(mean); v=rb(v); float sd=rb(sqrtf(rb(v+1e-5f)));
    for(int j=0;j<8;j++){ x[j]=rb(rb(x[j]-m)/sd); (pass?y2:xn)[r*D+l*8+j]=f2bf(x[j]); }
  }
}
__global__ void k_kv(const u16* k,const u16* v,const u16* sk,u16* kvT){
  int i=blockIdx.x*blockDim.x+threadIdx.x; int s=i/D, d=i%D;
  float a=0;
  for(int t=0;t<4;t++){ int ss=s+t-3; float kk= ss>=0? bf2f(k[ss*D+d]):0.f; a=rb(a+rb(kk*bf2f(sk[d*4+3-t]))); }
  kvT[d*S+s]=f2bf(a*bf2f(v[i]));
}
__global__ void k_conv(const u16* kvT,const u16* h,const u16* Dd,u16* y){
  __shared__ float su[S], sh[S];
  int d=blockIdx.x;
  for(int i=threadIdx.x;i<S;i+=blockDim.x){ su[i]=bf2f(kvT[d*S+i]); sh[i]=bf2f(h[d*S+i]); }
  __syncthreads();
  for(int s=threadIdx.x;s<S;s+=blockDim.x){ float a=0; for(int l=0;l<=s;l++) a+=sh[l]*su[s-l];
    y[s*D+d]=f2bf(rb(a)+rb(su[s]*bf2f(Dd[d]))); }
}
__global__ void k_g1(const u16* y2,const u16* w1,const u16* b1,u16* h){
  int f=blockIdx.x*blockDim.x+threadIdx.x; int r0=blockIdx.y*8;
  float a[8]={};
  for(int k=0;k<D;k++){ float w=bf2f(w1[k*F+f]); for(int j=0;j<8;j++) a[j]+=bf2f(y2[(r0+j)*D+k])*w; }
  for(int j=0;j<8;j++){ float x=rb(rb(a[j])+bf2f(b1[f])); float t=tanhf(0.7978845608f*(x+0.044715f*x*x*x)); h[(r0+j)*F+f]=f2bf(0.5f*x*(1.f+t)); }
}
__global__ void k_g2(const u16* h,const u16* w2,const u16* b2,const u16* q,const u16* xn,const u16* y,u16* out){
  int c=blockIdx.x*blockDim.x+threadIdx.x; int r0=blockIdx.y*8;
  float a[8]={};
  for(int k=0;k<F;k++){ float w=bf2f(w2[k*D+c]); for(int j=0;j<8;j++) a[j]+=bf2f(h[(r0+j)*F+k])*w; }
  for(int j=0;j<8;j++){ int i=(r0+j)*D+c; float x=rb(rb(a[j])+bf2f(b2[c])); float g=rb(x/(1.f+__expf(-x)));
    float s=rb(bf2f(xn[i])*bf2f(y[i])); out[i]=f2bf(bf2f(q[i])+rb(s*g)); }
}
extern "C" void kernel_launch(void* const* d_in, const int* in_sizes, int n_in, void* d_out, int out_size, void* d_ws, size_t ws_size, hipStream_t stream) {
  const u16* q=(const u16*)d_in[0]; const u16* k=(const u16*)d_in[1]; const u16* v=(const u16*)d_in[2];
  const u16* sk=(const u16*)d_in[3]; const u16* ssk=(const u16*)d_in[4]; const u16* sD=(const u16*)d_in[5];
  const u16* w1=(const u16*)d_in[7]; const u16* b1=(const u16*)d_in[8]; const u16* w2=(const u16*)d_in[9]; const u16* b2=(const u16*)d_in[10];
  u16* ws=(u16*)d_ws; u16* xn=ws; u16* y2=xn+S*D; u16* kvT=y2+S*D; u16* y=kvT+S*D; u16* h=y+S*D;
  k_ln<<<S,64,0,stream>>>(q,xn,y2);
  k_kv<<<S*D/256,256,0,stream>>>(k,v,sk,kvT);
  k_conv<<<D,256,0,stream>>>(kvT,ssk,sD,y);
  k_g1<<<dim3(F/256,S/8),256,0,stream>>>(y2,w1,b1,h);
  k_g2<<<dim3(D/256,S/8),256,0,stream>>>(h,w2,b2,q,xn,y,(u16*)d_out);
}
```

```cpp
#include <hip/hip_runtime.h>
typedef unsigned short u16;
typedef unsigned u32;
typedef unsigned long long u64;
typedef __bf16 bf16x8 __attribute__((ext_vector_type(8)));
typedef __bf16 bf16x2 __attribute__((ext_vector_type(2)));
typedef short s16x4 __attribute__((ext_vector_type(4)));
typedef float f32x4 __attribute__((ext_vector_type(4)));
typedef float f32x2 __attribute__((ext_vector_type(2)));
typedef unsigned u32x4 __attribute__((ext_vector_type(4)));
typedef unsigned u32x2 __attribute__((ext_vector_type(2)));
#define LAS __attribute__((address_space(3)))
#define GAS __attribute__((address_space(1)))
#define DI __device__ __forceinline__
#pragma clang diagnostic ignored "-Wint-to-pointer-cast"

constexpr int S_ = 2048, D_ = 512, F_ = 1792;
constexpr u32 OFF_E = 0, OFF_FU = 4096, OFF_FH = 8192, OFF_FY = 12288, OFF_FW = 16384, OFF_ST = 32768;
constexpr u32 OFF_U = 1u << 20, OFF_Y = 3u << 20, OFF_W2T = 5u << 20, OFF_H = 7u << 20, WS_BYTES = 14u << 20;
constexpr u32 SLOT = 32768, L_PK = 131072, L_PV = 140288, L_RAW = 148480, L_SK = 156800, L_B1 = 157312, L_CS = 157568, L_CNT = 158592,
              L_HC = 0, HCS = 4160, L_UR = 33280, URS = 4576, L_CP = 98304;

#define STAMP(i)
#define STAMP_INIT
#define STAMP_END
#define STAMP2(i)

DI u32x4 mk_srd(const void* p, u32 bytes) { u64 a = (u64)p; return u32x4{(u32)a, (u32)(a >> 32) & 0xffffu, bytes, 0x00020000u}; }
DI void dma16(u32x4 srd, u32 voff, u32 soff, u32 lds) {
  asm volatile("s_mov_b32 m0, %3\n\ts_nop 0\n\tbuffer_load_dwordx4 %0, %1, %2 offen lds" ::"v"(voff), "s"(srd), "s"(soff), "s"(lds) : "memory");
}
DI void dma16m(u32x4 srd, u32 voff, u32 soff, u32 lds, u64 mask) {
  asm volatile("s_mov_b32 m0, %3\n\ts_mov_b64 exec, %4\n\tbuffer_load_dwordx4 %0, %1, %2 offen lds\n\ts_mov_b64 exec, -1"
               ::"v"(voff), "s"(srd), "s"(soff), "s"(lds), "s"(mask) : "memory");
}
template <int N> DI void waitv() { asm volatile("s_waitcnt vmcnt(%0)" ::"i"(N) : "memory"); }
#define WAITL0() asm volatile("s_waitcnt lgkmcnt(0)" ::: "memory")
#define CFENCE() asm volatile("" ::: "memory")
#define SB() __builtin_amdgcn_sched_barrier(0)
DI void bar() { SB(); WAITL0(); __builtin_amdgcn_s_barrier(); CFENCE(); SB(); }
DI float bflo(u32 x) { return __uint_as_float(x << 16); }
DI float bfhi(u32 x) { return __uint_as_float(x & 0xffff0000u); }
DI u32 pk2(float a, float b) { f32x2 p = {a, b}; return __builtin_bit_cast(u32, __builtin_convertvector(p, bf16x2)); }
DI float rb(float a) { return bflo(pk2(a, a)); }
template <class T> DI T lds_ld(u32 off) { return *(const LAS T*)(off); }
template <class T> DI void lds_st(u32 off, T v) { *(LAS T*)(off) = v; }
DI u32x2 lds_tr(u32 off) { return __builtin_bit_cast(u32x2, __builtin_amdgcn_ds_read_tr16_b64_v4i16((LAS s16x4*)(off))); }
DI u32x4 lds_tr2(u32 o0, u32 o1) { u32x2 a = lds_tr(o0), b = lds_tr(o1); return u32x4{a.x, a.y, b.x, b.y}; }
DI u32x4 lds_2x64(u32 o) { u32x2 a = *(const volatile LAS u32x2*)(o), b = *(const volatile LAS u32x2*)(o + 8); return u32x4{a.x, a.y, b.x, b.y}; }
DI f32x4 mfma(u32x4 a, u32x4 b, f32x4 c) {
  return __builtin_amdgcn_mfma_f32_16x16x32_bf16(__builtin_bit_cast(bf16x8, a), __builtin_bit_cast(bf16x8, b), c, 0, 0, 0);
}
template <int C> DI float dpp_add(float v) {
  return v + __builtin_bit_cast(float, __builtin_amdgcn_update_dpp(0, __builtin_bit_cast(int, v), C, 0xf, 0xf, true));
}
DI float wave_sum(float v) {
  v = dpp_add<0xB1>(v); v = dpp_add<0x4E>(v); v = dpp_add<0x141>(v); v = dpp_add<0x140>(v);
  int iv = __builtin_bit_cast(int, v);
  return (__builtin_bit_cast(float, __builtin_amdgcn_readlane(iv, 0)) + __builtin_bit_cast(float, __builtin_amdgcn_readlane(iv, 16))) +
         (__builtin_bit_cast(float, __builtin_amdgcn_readlane(iv, 32)) + __builtin_bit_cast(float, __builtin_amdgcn_readlane(iv, 48)));
}
constexpr int SPIN_CAP = 1 << 16;
DI void poll_flags(const char* ws, u32 off, u64 tag) {
  const GAS u64* p = (const GAS u64*)(ws + off);
#pragma unroll 1
  for (int it = 0; it < SPIN_CAP; ++it) {
    u64 v = __hip_atomic_load(p, __ATOMIC_RELAXED, __HIP_MEMORY_SCOPE_AGENT);
    if (__all(v == tag)) break;
    __builtin_amdgcn_s_sleep(1);
  }
  CFENCE();
}
DI void set_flag(char* ws, u32 off, u64 tag) { __hip_atomic_store((GAS u64*)(ws + off), tag, __ATOMIC_RELAXED, __HIP_MEMORY_SCOPE_AGENT); }
DI void arrive_flag(char* ws, u32 cnt, u32 off, u64 tag, int l) {
  if (l == 0) {
    u32 old = __hip_atomic_fetch_add((LAS u32*)cnt, 1u, __ATOMIC_RELAXED, __HIP_MEMORY_SCOPE_WORKGROUP);
    if (old == 7) set_flag(ws, off, tag);
  }
  CFENCE();
}
DI float gelu(float x) {
  const float c1 = -2.f * 0.7978845608028654f * 1.4426950408889634f, c2 = c1 * 0.044715f;
  float e = __builtin_amdgcn_exp2f(x * (c1 + c2 * (x * x)));
  return x * __builtin_amdgcn_rcpf(1.f + e);
}

__global__ __launch_bounds__(512, 2) void fused_kernel(const u16* __restrict__ q, const u16* __restrict__ kk, const u16* __restrict__ vv,
                                                       const u16* __restrict__ sk, const u16* __restrict__ ssk, const u16* __restrict__ sD,
                                                       const u16* __restrict__ w1, const u16* __restrict__ b1, const u16* __restrict__ w2,
                                                       const u16* __restrict__ b2, u16* __restrict__ out, char* __restrict__ ws) {
  __shared__ __attribute__((aligned(16))) char sm[163840];
  asm volatile("" ::"v"(sm) : "memory");
  const int tid = threadIdx.x, l = tid & 63;
  const int w = __builtin_amdgcn_readfirstlane(tid >> 6);
  const int b = blockIdx.x, xcd = b & 7, r = b >> 3;
  const bool mainwg = r < 28;
  const int rp = (xcd & 3) * 4 + (r & 3), cc = (xcd >> 2) * 7 + (r >> 2);
  const int rt = (xcd & 3) * 8 + (r & 7), ct = (xcd >> 2) * 4 + (r >> 3);
  const int kc0 = ((r >> 2) + 2 * (r & 3)) & 7;
  const int fw = w & 3, th = w >> 2;
  const int n16 = l & 15, g4 = l >> 4;
  STAMP_INIT
  STAMP(0);
  const u32x4 srd_q = mk_srd(q, S_ * D_ * 2), srd_w1 = mk_srd(w1, D_ * F_ * 2);
  const __amdgpu_buffer_rsrc_t rs_ws = __builtin_amdgcn_make_buffer_rsrc(ws, 0, WS_BYTES, 0x00020000);

  u32x2 ev; u32x4 x0;
  const int srow = (xcd & 3) * 512 + ((xcd >> 2) * 32 + r) * 8 + w;
  asm volatile("global_load_dwordx2 %0, %1, %2 sc1" : "=v"(ev) : "v"(0u), "s"(ws) : "memory");
  asm volatile("buffer_load_dwordx4 %0, %1, %2, %3 offen" : "=v"(x0) : "v"(l * 16), "s"(srd_q), "s"(srow * 1024) : "memory");

  const u32 vx = (l >> 3) * 1024 + (((l & 7) ^ (l >> 3)) << 4);
  const u32 vw = g4 * (F_ * 2) + ((n16 ^ ((g4 | ((w & 1) << 2)) << 1)) << 4);
  const u32 sx0 = rp * 131072 + w * 16384, sw0 = w * 8 * (F_ * 2) + cc * 256;
  auto stage_dma = [&](int s, int i) {
    const int kc = (kc0 + s) & 7;
    const u32 slot = (s & 3) * SLOT + w * 2048;
    if (i < 2) dma16(srd_q, vx, sx0 + kc * 128 + i * 8192, slot + i * 1024);
    else dma16(srd_w1, vw, sw0 + (kc * 64 + (i - 2) * 4) * (F_ * 2), slot + 16384 + (i - 2) * 1024);
  };
  auto p_dma = [&](int i) {
    const u32 vr = (l >> 3) * 1024 + (l & 7) * 16;
    if (i == 0) dma16(mk_srd(kk, S_ * D_ * 2), vr, (rt * 64 + w * 8) * 1024 + ct * 128, L_PK + 1024 + w * 1024);
    if (i == 1) dma16(mk_srd(vv, S_ * D_ * 2), vr, (rt * 64 + w * 8) * 1024 + ct * 128, L_PV + w * 1024);
    if (i == 2) dma16(mk_srd(ssk, S_ * D_ * 2), l * 16, b * 8192 + w * 1024, L_RAW + (w >> 2) * HCS + 32 + (w & 3) * 1024);
    if (i == 3) {
      const u32x4 sr = w == 0 ? mk_srd(kk, rt ? S_ * D_ * 2 : 0) : w == 1 ? mk_srd(sk, D_ * 8) : mk_srd(b1, F_ * 2);
      dma16m(sr, w == 0 ? vr : l * 16, w == 0 ? (rt ? (rt * 64 - 8) * 1024 + ct * 128 : 0) : w == 1 ? ct * 512 : cc * 256,
             w == 0 ? L_PK : w == 1 ? L_SK : L_B1, w == 0 ? ~0ull : w == 1 ? 0xffffffffull : 0xffffull);
    }
  };
  auto kv_tile = [&]() {
    const int cpl = l & 31, hh = l >> 5, s0 = w * 8 + hh * 4, tgp = w * 2 + hh;
    u32 kr[7], vr4[4];
#pragma unroll
    for (int i = 0; i < 7; ++i) kr[i] = lds_ld<u32>(L_PK + (s0 + 5 + i) * 128 + cpl * 4);
#pragma unroll
    for (int i = 0; i < 4; ++i) vr4[i] = lds_ld<u32>(L_PV + (s0 + i) * 128 + cpl * 4);
    const u32x4 sv = lds_ld<u32x4>(L_SK + cpl * 16);
    float u0[4], u1[4];
#pragma unroll
    for (int i = 0; i < 4; ++i) {
      float a0 = bflo(kr[i]) * bfhi(sv.y) + bflo(kr[i + 1]) * bflo(sv.y) + bflo(kr[i + 2]) * bfhi(sv.x) + bflo(kr[i + 3]) * bflo(sv.x);
      float a1 = bfhi(kr[i]) * bfhi(sv.w) + bfhi(kr[i + 1]) * bflo(sv.w) + bfhi(kr[i + 2]) * bfhi(sv.z) + bfhi(kr[i + 3]) * bflo(sv.z);
      const u32 p = pk2(a0, a1);
      u0[i] = bflo(p) * bflo(vr4[i]); u1[i] = bfhi(p) * bfhi(vr4[i]);
    }
    lds_st<u32x4>(L_PV + w * 1024 + (hh * 32 + (cpl ^ tgp)) * 16, u32x4{pk2(u0[3], u0[2]), pk2(u0[1], u0[0]), pk2(u1[3], u1[2]), pk2(u1[1], u1[0])});
  };
  auto u_store = [&]() {
    const int cpl = tid >> 4, tgp = tid & 15;
    const u32x4 uv = lds_ld<u32x4>(L_PV + (tgp >> 1) * 1024 + ((tgp & 1) * 32 + (cpl ^ tgp)) * 16);
    __builtin_amdgcn_raw_buffer_store_b128(uv, rs_ws, (cpl * 512 + tgp) * 16, OFF_U + (ct * 32 * 512 + rt * 16) * 16, 16);
    CFENCE();
  };
  auto h_copies = [&]() {
#pragma unroll
    for (int pass = 0; pass < 2; ++pass) if (pass == 0 || tid < 4) {
      const int ch = pass ? (tid >> 1) : (tid >> 8), g = pass ? (tid & 1) : (tid & 255) + 2;
      const u32x4 a = lds_ld<u32x4>(L_RAW + ch * HCS + g * 16);
      const u32x2 c = lds_ld<u32x2>(L_RAW + ch * HCS + g * 16 + 16);
      const u32 o0 = __builtin_amdgcn_alignbit(a.y, a.x, 16), o1 = __builtin_amdgcn_alignbit(a.z, a.y, 16), o2 = __builtin_amdgcn_alignbit(a.w, a.z, 16),
                o3 = __builtin_amdgcn_alignbit(c.x, a.w, 16), o4 = __builtin_amdgcn_alignbit(c.y, c.x, 16);
      const u32 d = L_HC + ch * 4 * HCS + g * 16;
      lds_st<u32x4>(d, a); lds_st<u32x4>(d + HCS, u32x4{o0, o1, o2, o3});
      lds_st<u32x4>(d + 2 * HCS, u32x4{a.y, a.z, a.w, c.x}); lds_st<u32x4>(d + 3 * HCS, u32x4{o1, o2, o3, o4});
    }
    if (tid < 60) lds_st<u32x4>(L_UR + (tid / 30) * URS + 4096 + (tid % 30) * 16, u32x4{0, 0, 0, 0});
  };

  const int sp = (r - 28) * 8 + xcd;
  if (mainwg) {
#pragma unroll
    for (int s = 0; s < 3; ++s)
#pragma unroll
      for (int i = 0; i < 4; ++i) stage_dma(s, i);
  } else {
    const u32x4 srd_w2 = mk_srd(w2, D_ * F_ * 2);
#pragma unroll
    for (int i = 0; i < 7; ++i) dma16(srd_w2, g4 * 1024 + n16 * 16, (((sp >> 2) * 7 + i) * 32 + w * 4) * 1024 + (sp & 3) * 256, 65536 + i * 8192 + w * 1024);
#pragma unroll
    for (int i = 0; i < 4; ++i) p_dma(i);
    p_dma(3);
  }
  if (tid < 4) lds_st<u32x4>(L_RAW + (tid >> 1) * HCS + (tid & 1) * 16, u32x4{0, 0, 0, 0});
  if (tid == 4) lds_st<u32x4>(L_CNT, u32x4{0, 0, 0, 0});
  asm volatile("s_waitcnt vmcnt(12)" : "+v"(ev), "+v"(x0)::"memory");
  const u64 tag = (((u64)__builtin_amdgcn_readfirstlane(ev.y) << 32) | (u32)__builtin_amdgcn_readfirstlane(ev.x)) + 1;
  {
    float x[8] = {bflo(x0.x), bfhi(x0.x), bflo(x0.y), bfhi(x0.y), bflo(x0.z), bfhi(x0.z), bflo(x0.w), bfhi(x0.w)};
    float s = ((x[0] + x[1]) + (x[2] + x[3])) + ((x[4] + x[5]) + (x[6] + x[7]));
    const float mean = wave_sum(s) * (1.f / D_);
    float qq = 0;
#pragma unroll
    for (int j = 0; j < 8; ++j) { float d = x[j] - mean; qq += d * d; }
    const float var = wave_sum(qq) * (1.f / D_);
    const float m1 = rb(mean), sd1 = rb(__builtin_amdgcn_sqrtf(rb(rb(var) + 1e-5f)));
    const float i1 = __builtin_amdgcn_rcpf(sd1);
    const float m2 = (mean - m1) * i1, sd2 = __builtin_amdgcn_sqrtf(rb(rb(var * i1 * i1) + 1e-5f));
    if (l == 0) __builtin_amdgcn_raw_buffer_store_b128(u32x4{pk2(m1, sd1), (u32)tag, pk2(m2, sd2), (u32)(tag >> 32)}, rs_ws, OFF_ST + srow * 16, 0, 16);
    CFENCE();
  }
  STAMP(1);

  f32x4 acc[2][4], cs[2];
#pragma unroll
  for (int i = 0; i < 8; ++i) acc[i >> 2][i & 3] = f32x4{0, 0, 0, 0};
  cs[0] = f32x4{0, 0, 0, 0}; cs[1] = f32x4{0, 0, 0, 0};
  if (mainwg) {
    const u32 xa = (th * 64 + n16) * 128 + ((g4 ^ (n16 & 7)) << 4);
    const u32 wa = 16384 + (8 * g4 + (n16 >> 2)) * 256 + (((2 * fw) ^ ((n16 >> 2) | ((g4 & 1) << 2))) << 5) + (l & 3) * 8;
    u32x4 ones = {0x3f803f80u, 0x3f803f80u, 0x3f803f80u, 0x3f803f80u};
    asm volatile("" : "+v"(ones));
    u32x4 xf[2][4], wf[2][2];
    auto rd = [&](int s, int ks) {
      const u32 slot = (s & 3) * SLOT;
#pragma unroll
      for (int e = 0; e < 2; ++e) { const u32 a = slot + (wa ^ (e * 32)) + ks * 8192; wf[ks][e] = lds_tr2(a, a + 1024); }
#pragma unroll
      for (int tt = 0; tt < 4; ++tt) xf[ks][tt] = lds_ld<u32x4>(slot + (xa ^ (ks * 64)) + tt * 2048);
      SB();
    };
    auto issue = [&](int hs, int i) {
      if (hs == 0) stage_dma(3, i);
      else if (hs == 1) p_dma(i);
      else if (hs == 2) stage_dma(4, i);
      else if (hs >= 3 && hs <= 8) { if (i < 2) stage_dma(5 + (hs - 3) / 2, ((hs - 3) & 1) * 2 + i); }
    };
    auto mm = [&](int s, int ks) {
#pragma unroll
      for (int tt = 0; tt < 4; ++tt) {
        acc[0][tt] = mfma(wf[ks][0], xf[ks][tt], acc[0][tt]);
        acc[1][tt] = mfma(wf[ks][1], xf[ks][tt], acc[1][tt]);
        SB(); issue(2 * s + ks, tt); SB();
      }
      if (th == ks) { cs[0] = mfma(wf[ks][0], ones, cs[0]); cs[1] = mfma(wf[ks][1], ones, cs[1]); }
      SB();
    };
    waitv<9>();
    bar();
    rd(0, 0);
#pragma unroll
    for (int s = 0; s < 8; ++s) {
      STAMP2(16 + s * 4);
      rd(s, 1);
      mm(s, 0);
      STAMP2(17 + s * 4);
      if (s == 0) waitv<9>(); else if (s == 1) waitv<13>(); else if (s == 2) waitv<12>(); else if (s == 3) waitv<8>();
      else if (s == 4) waitv<8>(); else if (s == 5) waitv<4>(); else if (s == 6) waitv<0>();
      STAMP2(18 + s * 4);
      if (s < 7) bar();
      STAMP2(19 + s * 4);
      if (s == 6) arrive_flag(ws, L_CNT, OFF_FU + (ct * 32 + rt) * 8, tag, l);
      if (s < 7) rd(s + 1, 0);
      mm(s, 1);
      if (s == 3) kv_tile();
      if (s == 4) u_store();
      if (s == 5) h_copies();
    }
    if (n16 == 0) { lds_st<f32x4>(L_CS + th * 512 + fw * 128 + g4 * 16, cs[0]); lds_st<f32x4>(L_CS + th * 512 + fw * 128 + 64 + g4 * 16, cs[1]); }
  } else {
    waitv<6>();
    bar();
#pragma unroll
    for (int i = 0; i < 7; ++i) {
      const u32 a = 65536 + i * 8192 + (4 * g4 + (n16 >> 2)) * 256 + w * 32 + (l & 3) * 8;
      __builtin_amdgcn_raw_buffer_store_b128(lds_tr2(a, a + 4096), rs_ws, l * 16,
          OFF_W2T + ((((sp & 3) * 2 + (w >> 2)) * 56 + (sp >> 2) * 7 + i) * 4 + (w & 3)) * 1024, 16);
    }
    waitv<0>();
    arrive_flag(ws, L_CNT + 4, OFF_FW + sp * 8, tag, l);
    bar();
    kv_tile();
    h_copies();
    bar();
    u_store();
    waitv<0>();
    arrive_flag(ws, L_CNT, OFF_FU + (ct * 32 + rt) * 8, tag, l);
  }
  STAMP(2);
  const int rot = ((r & 7) + 2 * (r >> 3)) % 7;
  u32x4 st[4];
  {
    const GAS u64* pw = (const GAS u64*)(ws + OFF_FW + (w * 4 + (ct >> 1)) * 8);
#pragma unroll 1
    for (int it = 0; it < SPIN_CAP; ++it) {
      const u64 fv = __hip_atomic_load(pw, __ATOMIC_RELAXED, __HIP_MEMORY_SCOPE_AGENT);
      bool ok = fv == tag;
#pragma unroll
      for (int tt = 0; tt < 4; ++tt) {
        st[tt] = __builtin_amdgcn_raw_buffer_load_b128(rs_ws, n16 * 16 + tt * 256, OFF_ST + (rp * 128 + th * 64) * 16, 16);
        ok = ok & (st[tt].y == (u32)tag) & (st[tt].w == (u32)(tag >> 32));
      }
      if (__all(ok)) break;
      __builtin_amdgcn_s_sleep(1);
      CFENCE();
    }
    CFENCE();
  }
  u32x4 w2f[28];
  auto load_w2 = [&](int j) {
    int kp = (j >> 2) + rot; kp = kp >= 7 ? kp - 7 : kp;
    w2f[j] = __builtin_amdgcn_raw_buffer_load_b128(rs_ws, l * 16 + (j & 3) * 1024, OFF_W2T + (ct * 56 + 7 * w + kp) * 4096, 16);
    SB();
  };
  SB(); load_w2(0); load_w2(1);
  bar();
  STAMP(3);
  {
    f32x4 c[2]; float bv[2][4];
#pragma unroll
    for (int e = 0; e < 2; ++e) {
      const u32 co = L_CS + fw * 128 + e * 64 + g4 * 16;
      c[e] = lds_ld<f32x4>(co) + lds_ld<f32x4>(co + 512);
      const u32x2 bb = lds_ld<u32x2>(L_B1 + fw * 64 + e * 32 + g4 * 8);
      bv[e][0] = bflo(bb.x); bv[e][1] = bfhi(bb.x); bv[e][2] = bflo(bb.y); bv[e][3] = bfhi(bb.y);
    }
#pragma unroll
    for (int tt = 0; tt < 4; ++tt) {
      u32 hp[4];
      const float i2 = __builtin_amdgcn_rcpf(bfhi(st[tt].z));
      const float ra = __builtin_amdgcn_rcpf(bfhi(st[tt].x)) * i2, rbb = -bflo(st[tt].x) * ra - bflo(st[tt].z) * i2;
#pragma unroll
      for (int e = 0; e < 2; ++e) {
        float h[4];
#pragma unroll
        for (int j = 0; j < 4; ++j) h[j] = gelu(acc[e][tt][j] * ra + (c[e][j] * rbb + bv[e][j]));
        hp[2 * e] = pk2(h[0], h[1]); hp[2 * e + 1] = pk2(h[2], h[3]);
        SB(); load_w2(2 + tt * 2 + e);
      }
      if (mainwg) __builtin_amdgcn_raw_buffer_store_b128(u32x4{hp[0], hp[1], hp[2], hp[3]}, rs_ws, l * 16,
          OFF_H + ((rp * 8 + th * 4 + tt) * 56 + cc * 4 + fw) * 1024, 16);
      CFENCE(); SB();
    }
  }
  STAMP(4);
  {
    const int rt2 = 4 * w + (l & 3);
    poll_flags(ws, OFF_FU + ((b >> 5) * 32 + rt2) * 8, tag);
  }
  waitv<0>();
  if (mainwg) arrive_flag(ws, L_CNT + 4, OFF_FH + (rp * 16 + cc) * 8, tag, l);
  const u32x4 uu = __builtin_amdgcn_raw_buffer_load_b128(rs_ws, tid * 16, OFF_U + b * 8192, 16);
  SB(); load_w2(10); load_w2(11);
  lds_st<u32x2>(L_UR + (2044 - 4 * tid) * 2, u32x2{uu.x, uu.y});
  lds_st<u32x2>(L_UR + URS + (2044 - 4 * tid) * 2, u32x2{uu.z, uu.w});
  bar();
  STAMP(5);

  {
    const int ch = w & 1, kh = (w >> 1) & 1;
    f32x4 ca[4] = {f32x4{0, 0, 0, 0}, f32x4{0, 0, 0, 0}, f32x4{0, 0, 0, 0}, f32x4{0, 0, 0, 0}};
    const u32 ab = L_HC + (ch * 4 + ((n16 + 1) & 3)) * HCS + (((n16 + 1) & ~3) + 8 * g4) * 2 + kh * 256;
    const u32 bb = L_UR + ch * URS + (2032 - 1792 - 16 * n16 + 8 * g4) * 2 + kh * 256 + (1 - th) * 512;
    u32x4 A1[2][4], B1[2][4], A2[4], B2[4];
    auto rd1 = [&](int k0) {
#pragma unroll
      for (int i = 0; i < 4; ++i) { A1[k0 & 1][i] = lds_2x64(ab + k0 * 64 + i * 512); B1[k0 & 1][(i + 1) & 3] = lds_ld<u32x4>(bb + k0 * 64 + (7 - ((i + 1) & 3)) * 512); }
      SB();
    };
    rd1(0);
#pragma unroll
    for (int k0 = 0; k0 < 4; ++k0) {
      u32x4 (&A)[4] = A1[k0 & 1]; u32x4 (&B)[4] = B1[k0 & 1];
#pragma unroll
      for (int i = 0; i < 4; ++i) { B2[i] = lds_ld<u32x4>(bb + k0 * 64 + (3 - i) * 512); A2[i] = lds_2x64(ab + k0 * 64 + (4 + i) * 512); }
      SB();
      ca[0] = mfma(A[0], B[1], ca[0]); ca[1] = mfma(A[1], B[2], ca[1]); ca[2] = mfma(A[2], B[3], ca[2]);
      SB(); load_w2(12 + k0 * 4);
      ca[1] = mfma(A[0], B[3], ca[1]); ca[2] = mfma(A[3], B[2], ca[2]); ca[1] = mfma(A[2], B[1], ca[1]);
      SB(); load_w2(13 + k0 * 4);
      if (k0 < 3) rd1(k0 + 1);
      ca[2] = mfma(A[1], B2[0], ca[2]); ca[3] = mfma(A[3], B2[0], ca[3]); ca[2] = mfma(A2[0], B[1], ca[2]); ca[3] = mfma(A2[0], B[3], ca[3]);
      ca[2] = mfma(A[0], B2[1], ca[2]);
      SB(); load_w2(14 + k0 * 4);
      ca[3] = mfma(A[2], B2[1], ca[3]); ca[3] = mfma(A2[1], B[2], ca[3]); ca[3] = mfma(A[1], B2[2], ca[3]); ca[3] = mfma(A2[2], B[1], ca[3]);
      ca[3] = mfma(A[0], B2[3], ca[3]);
      SB(); load_w2(15 + k0 * 4);
      if (th) { ca[0] = mfma(A[1], B[0], ca[0]); ca[1] = mfma(A[3], B[0], ca[1]); ca[2] = mfma(A2[1], B[0], ca[2]); ca[3] = mfma(A2[3], B[0], ca[3]); }
      SB();
    }
    const u32 pa = L_CP + (kh * 2 + ch) * 8192 + th * 1024 + (4 * n16 + (g4 ^ ((n16 >> 1) & 3))) * 16;
#pragma unroll
    for (int a = 0; a < 4; ++a) lds_st<f32x4>(pa + a * 2048, ca[a]);
  }
  STAMP(6);
  { const int c0 = (7 * w) >> 2, c1 = (7 * w + 6) >> 2; poll_flags(ws, OFF_FH + ((rt >> 1) * 16 + min(c0 + (l & 3), c1)) * 8, tag); }
  STAMP(7);
  u32x4 hf[4][7];
  auto load_h = [&](int t, int i) {
    int kp = i + rot; kp = kp >= 7 ? kp - 7 : kp;
    hf[t][i] = __builtin_amdgcn_raw_buffer_load_b128(rs_ws, l * 16, OFF_H + ((rt * 4 + ((t + (r >> 3)) & 3)) * 56 + 7 * w + kp) * 1024, 16);
    SB();
  };
#pragma unroll
  for (int i = 0; i < 7; ++i) load_h(0, i);
  bar();
  {
    const u32 pa = L_CP + ((tid & ~3) | ((tid & 3) ^ ((tid >> 3) & 3))) * 16;
    const f32x4 y0 = lds_ld<f32x4>(pa) + lds_ld<f32x4>(pa + 16384), y1 = lds_ld<f32x4>(pa + 8192) + lds_ld<f32x4>(pa + 24576);
    const u32 dd = *(const u32*)(sD + 2 * b);
    const float d0 = bflo(dd), d1 = bfhi(dd);
    const float a0[4] = {bfhi(uu.y), bflo(uu.y), bfhi(uu.x), bflo(uu.x)}, a1[4] = {bfhi(uu.w), bflo(uu.w), bfhi(uu.z), bflo(uu.z)};
    u32 o[4];
#pragma unroll
    for (int j = 0; j < 4; ++j) { const u32 p = pk2(y0[j], y1[j]), t = pk2(a0[j] * d0, a1[j] * d1); o[j] = pk2(bflo(p) + bflo(t), bfhi(p) + bfhi(t)); }
    __builtin_amdgcn_raw_buffer_store_b128(u32x4{o[0], o[1], o[2], o[3]}, rs_ws, (tid >> 4) * 65536 + (tid & 15) * 16, OFF_Y + b * 256, 16);
  }
  CFENCE(); SB();
#pragma unroll
  for (int i = 0; i < 7; ++i) load_h(1, i);
  bar();
  STAMP(8);
  const int tok = tid >> 3, cg = tid & 7;
  u32x4 qv, b2v, stf; u32 yv[4];
#pragma unroll
  for (int t = 0; t < 4; ++t) {
    f32x4 a2[4] = {f32x4{0, 0, 0, 0}, f32x4{0, 0, 0, 0}, f32x4{0, 0, 0, 0}, f32x4{0, 0, 0, 0}};
#pragma unroll
    for (int i = 0; i < 7; ++i) {
#pragma unroll
      for (int nt = 0; nt < 4; ++nt) a2[nt] = mfma(w2f[i * 4 + nt], hf[t][i], a2[nt]);
      SB();
      if (t < 2) load_h(t + 2, i);
    }
#pragma unroll
    for (int nt = 0; nt < 4; ++nt) lds_st<f32x4>(w * 16384 + (((t + (r >> 3)) & 3) * 16 + n16) * 256 + (((4 * nt + g4) ^ (n16 & 7)) << 4), a2[nt]);
    SB();
    if (t == 0) { waitv<14>(); arrive_flag(ws, L_CNT + 8, OFF_FY + b * 8, tag, l); }
    if (t == 1) {
      qv = *(const u32x4*)(q + (rt * 64 + tok) * D_ + ct * 64 + cg * 8);
      b2v = *(const u32x4*)(b2 + ct * 64 + cg * 8);
      stf = __builtin_amdgcn_raw_buffer_load_b128(rs_ws, tok * 16, OFF_ST + rt * 1024, 16);
      SB();
    }
    if (t == 2) {
      poll_flags(ws, OFF_FY + (ct * 32 + (l & 31)) * 8, tag);
#pragma unroll
      for (int i = 0; i < 4; ++i) yv[i] = __builtin_amdgcn_raw_buffer_load_b32(rs_ws, (cg * 4 + i) * 256 + tok * 4, OFF_Y + rt * 65536 + ct * 8192, 16);
      SB();
    }
    STAMP(9 + t);
  }
  bar();
  STAMP(13);
  {
    f32x4 s0 = {0, 0, 0, 0}, s1 = {0, 0, 0, 0};
    const u32 ra0 = tok * 256 + (((2 * cg) ^ (tok & 7)) << 4);
#pragma unroll
    for (int i = 0; i < 8; ++i) { s0 += lds_ld<f32x4>(i * 16384 + ra0); s1 += lds_ld<f32x4>(i * 16384 + (ra0 ^ 16)); }
    const float m1 = bflo(stf.x), i1 = __builtin_amdgcn_rcpf(bfhi(stf.x));
    u32 o[4];
#pragma unroll
    for (int i = 0; i < 4; ++i) {
      float res[2];
#pragma unroll
      for (int e = 0; e < 2; ++e) {
        const int j = 2 * i + e;
        const float x = e ? bfhi(qv[i]) : bflo(qv[i]), y = e ? bfhi(yv[i]) : bflo(yv[i]), bb = e ? bfhi(b2v[i]) : bflo(b2v[i]);
        const float z = (j < 4 ? s0[j & 3] : s1[j & 3]) + bb;
        const float g = z * __builtin_amdgcn_rcpf(1.f + __builtin_amdgcn_exp2f(z * -1.4426950408889634f));
        res[e] = x + ((x - m1) * i1) * y * g;
      }
      o[i] = pk2(res[0], res[1]);
    }
    *(u32x4*)(out + (rt * 64 + tok) * D_ + ct * 64 + cg * 8) = u32x4{o[0], o[1], o[2], o[3]};
  }
  if (b == 0 && w == 0) {
#pragma unroll
    for (int i = 0; i < 4; ++i) poll_flags(ws, OFF_FU + (i * 64 + l) * 8, tag);
    if (l == 0) set_flag(ws, OFF_E, tag);
  }
  STAMP(14);
  STAMP_END;
}

extern "C" void kernel_launch(void* const* d_in, const int* in_sizes, int n_in, void* d_out, int out_size, void* d_ws, size_t ws_size,
                              hipStream_t stream) {
  fused_kernel<<<256, 512, 0, stream>>>((const u16*)d_in[0], (const u16*)d_in[1], (const u16*)d_in[2], (const u16*)d_in[3], (const u16*)d_in[4],
                                        (const u16*)d_in[5], (const u16*)d_in[7], (const u16*)d_in[8], (const u16*)d_in[9], (const u16*)d_in[10],
                                        (u16*)d_out, (char*)d_ws);
}
```

```cpp
#include <hip/hip_runtime.h>
typedef unsigned short u16;
typedef unsigned u32;
typedef unsigned long long u64;
typedef __bf16 bf16x8 __attribute__((ext_vector_type(8)));
typedef __bf16 bf16x2 __attribute__((ext_vector_type(2)));
typedef short s16x4 __attribute__((ext_vector_type(4)));
typedef float f32x4 __attribute__((ext_vector_type(4)));
typedef float f32x2 __attribute__((ext_vector_type(2)));
typedef unsigned u32x4 __attribute__((ext_vector_type(4)));
typedef unsigned u32x2 __attribute__((ext_vector_type(2)));
#define LAS __attribute__((address_space(3)))
#define GAS __attribute__((address_space(1)))
#define DI __device__ __forceinline__
#pragma clang diagnostic ignored "-Wint-to-pointer-cast"

constexpr int S_ = 2048, D_ = 512, F_ = 1792;
constexpr u32 OFF_E = 0, OFF_CS = 16384, OFF_ST = 32768, FS = 256, OFF_FU = 65536, OFF_FW = OFF_FU + 256 * FS, OFF_FH = OFF_FW + 256 * FS, OFF_FY = OFF_FH + 256 * FS;
constexpr u32 OFF_U = 1u << 20, OFF_Y = 3u << 20, OFF_W2T = 5u << 20, OFF_H = 7u << 20, WS_BYTES = 14u << 20;
constexpr u32 SLOT = 32768, L_PK = 131072, L_PV = 140288, L_RAW = 148480, L_SK = 156800, L_B1 = 157312, L_CNT = 157568, L_CSX = 157696,
              L_HC0 = 131072, L_HC1 = 0, HCS = 4160, L_UR = 16640, URS = 4576, L_CP = 65536, L_YS = 131072, L_QS = 148480;
constexpr int NST = 3;
#define PSLEEP 4
#define NWA 4
#define NT_G 0
#define NT_P 1
#define KROT 0
#define WS_TAIL 0
#define WROT 0
#define UDR 5
#define HLA 9

#define PRO 16
#define CNTS {0, 2, 2, 2, 2, 2, 2, 2, 2}
constexpr int kCnt[16] = CNTS;
constexpr int issued_before(int hs) { int n = PRO; for (int i = 0; i < hs; ++i) n += kCnt[i]; return n; }
constexpr bool sched_ok() { for (int hs = 0; hs < 16; ++hs) { if (issued_before(hs + 1) > 4 * ((hs + 1) / 2 + 4)) return false; if (!(hs & 1) && hs < 14 && issued_before(hs + 1) < 4 * (hs / 2 + 2)) return false; } return issued_before(16) == 32 && issued_before(11) == 32; }
static_assert(sched_ok(), "transfer schedule");

#define STAMP(i)
#define STAMP_INIT
#define STAMP_END
#define STAMP2(i)

template <int V> struct IC { static constexpr int v = V; };
template <int I, int N, class F> DI void sfor(F&& f) { if constexpr (I < N) { f(IC<I>{}); sfor<I + 1, N>(f); } }
DI u32x4 mk_srd(const void* p, u32 bytes) { u64 a = (u64)p; return u32x4{(u32)a, (u32)(a >> 32) & 0xffffu, bytes, 0x00020000u}; }
template <int NT> DI void dma16(u32x4 srd, u32 voff, u32 soff, u32 lds) {
  if (NT) asm volatile("s_mov_b32 m0, %3\n\ts_nop 0\n\tbuffer_load_dwordx4 %0, %1, %2 offen nt lds" ::"v"(voff), "s"(srd), "s"(soff), "s"(lds) : "memory");
  else asm volatile("s_mov_b32 m0, %3\n\ts_nop 0\n\tbuffer_load_dwordx4 %0, %1, %2 offen lds" ::"v"(voff), "s"(srd), "s"(soff), "s"(lds) : "memory");
}
template <int NT, int I> DI void dma16g(u32x4 srd, u32 voff, u32 soff, u32 lds) {
  if (I == 0) dma16<NT>(srd, voff, soff, lds);
  else if (NT) asm volatile("buffer_load_dwordx4 %0, %1, %2 offen offset:%3 nt lds" ::"v"(voff), "s"(srd), "s"(soff), "i"(I * 1024) : "memory");
  else asm volatile("buffer_load_dwordx4 %0, %1, %2 offen offset:%3 lds" ::"v"(voff), "s"(srd), "s"(soff), "i"(I * 1024) : "memory");
}
DI void dma16m(u32x4 srd, u32 voff, u32 soff, u32 lds, u64 mask) {
  asm volatile("s_mov_b32 m0, %3\n\ts_mov_b64 exec, %4\n\tbuffer_load_dwordx4 %0, %1, %2 offen lds\n\ts_mov_b64 exec, -1"
               ::"v"(voff), "s"(srd), "s"(soff), "s"(lds), "s"(mask) : "memory");
}
template <int N> DI void waitv() { asm volatile("s_waitcnt vmcnt(%0)" ::"i"(N) : "memory"); }
#define WAITL0() asm volatile("s_waitcnt lgkmcnt(0)" ::: "memory")
#define CFENCE() asm volatile("" ::: "memory")
#define SB() __builtin_amdgcn_sched_barrier(0)
DI void bar() { SB(); CFENCE(); __builtin_amdgcn_s_waitcnt(0xC07F); __builtin_amdgcn_s_barrier(); CFENCE(); SB(); }
DI float bflo(u32 x) { return __uint_as_float(x << 16); }
DI float bfhi(u32 x) { return __uint_as_float(x & 0xffff0000u); }
DI u32 pk2(float a, float b) { f32x2 p = {a, b}; return __builtin_bit_cast(u32, __builtin_convertvector(p, bf16x2)); }
DI float rb(float a) { return bflo(pk2(a, a)); }
DI f32x2 bf2(u32 x) { return f32x2{bflo(x), bfhi(x)}; }
DI f32x2 rb2(f32x2 v) { return bf2(pk2(v.x, v.y)); }
DI f32x2 sp2(float v) { return f32x2{v, v}; }
DI f32x2 exp2x2(f32x2 v) { return f32x2{__builtin_amdgcn_exp2f(v.x), __builtin_amdgcn_exp2f(v.y)}; }
DI f32x2 rcpx2(f32x2 v) { return f32x2{__builtin_amdgcn_rcpf(v.x), __builtin_amdgcn_rcpf(v.y)}; }
DI f32x2 fma2(f32x2 a, f32x2 b, f32x2 c) { return __builtin_elementwise_fma(a, b, c); }
template <class T> DI T lds_ld(u32 off) { return *(const LAS T*)(off); }
template <class T> DI void lds_st(u32 off, T v) { *(LAS T*)(off) = v; }
DI u32x2 lds_tr(u32 off) { return __builtin_bit_cast(u32x2, __builtin_amdgcn_ds_read_tr16_b64_v4i16((LAS s16x4*)(off))); }
DI u32x4 lds_tr2(u32 o0, u32 o1) { u32x2 a = lds_tr(o0), b = lds_tr(o1); return u32x4{a.x, a.y, b.x, b.y}; }
DI u32x4 lds_2x64(u32 o) { u32x2 a = *(const volatile LAS u32x2*)(o), b = *(const volatile LAS u32x2*)(o + 8); return u32x4{a.x, a.y, b.x, b.y}; }
DI f32x4 mfma(u32x4 a, u32x4 b, f32x4 c) {
  return __builtin_amdgcn_mfma_f32_16x16x32_bf16(__builtin_bit_cast(bf16x8, a), __builtin_bit_cast(bf16x8, b), c, 0, 0, 0);
}
DI void st128(u32x4 v, __amdgpu_buffer_rsrc_t rs, u32 voff, u32 soff) {
  __builtin_amdgcn_raw_buffer_store_b128(v, rs, voff, soff, 16);
  asm volatile("s_nop 1" ::"v"(v) : "memory");
}
DI u32x4 ld128(__amdgpu_buffer_rsrc_t rs, u32 voff, u32 soff) { return __builtin_amdgcn_raw_buffer_load_b128(rs, voff, soff, 16); }
template <int C> DI float dpp_add(float v) {
  return v + __builtin_bit_cast(float, __builtin_amdgcn_update_dpp(0, __builtin_bit_cast(int, v), C, 0xf, 0xf, true));
}
DI float bperm(int src_lane4, float v) { return __builtin_bit_cast(float, __builtin_amdgcn_ds_bpermute(src_lane4, __builtin_bit_cast(int, v))); }
DI float rl(float v, int i) { return __builtin_bit_cast(float, __builtin_amdgcn_readlane(__builtin_bit_cast(int, v), i)); }
DI float wave_sum(float v) {
  v = dpp_add<0xB1>(v); v = dpp_add<0x4E>(v); v = dpp_add<0x141>(v); v = dpp_add<0x140>(v);
  return (rl(v, 0) + rl(v, 16)) + (rl(v, 32) + rl(v, 48));
}
constexpr int SPIN_CAP = 1 << 16;
DI u64 flag_ld(const char* ws, u32 off) { return __hip_atomic_load((const GAS u64*)(ws + off), __ATOMIC_RELAXED, __HIP_MEMORY_SCOPE_AGENT); }
DI void poll_flags(const char* ws, u32 off, u64 tag) {
#pragma unroll 1
  for (int it = 0; it < SPIN_CAP; ++it) {
    if (__all(flag_ld(ws, off) == tag)) break;
    __builtin_amdgcn_s_sleep(PSLEEP);
  }
  CFENCE();
}
DI void flag_chk(u64 v, const char* ws, u32 off, u64 tag) { if (!__all(v == tag)) poll_flags(ws, off, tag); CFENCE(); }
DI void set_flag(char* ws, u32 off, u64 tag) { __hip_atomic_store((GAS u64*)(ws + off), tag, __ATOMIC_RELAXED, __HIP_MEMORY_SCOPE_AGENT); }
DI void arrive_flag(char* ws, u32 cnt, u32 off, u64 tag, int l) {
  if (l == 0) {
    u32 old;
    asm volatile("ds_add_rtn_u32 %0, %1, %2\n\ts_waitcnt lgkmcnt(0)" : "=&v"(old) : "v"(cnt), "v"(1u) : "memory");
    if (old == 7) set_flag(ws, off, tag);
  }
  CFENCE();
}

__global__ __launch_bounds__(512, 2) void fused_kernel(const u16* __restrict__ q, const u16* __restrict__ kk, const u16* __restrict__ vv,
                                                       const u16* __restrict__ sk, const u16* __restrict__ ssk, const u16* __restrict__ sD,
                                                       const u16* __restrict__ w1, const u16* __restrict__ b1, const u16* __restrict__ w2,
                                                       const u16* __restrict__ b2, u16* __restrict__ out, char* __restrict__ ws) {
  __shared__ __attribute__((aligned(16))) char sm[163840];
  asm volatile("" ::"v"(sm) : "memory");
  const int tid = threadIdx.x, l = tid & 63;
  const int w = __builtin_amdgcn_readfirstlane(tid >> 6);
  const int b = blockIdx.x, xcd = b & 7, r = b >> 3;
  const bool mainwg = r < 28;
  const int rp = (xcd & 3) * 4 + (r & 3), cc = (xcd >> 2) * 7 + (r >> 2);
  const int rt = (xcd & 3) * 8 + (xcd >> 2) * 4 + (r & 3), ct = r >> 2;
  const int kc0 = KROT == 1 ? ((r >> 2) + 2 * (r & 3)) & 7 : KROT == 2 ? (((r >> 2) + r) & 1) * 4 : 0;
  const int fw = w & 3, th = w >> 2;
  const int n16 = l & 15, g4 = l >> 4;
  STAMP_INIT
  STAMP(0);
  const u32x4 srd_q = mk_srd(q, S_ * D_ * 2), srd_w1 = mk_srd(w1, D_ * F_ * 2);
  const u32 vr = (l >> 3) * 1024 + (l & 7) * 16;
  const int sp = (r - 28) * 8 + xcd;
  if (!mainwg) {
    const u32 v1 = (l >> 3) * (F_ * 2) + (((l & 7) ^ ((l >> 3) & 6)) << 4);
#pragma unroll
    for (int i = 0; i < 8; ++i) dma16<0>(srd_w1, v1, sp < 28 ? (w * 64 + i * 8) * (F_ * 2) + sp * 128 : 0x7f000000u, 65536 + w * 8192 + i * 1024);
  }
  dma16<NT_P>(mk_srd(kk, S_ * D_ * 2), vr, (rt * 64 + w * 8) * 1024 + ct * 128, L_PK + 1024 + w * 1024);
  dma16<NT_P>(mk_srd(vv, S_ * D_ * 2), vr, (rt * 64 + w * 8) * 1024 + ct * 128, L_PV + w * 1024);
  u32x2 ev; u32x4 x0;
  const int srow = rt * 64 + ct * 8 + w;
  asm volatile("s_mov_b64 exec, 1\n\tglobal_load_dwordx2 %0, %1, %2 sc1\n\ts_mov_b64 exec, -1" : "=v"(ev) : "v"(0u), "s"(ws) : "memory");
  asm volatile("buffer_load_dwordx4 %0, %1, %2, %3 offen" : "=v"(x0) : "v"(l * 16), "s"(srd_q), "s"(srow * 1024) : "memory");
  {
    const u64 pk_ = (u64)kk, ps_ = (u64)sk, pb_ = (u64)b1;
    dma16m(mk_srd((const void*)(w == 0 ? pk_ : w == 1 ? ps_ : pb_), w == 0 ? (rt ? S_ * D_ * 2 : 0) : w == 1 ? D_ * 8 : F_ * 2), w == 0 ? vr : l * 16,
           w == 0 ? max(rt * 64 - 8, 0) * 1024 + ct * 128 : w == 1 ? ct * 512 : cc * 256, w == 0 ? L_PK : w == 1 ? L_SK : L_B1,
           w == 0 ? ~0ull : w == 1 ? 0xffffffffull : 0xffffull);
  }
  SB();
  STAMP2(40);
  const u32 vx0 = (l >> 3) * 1024 + (((l & 7) ^ (l >> 3)) << 4), vx1 = vx0 + 8192 - 1024;
  const u32 vw0 = (g4 + 1) * (F_ * 2) + ((n16 ^ ((g4 | ((w & 1) << 2)) << 1)) << 4) - 2048, vw1 = vw0 + 4 * (F_ * 2) - 1024;
  const u32 sx0 = rp * 131072 + w * 16384, sw0 = w * 8 * (F_ * 2) + cc * 256;
  const u32x4 srd_w1m = mk_srd((const char*)w1 - F_ * 2, D_ * F_ * 2 + F_ * 2);
  auto stage_dma = [&](int s, int i) __attribute__((always_inline)) {
    const int kc = (kc0 + s) & 7;
    const u32 dst = (s & 3) * SLOT + w * 4096;
    if (i == 0) dma16g<NT_G, 0>(srd_q, vx0, sx0 + kc * 128, dst);
    if (i == 1) dma16g<NT_G, 1>(srd_q, vx1, sx0 + kc * 128, dst);
    if (i == 2) dma16g<NT_G, 2>(srd_w1m, vw0, sw0 + kc * 64 * (F_ * 2), dst);
    if (i == 3) dma16g<NT_G, 3>(srd_w1m, vw1, sw0 + kc * 64 * (F_ * 2), dst);
  };
  const __amdgpu_buffer_rsrc_t rs_ws = __builtin_amdgcn_make_buffer_rsrc(ws, 0, WS_BYTES, 0x00020000);
  const u32x4 srd_w2 = mk_srd(w2, D_ * F_ * 2);
  auto raw_dma = [&]() __attribute__((always_inline)) { dma16<NT_P>(mk_srd(ssk, S_ * D_ * 2), l * 16, b * 8192 + w * 1024, L_RAW + (w >> 2) * HCS + 32 + (w & 3) * 1024); };
  const u32 dd = *(const u32*)(sD + 2 * b);
  if (tid < 3) lds_st<u32x4>(tid < 2 ? L_RAW + tid * HCS + 16 : L_CNT, u32x4{0, 0, 0, 0});

  u64 tag; float c0, sa, sq, mean, var, m1, sd1, i1, m2, sd2; f32x2 sd_[4], s2a, s2b, q2a, q2b;
  auto st_step = [&](int n) __attribute__((always_inline)) {
    if (n == 0) { asm volatile("" : "+v"(ev), "+v"(x0));
      tag = (((u64)__builtin_amdgcn_readfirstlane(ev.y) << 32) | (u32)__builtin_amdgcn_readfirstlane(ev.x)) + 0x9E3779B97F4A7C15ull;
      c0 = bflo(__builtin_amdgcn_readfirstlane(x0.x)); }
    else if (n < 5) sd_[n - 1] = bf2(x0[n - 1]) - sp2(c0);
    else if (n == 5) { s2a = sd_[0] + sd_[1]; s2b = sd_[2] + sd_[3]; }
    else if (n == 6) { q2a = sd_[0] * sd_[0]; q2b = sd_[2] * sd_[2]; }
    else if (n == 7) { q2a = fma2(sd_[1], sd_[1], q2a); q2b = fma2(sd_[3], sd_[3], q2b); }
    else if (n == 8) { s2a += s2b; q2a += q2b; }
    else if (n == 9) { sa = s2a.x + s2a.y; sq = q2a.x + q2a.y; }
    else if (n == 10) { sa = dpp_add<0xB1>(sa); sq = dpp_add<0xB1>(sq); }
    else if (n == 11) { sa = dpp_add<0x4E>(sa); sq = dpp_add<0x4E>(sq); }
    else if (n == 12) { sa = dpp_add<0x141>(sa); sq = dpp_add<0x141>(sq); }
    else if (n == 13) { sa = dpp_add<0x140>(sa); sq = dpp_add<0x140>(sq); }
    else if (n == 14) { sa = dpp_add<0x142>(sa); sq = dpp_add<0x142>(sq); }
    else if (n == 15) { sa = dpp_add<0x143>(sa); sq = dpp_add<0x143>(sq); }
    else if (n == 16) { sa *= 1.f / D_; sq *= 1.f / D_; }
    else if (n == 17) { mean = c0 + sa; var = fmaxf(sq - sa * sa, 0.f); }
    else if (n == 18) { m1 = rb(mean); sd1 = rb(var); }
    else if (n == 19) sd1 = rb(sd1 + 1e-5f);
    else if (n == 20) sd1 = rb(__builtin_amdgcn_sqrtf(sd1));
    else if (n == 21) { i1 = __builtin_amdgcn_rcpf(sd1); m2 = mean - m1; }
    else if (n == 22) { m2 *= i1; sd2 = var * i1 * i1; }
    else if (n == 23) sd2 = rb(sd2);
    else if (n == 24) sd2 = rb(sd2 + 1e-5f);
    else if (n == 25) sd2 = __builtin_amdgcn_sqrtf(sd2);
    else if (n == 26) st128(u32x4{pk2(m1, sd1), (u32)tag, pk2(m2, sd2), (u32)(tag >> 32)}, rs_ws, l == 63 ? 0 : 0x7f000000u, OFF_ST + srow * 16);
    if (n >= 9 && n <= 16) asm volatile("" : "+v"(sa), "+v"(sq));
    if (n >= 18 && n <= 20) asm volatile("" : "+v"(sd1));
    if (n >= 22 && n <= 25) asm volatile("" : "+v"(sd2));
  };
  u32 kr[7], vr4[4], kvo[4]; u32x4 sv; f32x2 kT[4], kK[7], kV[4], ka[4];
  auto kv_step = [&](int n) __attribute__((always_inline)) {
    const int cpl = l & 31, hh = l >> 5;
    if (n == 0) {
      const u32 a = (w * 8 + hh * 4) * 128 + cpl * 4;
#pragma unroll
      for (int i = 0; i < 7; ++i) kr[i] = lds_ld<u32>(L_PK + 5 * 128 + a + i * 128);
#pragma unroll
      for (int i = 0; i < 4; ++i) vr4[i] = lds_ld<u32>(L_PV + a + i * 128);
      sv = lds_ld<u32x4>(L_SK + cpl * 16);
    } else if (n < 5) { const int j = n - 1; kT[j] = (j & 1) ? f32x2{bfhi(sv[j >> 1]), bfhi(sv[2 + (j >> 1)])} : f32x2{bflo(sv[j >> 1]), bflo(sv[2 + (j >> 1)])}; }
    else if (n < 12) kK[n - 5] = bf2(kr[n - 5]);
    else if (n < 16) kV[n - 12] = bf2(vr4[n - 12]);
    else if (n < 24) { const int t = (n - 16) >> 1, i = ((n - 16) & 1) * 2;
      if (t == 0) { ka[i] = kK[i] * kT[3]; ka[i + 1] = kK[i + 1] * kT[3]; }
      else { ka[i] = fma2(kK[i + t], kT[3 - t], ka[i]); ka[i + 1] = fma2(kK[i + 1 + t], kT[3 - t], ka[i + 1]); } }
    else if (n < 28) ka[n - 24] = rb2(ka[n - 24]);
    else if (n < 30) { const int i = (n - 28) * 2; ka[i] *= kV[i]; ka[i + 1] *= kV[i + 1]; }
    else if (n == 30) { kvo[0] = pk2(ka[3].x, ka[2].x); kvo[1] = pk2(ka[1].x, ka[0].x); }
    else if (n == 31) { kvo[2] = pk2(ka[3].y, ka[2].y); kvo[3] = pk2(ka[1].y, ka[0].y); }
    else if (n == 32) lds_st<u32x4>(L_PV + w * 1024 + (hh * 32 + (cpl ^ (w * 2 + hh))) * 16, u32x4{kvo[0], kvo[1], kvo[2], kvo[3]});
  };
  u32x4 uv;
  auto u_read = [&]() __attribute__((always_inline)) { const int cpl = tid >> 4, tgp = tid & 15; uv = lds_ld<u32x4>(L_PV + (tgp >> 1) * 1024 + ((tgp & 1) * 32 + (cpl ^ tgp)) * 16); };
  auto u_store = [&]() __attribute__((always_inline)) { st128(uv, rs_ws, ((tid >> 4) * 512 + (tid & 15)) * 16, OFF_U + (ct * 32 * 512 + rt * 16) * 16); };
  u32x4 ha; u32x2 hp_; u32 ho[5];
  auto cp_step = [&](int n) __attribute__((always_inline)) {
    const u32 d = (th ? L_HC1 : L_HC0) + 32 + (tid & 255) * 16;
    if (n == 0) { const u32 a = L_RAW + th * HCS + 32 + (tid & 255) * 16; ha = lds_ld<u32x4>(a); hp_ = lds_ld<u32x2>(a - 8); }
    else if (n == 1) { if (tid < 76) lds_st<u32x4>(tid < 16 ? ((tid & 8) ? L_HC1 : L_HC0) + ((tid >> 1) & 3) * HCS + (tid & 1) * 16
                                                         : L_UR + 4096 + (tid - 16) * 16 + (tid >= 46 ? URS - 480 : 0), u32x4{0, 0, 0, 0}); }
    else if (n == 2) { lds_st<u32x4>(d, ha); ho[0] = __builtin_amdgcn_alignbit(hp_.y, hp_.x, 16); ho[1] = __builtin_amdgcn_alignbit(ha.x, hp_.y, 16); }
    else if (n == 3) { ho[2] = __builtin_amdgcn_alignbit(ha.y, ha.x, 16); ho[3] = __builtin_amdgcn_alignbit(ha.z, ha.y, 16); ho[4] = __builtin_amdgcn_alignbit(ha.w, ha.z, 16); }
    else if (n == 4) lds_st<u32x4>(d + HCS, u32x4{ho[1], ho[2], ho[3], ho[4]});
    else if (n == 5) lds_st<u32x4>(d + 2 * HCS, u32x4{hp_.y, ha.x, ha.y, ha.z});
    else if (n == 6) lds_st<u32x4>(d + 3 * HCS, u32x4{ho[0], ho[1], ho[2], ho[3]});
  };
  const u32 v2 = (l >> 3) * 1024 + (((l & 7) ^ ((l >> 3) & 6)) << 4);
  auto w2_dma = [&](int p, int i) __attribute__((always_inline)) {
    const u32 so = (7 * w + p) * 32768 + ct * 128 + i * 7168, dst = (p + 1) * SLOT + w * 4096;
    if (i == 0) dma16g<0, 0>(srd_w2, v2, so, dst);
    if (i == 1) dma16g<0, 1>(srd_w2, v2, so, dst);
    if (i == 2) dma16g<0, 2>(srd_w2, v2, so, dst);
    if (i == 3) dma16g<0, 3>(srd_w2, v2, so, dst);
  };

  u32x4 st1; u32x2 cs1;
  auto load_st = [&]() __attribute__((always_inline)) {
    st1 = ld128(rs_ws, l * 16, OFF_ST + (rp * 128 + th * 64) * 16);
    cs1 = __builtin_amdgcn_raw_buffer_load_b64(rs_ws, (l & 31) * 8, OFF_CS + (cc * 128 + fw * 32) * 8, 16);
    CFENCE();
  };
  auto st_ok = [&]() __attribute__((always_inline)) { return __all((st1.y == (u32)tag) & (st1.w == (u32)(tag >> 32)) & (cs1.y == (u32)tag)); };
  float ra[4], rbb[4]; f32x2 c[4], bv[4];

  f32x4 acc[2][4];
#pragma unroll
  for (int i = 0; i < 8; ++i) acc[i >> 2][i & 3] = f32x4{0, 0, 0, 0};
  if (mainwg) {
    __builtin_amdgcn_s_barrier();
#pragma unroll
    for (int n = 0; n < 8; ++n) { stage_dma(n >> 2, n & 3); if (n == 3) __builtin_amdgcn_s_barrier(); }
    STAMP2(41);
    waitv<8>();
    bar();
    STAMP2(42);
    kv_step(0);
    sfor<0, 33>([&](auto N) __attribute__((always_inline)) {
      constexpr int n = N.v;
      if (n < 27) st_step(n);
      if (n >= 1) kv_step(n);
      if ((n & 3) == 3 && 8 + (n >> 2) < PRO) stage_dma(2 + (n >> 4), (n >> 2) & 3);
      SB();
    });
    raw_dma();
    const u32 xa = th * 16384 + n16 * 128 + ((g4 ^ (n16 & 7)) << 4);
    const u32 wa = g4 * 4096 + 2048 + (n16 >> 2) * 256 + (((2 * fw) ^ ((n16 >> 2) | ((g4 & 1) << 2))) << 5) + (l & 3) * 8;
    u32 ad[2][4] = {{wa, wa ^ 32, xa, xa ^ 64}, {wa + 65536, (wa ^ 32) + 65536, xa + 65536, (xa ^ 64) + 65536}};
    asm volatile("" : "+v"(ad[1][0]), "+v"(ad[1][1]), "+v"(ad[1][2]), "+v"(ad[1][3]));
    u32x4 xf[2][4], wf[2][2];
    float ra1, rb1; u32x2 bb[2];
    auto rd = [&](int hs, int p) __attribute__((always_inline)) {
      const int s = hs >> 1, ks = hs & 1, hi = (s >> 1) & 1;
      const u32 so = (s & 1) * SLOT;
      if (hs > 15) return;
      if (p < 2) { const u32 a = ad[hi][p] + so + ks * 16384; wf[ks][p] = lds_tr2(a, a + 1024); }
      else { xf[ks][2 * p - 4] = lds_ld<u32x4>(ad[hi][2 + ks] + so + (2 * p - 4) * 4096); xf[ks][2 * p - 3] = lds_ld<u32x4>(ad[hi][2 + ks] + so + (2 * p - 3) * 4096); }
    };
    auto extra = [&](int hs, int j) __attribute__((always_inline)) {
      if (j >= 4) {
        const int i = j - 4, cnt = kCnt[hs];
        if (cnt > 2 && i < cnt) { const int n = issued_before(hs) + i; stage_dma(n >> 2, n & 3); }
        if (cnt <= 2 && !(i & 1) && (i >> 1) < cnt) { const int n = issued_before(hs) + (i >> 1); stage_dma(n >> 2, n & 3); }
        if (hs >= 11 && !(i & 1)) { const int n = (hs - 11) * 2 + (i >> 1); w2_dma(n >> 2, n & 3); }
      }
      if (hs == 0 && j == 4) u_read();
      if (hs == 0 && j == 7) u_store();
      if (hs == 9 && j >= 4) cp_step(j - 4);
      if (hs == 10 && j >= 5) cp_step(j - 1);
      if (hs == 11 && j == 1) {
        asm volatile("buffer_load_dwordx4 %0, %2, %3, %4 offen sc1\n\tbuffer_load_dwordx2 %1, %5, %3, %6 offen sc1" : "=&v"(st1), "=&v"(cs1)
                     : "v"(l * 16), "s"(mk_srd(ws, WS_BYTES)), "s"(OFF_ST + (rp * 128 + th * 64) * 16), "v"((l & 31) * 8), "s"(OFF_CS + (cc * 128 + fw * 32) * 8) : "memory");
      }
      if (hs == 13 && j == 3) {
        asm volatile("s_waitcnt vmcnt(4)" : "+v"(st1), "+v"(cs1)::"memory");
        if (!st_ok()) {
#pragma unroll 1
          for (int it = 0; it < SPIN_CAP; ++it) { __builtin_amdgcn_s_sleep(8); load_st(); __builtin_amdgcn_s_waitcnt(0x0F70); if (st_ok()) break; } }
      }
      if (hs == 13 && j == 5) { ra1 = __builtin_amdgcn_rcpf(bfhi(st1.x)); rb1 = __builtin_amdgcn_rcpf(bfhi(st1.z)); asm volatile("" : "+v"(ra1), "+v"(rb1)); }
      if (hs == 13 && j == 7) { ra1 *= rb1; rb1 = -bflo(st1.x) * ra1 - bflo(st1.z) * rb1; asm volatile("" : "+v"(ra1), "+v"(rb1)); }
      if (hs == 12 && j == 7) { bb[0] = lds_ld<u32x2>(L_B1 + fw * 64 + g4 * 8); bb[1] = lds_ld<u32x2>(L_B1 + fw * 64 + 32 + g4 * 8); }
      if (hs == 14 && j >= 4) { const int tt = j - 4; ra[tt] = bperm(n16 * 4 + tt * 64, ra1); rbb[tt] = bperm(n16 * 4 + tt * 64, rb1); }
      if (hs == 15) {
        if (j < 4) { const int sl = g4 * 16 + ((j >> 1) * 16 + (j & 1) * 2) * 4; const float cv = __builtin_bit_cast(float, cs1.x); c[j] = f32x2{bperm(sl, cv), bperm(sl + 4, cv)}; }
        if (j >= 4) { bv[j - 4] = bf2(bb[(j - 4) >> 1][j & 1]); asm volatile("" : "+v"(bv[j - 4])); }
      }
    };
    auto half = [&](int hs) __attribute__((always_inline)) {
      const int ks = hs & 1;
      sfor<0, 8>([&](auto J) __attribute__((always_inline)) {
        constexpr int j = J.v;
        acc[j & 1][j >> 1] = mfma(wf[ks][j & 1], xf[ks][j >> 1], acc[j & 1][j >> 1]);
        SB();
        if (j < 4) rd(hs + 1, j);
        extra(hs, j);
        SB();
      });
    };
    STAMP2(34);
    waitv<PRO - 4>();
    bar();
    STAMP2(35);
#pragma unroll
    for (int p = 0; p < 4; ++p) rd(0, p);
    SB();
    sfor<0, 8>([&](auto S) __attribute__((always_inline)) {
      constexpr int s = S.v;
      half(2 * s);
      STAMP2(48 + s);
      if constexpr (s == UDR) waitv<0>(); else if constexpr (s < 5) waitv<issued_before(2 * s + 1) - 4 * (s + 2)>(); else if constexpr (s == 5) waitv<4>(); else if constexpr (s == 6) waitv<6>();
      bar();
      STAMP2(16 + s);
      if (s == UDR && tid == 0) set_flag(ws, OFF_FU + (ct * 32 + rt) * FS, tag);
      half(2 * s + 1);
    });
  } else {
    raw_dma();
#pragma unroll
    for (int i = 0; i < 7 - NST; ++i) dma16<0>(srd_w2, g4 * 1024 + n16 * 16, (((sp >> 2) * 7 + NST + i) * 32 + w * 4) * 1024 + (sp & 3) * 256, SLOT + i * 8192 + w * 1024);
    waitv<6 + 7 - NST>();
    {
      const u32x4 ones = {0x3f803f80u, 0x3f803f80u, 0x3f803f80u, 0x3f803f80u};
      const u32 a0 = 65536 + w * 8192 + (4 * g4 + (n16 >> 2)) * 128 + (l & 3) * 8, sx = ((2 * g4 + (n16 >> 3)) & 3) << 5;
      f32x4 cs[4];
#pragma unroll
      for (int nt = 0; nt < 4; ++nt) { const u32 a = a0 + ((nt * 32) ^ sx); cs[nt] = mfma(lds_tr2(a + 4096, a + 6144), ones, mfma(lds_tr2(a, a + 2048), ones, f32x4{0, 0, 0, 0})); }
      if (n16 == 0) {
#pragma unroll
        for (int nt = 0; nt < 4; ++nt) lds_st<f32x4>(L_CSX + w * 256 + nt * 64 + g4 * 16, cs[nt]);
      }
    }
    waitv<1 + 7 - NST>();
    bar();
    st_step(0);
    if (w == 1 && sp < 28) {
      float t = 0;
#pragma unroll
      for (int i = 0; i < 8; ++i) t += lds_ld<float>(L_CSX + i * 256 + l * 4);
      __builtin_amdgcn_raw_buffer_store_b64(u32x2{__builtin_bit_cast(u32, t), (u32)tag}, rs_ws, l * 8, OFF_CS + sp * 512, 16);
    }
    STAMP2(41);
#pragma unroll
    for (int n = 1; n < 27; ++n) st_step(n);
#pragma unroll
    for (int n = 0; n < 33; ++n) kv_step(n);
    waitv<0>();
    bar();
    u_read(); u_store();
    STAMP2(34);
    u32x4 wt[7 - NST];
#pragma unroll
    for (int i = 0; i < 7 - NST; ++i) { const u32 a = SLOT + i * 8192 + (4 * g4 + (n16 >> 2)) * 256 + w * 32 + (l & 3) * 8; wt[i] = lds_tr2(a, a + 4096); }
    SB();
#pragma unroll
    for (int i = 0; i < 7 - NST; ++i) st128(wt[i], rs_ws, l * 16, OFF_W2T + ((((sp & 3) * 2 + (w >> 2)) * 56 + (sp >> 2) * 7 + NST + i) * 4 + (w & 3)) * 1024);
    waitv<0>();
    bar();
    if (tid == 0) { set_flag(ws, OFF_FU + (ct * 32 + rt) * FS, tag); set_flag(ws, OFF_FW + sp * FS, tag); }
    STAMP2(35);
#pragma unroll
    for (int n = 0; n < 7; ++n) cp_step(n);
#pragma unroll
    for (int n = 0; n < 10; ++n) w2_dma(n >> 2, n & 3);
#pragma unroll
    for (int i = 0; i < 4; ++i) { ra[i] = 0; rbb[i] = 0; c[i] = sp2(0.f); bv[i] = sp2(0.f); }
  }
  STAMP(2);
  const u32 fuw_off = (l & 4) ? OFF_FW + (w * 4 + (ct >> 1)) * FS : OFF_FU + ((b >> 5) * 32 + 4 * w + (l & 3)) * FS;
  const u64 fuv = flag_ld(ws, fuw_off);
  w2_dma(2, 2); w2_dma(2, 3);
  u32x4 uu;
  u32x4 w2f[28];
  auto kmap = [&](int p) __attribute__((always_inline)) { return p < NST || !WROT ? p : NST + ((p - NST + r) & 3); };
  auto load_w2 = [&](int j) __attribute__((always_inline)) { w2f[j] = ld128(rs_ws, l * 16 + (j & 3) * 1024, OFF_W2T + (ct * 56 + 7 * w + kmap(j >> 2)) * 4096); SB(); };
  const u32 t2a = SLOT + w * 4096 + (4 * g4 + (n16 >> 2)) * 128 + (l & 3) * 8, t2x = ((2 * g4 + (n16 >> 3)) & 3) << 5;
  auto w2_rd = [&](int p) __attribute__((always_inline)) {
#pragma unroll
    for (int nt = 0; nt < 4; ++nt) { const u32 a = t2a + ((nt * 32) ^ t2x) + p * SLOT; w2f[p * 4 + nt] = lds_tr2(a, a + 2048); }
    SB();
  };
  STAMP(3);
  {
    const float c1 = -2.f * 0.7978845608028654f * 1.4426950408889634f, c2 = c1 * 0.044715f;
    const u32 hso = mainwg ? OFF_H + ((rp * 8 + th * 4) * 56 + cc * 4 + fw) * 1024 : 0x7f000000u;
#define EACH4(stmt) do { _Pragma("unroll") for (int j = 0; j < 4; ++j) { stmt; } SB(); } while (0)
#pragma unroll
    for (int tt = 0; tt < 4; ++tt) {
      f32x2 x[4], p[4]; u32 hp[4];
      EACH4(x[j] = fma2(c[j], sp2(rbb[tt]), bv[j]));
      EACH4(x[j] = fma2(((j & 1) ? acc[j >> 1][tt].hi : acc[j >> 1][tt].lo), sp2(ra[tt]), x[j]));
      EACH4(p[j] = x[j] * x[j]);
      EACH4(p[j] = fma2(p[j], sp2(c2), sp2(c1)));
      EACH4(p[j] = p[j] * x[j]);
      if (tt >= 2 && (tt - 2) * 2 < NWA) load_w2(NST * 4 + (tt - 2) * 2);
      EACH4(p[j] = exp2x2(p[j]));
      EACH4(p[j] = p[j] + sp2(1.f));
      if (tt >= 2 && (tt - 2) * 2 + 1 < NWA) load_w2(NST * 4 + (tt - 2) * 2 + 1);
      EACH4(p[j] = rcpx2(p[j]));
      EACH4(p[j] = p[j] * x[j]);
      if (tt == 0) {
        asm volatile("" : "+v"(p[0]), "+v"(p[1]), "+v"(p[2]), "+v"(p[3]));
        waitv<0>();
        flag_chk(fuv, ws, fuw_off, tag); uu = ld128(rs_ws, tid * 16, OFF_U + b * 8192); SB();
      }
      EACH4(hp[j] = pk2(p[j].x, p[j].y));
      st128(u32x4{hp[0], hp[1], hp[2], hp[3]}, rs_ws, l * 16, hso + tt * 56 * 1024);
      SB();
      if (tt < NST) w2_rd(tt);
      if (tt == 1) {
        lds_st<u32x2>(L_UR + (2044 - 4 * tid) * 2, u32x2{uu.x, uu.y});
        lds_st<u32x2>(L_UR + URS + (2044 - 4 * tid) * 2, u32x2{uu.z, uu.w});
        SB();
      }
    }
  }
  STAMP(4);
  bar();
  STAMP(5);

  const u32 fh_off = OFF_FH + ((rt >> 1) * 16 + min(n16, 13)) * FS;
  {
    constexpr int NW1 = NST * 4 + NWA;
    const int ch = w & 1, kq = w >> 1;
    f32x4 ca[8];
#pragma unroll
    for (int i = 0; i < 8; ++i) ca[i] = f32x4{0, 0, 0, 0};
    const u32 ab = (ch ? L_HC1 : L_HC0) + ((15 - n16) & 3) * HCS + (16 - ((15 - n16) & 12) + 8 * g4) * 2 + kq * 128;
    const u32 bb = L_UR + ch * URS + (2032 - 1792 - 16 * n16 + 8 * g4) * 2 + kq * 128;
    const u32 pa = L_CP + (kq * 2 + ch) * 8192 + (4 * n16 + (g4 ^ ((n16 >> 1) & 3))) * 16;
    u32x4 A[8], B[8], A1[4], B1[4];
    auto rdp = [&](u32x4& a, u32x4& bq, int k0, int i) __attribute__((always_inline)) { SB(); a = lds_2x64(ab + k0 * 64 + i * 512); bq = lds_ld<u32x4>(bb + k0 * 64 + (7 - i) * 512); SB(); };
    int nv = NW1;
    auto vm = [&]() __attribute__((always_inline)) { SB(); if (nv < 28) load_w2(nv); if (nv == 28) dma16<0>(srd_q, vr, (rt * 64 + w * 8) * 1024 + ct * 128, L_QS + w * 1024); ++nv; SB(); };
#pragma unroll
    for (int i = 0; i < 4; ++i) rdp(A[i], B[i], 0, i);
#pragma unroll
    for (int n = 0; n < 16; ++n) { const int i = n >> 2, t = n & 3; ca[i + t] = mfma(A[i], B[t], ca[i + t]); if (n < 8 && (n & 1)) rdp(A[4 + (n >> 1)], B[4 + (n >> 1)], 0, 4 + (n >> 1)); }
    STAMP2(29); waitv<0>(); STAMP2(30);
    if (mainwg) arrive_flag(ws, L_CNT, OFF_FH + (rp * 16 + cc) * FS, tag, l);
    SB();
    {
      int cn = 0;
#pragma unroll
      for (int t = 0; t < 4; ++t)
#pragma unroll
        for (int i = 0; i + t < 4; ++i) {
          ca[4 + i + t] = mfma(A[i], B[4 + t], ca[4 + i + t]); ca[4 + i + t] = mfma(A[4 + t], B[i], ca[4 + i + t]);
          if (cn >= 4 && cn < 8) rdp(A1[cn - 4], B1[cn - 4], 1, cn - 4);
          if (!(cn & 1)) vm();
          ++cn;
        }
    }
#pragma unroll
    for (int i = 0; i < 4; ++i) rdp(A[4 + i], B[4 + i], 1, 4 + i);
    u64 fhv;
    {
      int cn = 0;
#pragma unroll
      for (int T = 0; T < 8; ++T) {
#pragma unroll
        for (int i = 0; i <= T; ++i) {
          ca[T] = mfma(i < 4 ? A1[i] : A[i], T - i < 4 ? B1[T - i] : B[T - i], ca[T]);
          if (T >= 2 && i == 1) { SB(); lds_st<f32x4>(pa + (T - 1) * 1024, ca[T - 1]); if (T == 2) lds_st<f32x4>(pa, ca[0]); SB(); }
          if ((cn & 3) == 0 && cn < 32) vm();
          if (cn == 30 && w == 0) { fhv = flag_ld(ws, fh_off); SB(); }
          ++cn;
        }
      }
    }
    SB();
    lds_st<f32x4>(pa + 7 * 1024, ca[7]);
#pragma unroll
    for (int j = 0; j < 16; ++j) if (nv <= 28) vm();
    STAMP(6);
    if (w == 0) flag_chk(fhv, ws, fh_off, tag);
    STAMP2(31);
  }
  bar();
  STAMP(7);
  u32x4 hf[28];
  auto load_h = [&](int n) __attribute__((always_inline)) { if (n < 28) { hf[n] = ld128(rs_ws, l * 16, OFF_H + ((rt * 4 + n / 7) * 56 + 7 * w + kmap(n % 7)) * 1024); SB(); } };
  {
    const u32 pa = L_CP + ((tid & ~3) | ((tid & 3) ^ ((tid >> 3) & 3))) * 16;
    f32x4 pr[8];
#pragma unroll
    for (int i = 0; i < 8; ++i) pr[i] = lds_ld<f32x4>(pa + i * 8192);
    SB();
    load_h(0); load_h(1); load_h(2);
    const f32x2 d2 = bf2(dd);
    f32x2 a[4] = {f32x2{bfhi(uu.y), bfhi(uu.w)}, f32x2{bflo(uu.y), bflo(uu.w)}, f32x2{bfhi(uu.x), bfhi(uu.z)}, f32x2{bflo(uu.x), bflo(uu.z)}};
#pragma unroll
    for (int j = 0; j < 4; ++j) a[j] = rb2(a[j] * d2);
    SB();
    load_h(3);
    const f32x4 y0 = (pr[0] + pr[2]) + (pr[4] + pr[6]), y1 = (pr[1] + pr[3]) + (pr[5] + pr[7]);
    u32 o[4];
#pragma unroll
    for (int j = 0; j < 4; ++j) { const f32x2 t = rb2(f32x2{y0[j], y1[j]}) + a[j]; o[j] = pk2(t.x, t.y); }
    st128(u32x4{o[0], o[1], o[2], o[3]}, rs_ws, (tid >> 4) * 65536 + (tid & 15) * 16, OFF_Y + b * 256);
  }
  SB();
#pragma unroll
  for (int n = 4; n < HLA; ++n) load_h(n);
  STAMP(8);
  const int tk = tid >> 5, cq = tid & 31;
  const u32 fy_off = OFF_FY + (ct * 32 + 4 * w + (l & 3)) * FS;
  const u32 ra0 = tk * 256 + (((cq >> 1) ^ (tk & 7)) << 4) + (cq & 1) * 8;
  u32 qv[4], stf, b2v; u64 fyv; u32x4 yl; f32x2 gg[4], pr[8], zz, xn[4]; float sm1[4], si1[4];
  auto gate = [&](int t, int part) __attribute__((always_inline)) {
    if (part == 0) {
#pragma unroll
      for (int i = 0; i < 8; ++i) pr[i] = *(const volatile LAS f32x2*)(t * 32768 + i * 4096 + ra0);
    } else if (part == 1) {
      zz = (((pr[0] + pr[1]) + (pr[2] + pr[3])) + ((pr[4] + pr[5]) + (pr[6] + pr[7]))) + bf2(b2v);
      asm volatile("" : "+v"(zz));
    } else {
      gg[t] = zz * rcpx2(exp2x2(zz * sp2(-1.4426950408889634f)) + sp2(1.f));
      asm volatile("" : "+v"(gg[t]));
    }
    SB();
  };
  f32x4 a2[2][4];
  const u32 wa2 = w * 4096 + n16 * 256;
  auto pwrite = [&](int t, int nt) __attribute__((always_inline)) { lds_st<f32x4>(t * 32768 + wa2 + (((4 * nt + g4) ^ (n16 & 7)) << 4), a2[t & 1][nt]); SB(); };
#pragma unroll
  for (int t = 0; t < 4; ++t) {
#pragma unroll
    for (int i = 0; i < 7; ++i) {
      if (t == 0 && i == 4) { waitv<0>(); arrive_flag(ws, L_CNT + 4, OFF_FY + b * FS, tag, l); SB();
#pragma unroll
        for (int n = 0; n < 4; ++n) load_h(HLA + n);
        b2v = *(const u32*)(b2 + ct * 64 + cq * 2);
        stf = __builtin_amdgcn_raw_buffer_load_b32(rs_ws, l * 16, OFF_ST + rt * 1024, 16);
        SB();
      }
#pragma unroll
      for (int nt = 0; nt < 4; ++nt) {
        a2[t & 1][nt] = mfma(w2f[i * 4 + nt], hf[t * 7 + i], i ? a2[t & 1][nt] : f32x4{0, 0, 0, 0});
        if (t > 0 && i < 2 && (nt & 1)) { SB(); pwrite(t - 1, i * 2 + (nt >> 1)); }
      }
      SB();
      if (t * 7 + i >= 4) load_h(t * 7 + i + HLA);
      if (t > 0 && i == 3) { bar(); gate(t - 1, 0); }
      if (t > 0 && i == 4) gate(t - 1, 1);
      if (t > 0 && i == 5) gate(t - 1, 2);
      if (t == 2 && i == 0) {
#pragma unroll
        for (int e = 0; e < 4; ++e) { qv[e] = lds_ld<u32>(L_QS + (e * 16 + tk) * 128 + cq * 4); sm1[e] = bperm((e * 16 + tk) * 4, __builtin_bit_cast(float, stf)); }
        SB();
      }
      if (t == 2 && (i == 1 || i == 2)) {
#pragma unroll
        for (int e = (i - 1) * 2; e < (i - 1) * 2 + 2; ++e) {
          const u32 sb = __builtin_bit_cast(u32, sm1[e]);
          xn[e] = rb2(rb2(bf2(qv[e]) - sp2(bflo(sb))) * sp2(__builtin_amdgcn_rcpf(bfhi(sb))));
          asm volatile("" : "+v"(xn[e]));
        }
        SB();
      }
      if (t == 2 && i == 6) { fyv = flag_ld(ws, fy_off); SB(); }
      if (t == 3 && i == 2) {
        STAMP2(32);
        flag_chk(fyv, ws, fy_off, tag);
        STAMP2(33);
        yl = ld128(rs_ws, tid * 16, OFF_Y + rt * 65536 + ct * 8192);
        SB();
      }
    }
    STAMP(9 + t);
  }
#pragma unroll
  for (int nt = 0; nt < 4; ++nt) pwrite(3, nt);
#pragma unroll
  for (int e = 0; e < 4; ++e) lds_st<u32>(L_YS + ((tid >> 4) * 65 + (tid & 15) * 4 + e) * 4, yl[e]);
  bar();
  STAMP(13);
  {
    u32 yv[4];
#pragma unroll
    for (int e = 0; e < 4; ++e) yv[e] = lds_ld<u32>(L_YS + (cq * 65 + e * 16 + tk) * 4);
    gate(3, 0);
    u16* op = out + (rt * 64 + tk) * D_ + ct * 64 + cq * 2;
#pragma unroll
    for (int e = 0; e < 4; ++e) {
      if (e == 3) { gate(3, 1); gate(3, 2); }
      const f32x2 res = fma2(rb2(xn[e] * bf2(yv[e])), gg[e], bf2(qv[e]));
      *(u32*)(op + e * 16 * D_) = pk2(res.x, res.y);
      SB();
    }
  }
  if (b == 0 && w == 0) {
#pragma unroll 1
    for (int i = 0; i < 4; ++i) poll_flags(ws, OFF_FU + (i * 64 + l) * FS, tag);
    if (l == 0) set_flag(ws, OFF_E, tag);
  }
  STAMP(14);
  STAMP_END;
}

extern "C" void kernel_launch(void* const* d_in, const int* in_sizes, int n_in, void* d_out, int out_size, void* d_ws, size_t ws_size,
                              hipStream_t stream) {
  char* ws = (char*)d_ws + (WS_TAIL ? ((ws_size - WS_BYTES) & ~(size_t)4095) : 0);
  fused_kernel<<<256, 512, 0, stream>>>((const u16*)d_in[0], (const u16*)d_in[1], (const u16*)d_in[2], (const u16*)d_in[3], (const u16*)d_in[4],
                                        (const u16*)d_in[5], (const u16*)d_in[7], (const u16*)d_in[8], (const u16*)d_in[9], (const u16*)d_in[10],
                                        (u16*)d_out, ws);
}
```

```cpp
#include <hip/hip_runtime.h>
typedef unsigned short u16;
typedef unsigned u32;
typedef unsigned long long u64;
typedef __bf16 bf16x8 __attribute__((ext_vector_type(8)));
typedef __bf16 bf16x2 __attribute__((ext_vector_type(2)));
typedef short s16x4 __attribute__((ext_vector_type(4)));
typedef float f32x4 __attribute__((ext_vector_type(4)));
typedef float f32x2 __attribute__((ext_vector_type(2)));
typedef unsigned u32x4 __attribute__((ext_vector_type(4)));
typedef unsigned u32x2 __attribute__((ext_vector_type(2)));
#define LAS __attribute__((address_space(3)))
#define GAS __attribute__((address_space(1)))
#define DI __device__ __forceinline__
#pragma clang diagnostic ignored "-Wint-to-pointer-cast"

#define FSTR 256
constexpr int S_ = 2048, D_ = 512, F_ = 1792;
constexpr u32 OFF_E = 0, OFF_CS = 16384, OFF_ST = 32768, FS = FSTR  , OFF_FU = 65536, OFF_FW = OFF_FU + 256 * FS, OFF_FH = OFF_FW + 256 * FS, OFF_FY = OFF_FH + 256 * FS;
constexpr u32 OFF_U = 1u << 20, OFF_Y = 3u << 20, OFF_W2T = 5u << 20, OFF_H = 7u << 20, WS_BYTES = 14u << 20;
constexpr u32 SLOT = 32768, L_PK = 131072, L_PV = 140288, L_PW = 148480, L_SK = 156928, L_CNT = 157696,
              L_RC = 157824  , L_CC = 158848  , L_CSX = 159872,
              L_HC0 = 131072, L_HC1 = 0, HCS = 4160, L_UR = 147712, URS = 4576, L_CP = 32768, L_YS = 131072, L_QS = 147712;
constexpr u32 C_FULL = L_CNT  , C_FREE = L_CNT + 48  , C_H = L_CNT + 80, C_W = L_CNT + 84, C_U = L_CNT + 88, C_Y = L_CNT + 92,
              C_S1 = L_CNT + 96, C_S2 = L_CNT + 100, C_S3 = L_CNT + 104, C_SA = L_CNT + 36  , C_T = L_CNT + 112  , L_B1 = 157440;
#define PSLEEP 8
#define NT_G 0
#define NT_P 1
#define WS_TAIL 0
#define BPRIO 1
#define NWB 24
#define WDR 1
#define SPQ 32
#define KVD -1
#define KROT 0
#define DEEP 0
#define PREPS 6
#define LPRIO 2
#define WGO 5
#define PFIRST 0
#define UDR 2
#define LFIRST 0
#define APRIO 1
#define CPS 7
#define HLA 16

#define STAMP(i)
#define STAMP_INIT
#define STAMP_END
#define STAMP2(i)

template <int V> struct IC { static constexpr int v = V; };
template <int I, int N, class F> DI void sfor(F&& f) { if constexpr (I < N) { f(IC<I>{}); sfor<I + 1, N>(f); } }
DI u32x4 mk_srd(const void* p, u32 bytes) { u64 a = (u64)p; return u32x4{(u32)a, (u32)(a >> 32) & 0xffffu, bytes, 0x00020000u}; }
template <int NT> DI void dma16(u32x4 srd, u32 voff, u32 soff, u32 lds) {
  if (NT) asm volatile("s_mov_b32 m0, %3\n\ts_nop 0\n\tbuffer_load_dwordx4 %0, %1, %2 offen nt lds" ::"v"(voff), "s"(srd), "s"(soff), "s"(lds) : "memory");
  else asm volatile("s_mov_b32 m0, %3\n\ts_nop 0\n\tbuffer_load_dwordx4 %0, %1, %2 offen lds" ::"v"(voff), "s"(srd), "s"(soff), "s"(lds) : "memory");
}
template <int NT, int I> DI void dma16g(u32x4 srd, u32 voff, u32 soff, u32 lds) {
  if (I == 0) dma16<NT>(srd, voff, soff, lds);
  else if (NT) asm volatile("buffer_load_dwordx4 %0, %1, %2 offen offset:%3 nt lds" ::"v"(voff), "s"(srd), "s"(soff), "i"(I * 1024) : "memory");
  else asm volatile("buffer_load_dwordx4 %0, %1, %2 offen offset:%3 lds" ::"v"(voff), "s"(srd), "s"(soff), "i"(I * 1024) : "memory");
}
DI void dma16m(u32x4 srd, u32 voff, u32 soff, u32 lds, u64 mask) {
  asm volatile("s_mov_b32 m0, %3\n\ts_mov_b64 exec, %4\n\tbuffer_load_dwordx4 %0, %1, %2 offen lds\n\ts_mov_b64 exec, -1"
               ::"v"(voff), "s"(srd), "s"(soff), "s"(lds), "s"(mask) : "memory");
}
template <int N> DI void waitv() { asm volatile("s_waitcnt vmcnt(%0)" ::"i"(N) : "memory"); }
#define WAITL0() asm volatile("s_waitcnt lgkmcnt(0)" ::: "memory")
#define CFENCE() asm volatile("" ::: "memory")
#define SB() __builtin_amdgcn_sched_barrier(0)
DI void bar() { SB(); CFENCE(); __builtin_amdgcn_s_waitcnt(0xC07F); __builtin_amdgcn_s_barrier(); CFENCE(); SB(); }
DI float bflo(u32 x) { return __uint_as_float(x << 16); }
DI float bfhi(u32 x) { return __uint_as_float(x & 0xffff0000u); }
DI u32 pk2(float a, float b) { f32x2 p = {a, b}; return __builtin_bit_cast(u32, __builtin_convertvector(p, bf16x2)); }
DI float rb(float a) { return bflo(pk2(a, a)); }
DI f32x2 bf2(u32 x) { return f32x2{bflo(x), bfhi(x)}; }
DI f32x2 rb2(f32x2 v) { return bf2(pk2(v.x, v.y)); }
DI f32x2 sp2(float v) { return f32x2{v, v}; }
DI f32x2 exp2x2(f32x2 v) { return f32x2{__builtin_amdgcn_exp2f(v.x), __builtin_amdgcn_exp2f(v.y)}; }
DI f32x2 rcpx2(f32x2 v) { return f32x2{__builtin_amdgcn_rcpf(v.x), __builtin_amdgcn_rcpf(v.y)}; }
DI f32x2 fma2(f32x2 a, f32x2 b, f32x2 c) { return __builtin_elementwise_fma(a, b, c); }
DI float sfma(float a, float b, float c) { return __builtin_fmaf(a, b, c); }
DI float smul(float a, float b) { return a * b; }
DI float sadd(float a, float b) { return a + b; }
DI f32x2 fma2s(f32x2 a, f32x2 b, f32x2 c) { return fma2(a, b, c); }
DI f32x2 mul2s(f32x2 a, f32x2 b) { return a * b; }
DI f32x2 add2s(f32x2 a, f32x2 b) { return a + b; }
template <class T> DI T lds_ld(u32 off) { return *(const LAS T*)(off); }
template <class T> DI void lds_st(u32 off, T v) { *(LAS T*)(off) = v; }
DI u32x2 lds_tr(u32 off) { return __builtin_bit_cast(u32x2, __builtin_amdgcn_ds_read_tr16_b64_v4i16((LAS s16x4*)(off))); }
DI u32x4 lds_tr2(u32 o0, u32 o1) { u32x2 a = lds_tr(o0), b = lds_tr(o1); return u32x4{a.x, a.y, b.x, b.y}; }
DI u32x4 lds_2x64(u32 o) { u32x2 a = *(const volatile LAS u32x2*)(o), b = *(const volatile LAS u32x2*)(o + 8); return u32x4{a.x, a.y, b.x, b.y}; }
DI f32x4 mfma(u32x4 a, u32x4 b, f32x4 c) {
  return __builtin_amdgcn_mfma_f32_16x16x32_bf16(__builtin_bit_cast(bf16x8, a), __builtin_bit_cast(bf16x8, b), c, 0, 0, 0);
}
DI void st128(u32x4 v, __amdgpu_buffer_rsrc_t rs, u32 voff, u32 soff) {
  __builtin_amdgcn_raw_buffer_store_b128(v, rs, voff, soff, 16);
  asm volatile("s_nop 1" ::"v"(v) : "memory");
}
DI u32x4 ld128(__amdgpu_buffer_rsrc_t rs, u32 voff, u32 soff) { return __builtin_amdgcn_raw_buffer_load_b128(rs, voff, soff, 16); }
template <int C> DI float dpp_add(float v) {
  return v + __builtin_bit_cast(float, __builtin_amdgcn_update_dpp(0, __builtin_bit_cast(int, v), C, 0xf, 0xf, true));
}
DI float bperm(int src_lane4, float v) { return __builtin_bit_cast(float, __builtin_amdgcn_ds_bpermute(src_lane4, __builtin_bit_cast(int, v))); }
DI float sel(bool c, float a, float b) { return c ? a : b; }
DI float rl(float v, int i) { return __builtin_bit_cast(float, __builtin_amdgcn_readlane(__builtin_bit_cast(int, v), i)); }
DI float wave_sum(float v) {
  v = dpp_add<0xB1>(v); v = dpp_add<0x4E>(v); v = dpp_add<0x141>(v); v = dpp_add<0x140>(v);
  return (rl(v, 0) + rl(v, 16)) + (rl(v, 32) + rl(v, 48));
}
constexpr int SPIN_CAP = 1 << 16;
DI u64 flag_ld(const char* ws, u32 off) { return __hip_atomic_load((const GAS u64*)(ws + off), __ATOMIC_RELAXED, __HIP_MEMORY_SCOPE_AGENT); }
DI void poll_flags(const char* ws, u32 off, u64 tag) {
#pragma unroll 1
  for (int it = 0; it < SPIN_CAP; ++it) {
    if (__all(flag_ld(ws, off) == tag)) break;
    __builtin_amdgcn_s_sleep(PSLEEP);
  }
  CFENCE();
}
DI void flag_chk(u64 v, const char* ws, u32 off, u64 tag) { if (!__all(v == tag)) poll_flags(ws, off, tag); CFENCE(); }
DI void set_flag(char* ws, u32 off, u64 tag) { __hip_atomic_store((GAS u64*)(ws + off), tag, __ATOMIC_RELAXED, __HIP_MEMORY_SCOPE_AGENT); }
DI void arrive_flag(char* ws, u32 cnt, u32 n, u32 off, u64 tag, int l) {
  if (l == 0) {
    u32 old;
    asm volatile("ds_add_rtn_u32 %0, %1, %2\n\ts_waitcnt lgkmcnt(0)" : "=&v"(old) : "v"(cnt), "v"(1u) : "memory");
    if (old == n - 1) set_flag(ws, off, tag);
  }
  CFENCE();
}
DI void cnt_add(u32 cnt) { asm volatile("s_mov_b64 exec, 1\n\tds_add_u32 %0, %1\n\ts_mov_b64 exec, -1" ::"v"(cnt), "v"(1u) : "memory"); }
DI u32 cnt_ld(u32 cnt) { return *(const volatile LAS u32*)cnt; }
DI void cnt_wait(u32 cnt, u32 n) {
#pragma unroll 1
  for (int it = 0; it < (1 << 22); ++it) { if ((u32)__builtin_amdgcn_readfirstlane(cnt_ld(cnt)) >= n) break; __builtin_amdgcn_s_sleep(1); }
  CFENCE(); SB();
}
DI void group_sync(u32 cnt) { SB(); WAITL0(); cnt_add(cnt); cnt_wait(cnt, 4); }
#define AI __attribute__((always_inline))
#define EACH4(stmt) do { _Pragma("unroll") for (int j = 0; j < 4; ++j) { stmt; } SB(); } while (0)
#define HALF(v, j) (((j) & 1) ? (v).hi : (v).lo)

__global__ __launch_bounds__(512, 2) void fused_kernel(const u16* __restrict__ q, const u16* __restrict__ kk, const u16* __restrict__ vv,
                                                       const u16* __restrict__ sk, const u16* __restrict__ ssk, const u16* __restrict__ sD,
                                                       const u16* __restrict__ w1, const u16* __restrict__ b1, const u16* __restrict__ w2,
                                                       const u16* __restrict__ b2, u16* __restrict__ out, char* __restrict__ ws) {
  __shared__ __attribute__((aligned(16))) char sm[163840];
  asm volatile("" ::"v"(sm) : "memory");
  const int tid = threadIdx.x, l = tid & 63;
  const int w = __builtin_amdgcn_readfirstlane(tid >> 6);
  const int b = blockIdx.x, xcd = b & 7, r = b >> 3;
  const bool mainwg = r < 28;
  const int rp = (xcd & 3) * 4 + (r & 3), cc = (xcd >> 2) * 7 + (r >> 2);
  const int rt = (xcd & 3) * 8 + (xcd >> 2) * 4 + (r & 3), ct = r >> 2;
  const int gw = w & 3;
  const int n16 = l & 15, g4 = l >> 4;
  STAMP_INIT
  STAMP(0);
  if (tid < 8) lds_st<u32x4>(L_CNT + tid * 16, u32x4{0, 0, 0, 0});
  const u32x4 srd_q = mk_srd(q, S_ * D_ * 2);
  const u32x4 srd_w1m = mk_srd((const char*)w1 - F_ * 2, D_ * F_ * 2 + F_ * 2);
  u32x2 ev;
  u64 tag;
  auto load_tag = [&]() AI { asm volatile("s_mov_b64 exec, 1\n\tglobal_load_dwordx2 %0, %1, %2 sc1\n\ts_mov_b64 exec, -1" : "=v"(ev) : "v"(0u), "s"(ws) : "memory"); };
  auto get_tag = [&]() AI {
    SB();
    asm volatile("" : "+v"(ev));
    tag = (((u64)__builtin_amdgcn_readfirstlane(ev.y) << 32) | (u32)__builtin_amdgcn_readfirstlane(ev.x)) + 0x9E3779B97F4A7C15ull;
  };
  const __amdgpu_buffer_rsrc_t rs_ws = __builtin_amdgcn_make_buffer_rsrc(ws, 0, WS_BYTES, 0x00020000);
  const u32 vr = (l >> 3) * 1024 + (l & 7) * 16;
  u32 qs_so = (rt * 64 + w * 8) * 1024 + ct * 128;
  asm volatile("" : "+s"(qs_so), "+s"(b2), "+s"(out), "+s"(b1), "+s"(ssk));
  u32x4 w2f[28];
  auto load_w2 = [&](int j) AI { w2f[j] = ld128(rs_ws, l * 16 + (j & 3) * 1024, OFF_W2T + (ct * 56 + 7 * w + (j >> 2)) * 4096); SB(); };
  const u32 fw_off = OFF_FW + (4 * w + (ct >> 1)) * FS;
  const u32 fh_off = OFF_FH + ((rt >> 1) * 16 + min(((7 * w) >> 2) + (l & 3), (7 * w + 6) >> 2)) * FS;

  const u32 sx32 = ((2 * g4 + (n16 >> 3)) & 3) << 5, tr0 = (4 * g4 + (n16 >> 2)) * 128 + (l & 3) * 8, vsw = ((l & 7) ^ ((l >> 3) & 6)) << 4;
  const u32 dd = *(const u32*)(sD + 2 * b);
  const int sp = (r - 28) * 8 + xcd;
  if (w >= 4) {
    const int t2 = tid & 255;
    u32x4 ha[2]; u32x2 hn[2];
    auto load_taps = [&]() AI {
      asm volatile("buffer_load_dwordx4 %0, %4, %5, %6 offen nt\n\tbuffer_load_dwordx4 %1, %4, %5, %6 offen offset:1024 nt\n\t"
                   "buffer_load_dwordx2 %2, %4, %5, %6 offen offset:16\n\tbuffer_load_dwordx2 %3, %4, %5, %6 offen offset:1040"
                   : "=&v"(ha[0]), "=&v"(ha[1]), "=&v"(hn[0]), "=&v"(hn[1]) : "v"(l * 16), "s"(mk_srd(ssk, S_ * D_ * 2)), "s"(b * 8192 + gw * 2048) : "memory");
    };
    __builtin_amdgcn_s_setprio(LPRIO);
    const int srow = rt * 64 + ct * 8 + gw * 2;
    u32x4 x0[2];
    load_tag();
    asm volatile("buffer_load_dwordx4 %0, %2, %3, %4 offen\n\tbuffer_load_dwordx4 %1, %2, %3, %4 offen offset:1024"
                 : "=&v"(x0[0]), "=&v"(x0[1]) : "v"(l * 16), "s"(srd_q), "s"(srow * 1024) : "memory");
    const u32 vx0 = (l >> 3) * 1024 + (((l & 7) ^ (l >> 3)) << 4), vx1 = vx0 + 8192 - 1024;
    const u32 vwb = (g4 + 1) * (F_ * 2) - 2048, vws = (n16 ^ (g4 << 1)) << 4;
    const u32 vw[4] = {vwb + vws, vwb + vws + 4 * (F_ * 2) - 1024, vwb + (vws ^ 128), vwb + (vws ^ 128) + 4 * (F_ * 2) - 1024};
    auto stage = [&](int s) AI {
      const u32 sx = rp * 131072 + gw * 32768 + s * 128, sw = (gw * 16 + s * 64) * (F_ * 2) + cc * 256, dst = (s & 3) * SLOT + gw * 8192;
#pragma unroll
      for (int i = 0; i < 2; ++i) {
        dma16g<NT_G, 0>(srd_q, vx0, sx + i * 16384, dst + i * 4096); dma16g<NT_G, 1>(srd_q, vx1, sx + i * 16384, 0);
        dma16g<NT_G, 2>(srd_w1m, vw[i * 2], sw + i * 8 * (F_ * 2), 0); dma16g<NT_G, 3>(srd_w1m, vw[i * 2 + 1], sw + i * 8 * (F_ * 2), 0);
      }
    };
    bar();
    if (mainwg) { stage(0); stage(1); }
    else {
#pragma unroll
      for (int p = 0; p < 4; ++p) {
        const int hp = gw * 4 + p;
        sfor<0, 4>([&](auto I) AI { dma16g<NT_P, I.v>(mk_srd(w2, hp < 14 ? D_ * F_ * 2 : 0), (l >> 3) * 1024 + vsw,
                                                     ((sp >> 2) * 7 + (hp >> 1)) * 32768 + (sp & 3) * 256 + (hp & 1) * 128 + I.v * 7168, hp * 4096); });
      }
    }
    waitv<16>();
    get_tag();
    STAMP2(40);
    {
      asm volatile("" : "+v"(x0[0]), "+v"(x0[1]));
      float c0[2], sa[2], sq[2];
#pragma unroll
      for (int i = 0; i < 2; ++i) {
        c0[i] = bflo(__builtin_amdgcn_readfirstlane(x0[i].x));
        const f32x2 d[4] = {bf2(x0[i].x) - sp2(c0[i]), bf2(x0[i].y) - sp2(c0[i]), bf2(x0[i].z) - sp2(c0[i]), bf2(x0[i].w) - sp2(c0[i])};
        const f32x2 s2 = (d[0] + d[1]) + (d[2] + d[3]), q2 = fma2(d[0], d[0], d[1] * d[1]) + fma2(d[2], d[2], d[3] * d[3]);
        sa[i] = s2.x + s2.y; sq[i] = q2.x + q2.y;
      }
#define RED4(C) do { sa[0] = dpp_add<C>(sa[0]); sq[0] = dpp_add<C>(sq[0]); sa[1] = dpp_add<C>(sa[1]); sq[1] = dpp_add<C>(sq[1]); SB(); } while (0)
      SB(); RED4(0xB1); RED4(0x4E); RED4(0x141); RED4(0x140); RED4(0x142); RED4(0x143);
      const bool odd = l & 1;
      const float md = sel(odd, sa[1], sa[0]) * (1.f / D_), mean = sel(odd, c0[1], c0[0]) + md, var = fmaxf(sel(odd, sq[1], sq[0]) * (1.f / D_) - md * md, 0.f);
      const float m1 = rb(mean), sd1 = rb(__builtin_amdgcn_sqrtf(rb(rb(var) + 1e-5f)));
      const float i1 = __builtin_amdgcn_rcpf(sd1);
      const float m2 = (mean - m1) * i1, sd2 = __builtin_amdgcn_sqrtf(rb(rb(var * i1 * i1) + 1e-5f));
      if (l >= 62) st128(u32x4{pk2(m1, sd1), (u32)tag, pk2(m2, sd2), (u32)(tag >> 32)}, rs_ws, (l - 62) * 16, OFF_ST + srow * 16);
    }
    STAMP2(43);
    if (mainwg) {
      waitv<9>(); cnt_add(C_FULL);
      u32x4 g;
#pragma unroll 1
      for (int s = 2; s < 8; ++s) {
        if (s >= 4) cnt_wait(C_FREE + 4 * (s - 4), 4);
        if (s == 6) {
          asm volatile("buffer_load_dwordx4 %0, %1, %2, %3 offen sc1" : "=v"(g) : "v"(l * 16), "s"(mk_srd(ws, WS_BYTES)), "s"(OFF_ST + rp * 2048 + (gw & 1) * 1024) : "memory");
          dma16m(mk_srd(b1, F_ * 2), l * 16, cc * 256, L_B1, 0xffffull);
        }
        stage(s);
        waitv<8>(); cnt_add(C_FULL + 4 * (s - 1));
      }
      STAMP2(41);
      SB();
      asm volatile("" : "+v"(g));
      load_taps();
      if (gw < 2) {
        auto g_ok = [&]() AI { return __all((g.y == (u32)tag) & (g.w == (u32)(tag >> 32))); };
        if (!g_ok()) {
#pragma unroll 1
          for (int it = 0; it < SPIN_CAP; ++it) { g = ld128(rs_ws, l * 16, OFF_ST + rp * 2048 + gw * 1024); if (g_ok()) break; __builtin_amdgcn_s_sleep(4); CFENCE(); }
        }
        const float i2 = __builtin_amdgcn_rcpf(bfhi(g.z));
        float ra = __builtin_amdgcn_rcpf(bfhi(g.x)) * i2;
        asm volatile("" : "+v"(ra));
        lds_st<f32x2>(L_RC + (gw * 64 + l) * 8, f32x2{ra, -bflo(g.x) * ra - bflo(g.z) * i2});
      } else lds_st<u32>(L_CC + ((gw & 1) * 64 + l) * 4, (u32)lds_ld<u16>(L_B1 + ((gw & 1) * 64 + l) * 2) << 16);
      waitv<4>();
      cnt_add(C_FULL + 28);
      STAMP2(42);
    } else {
      waitv<1>();
      SB();
#pragma unroll
      for (int p = 0; p < 4; ++p) {
        const int hp = gw * 4 + p;
        u32x4 wt[4];
#pragma unroll
        for (int nt = 0; nt < 4; ++nt) { const u32 a = hp * 4096 + tr0 + ((nt * 32) ^ sx32); wt[nt] = lds_tr2(a, a + 2048); }
#pragma unroll
        for (int nt = 0; nt < 4; ++nt)
          st128(wt[nt], rs_ws, l * 16, hp < 14 ? OFF_W2T + ((((sp & 3) * 2 + (hp & 1)) * 56 + (sp >> 2) * 7 + (hp >> 1)) * 4 + nt) * 1024 : 0x7f000000u);
      }
      STAMP2(41);
      waitv<0>();
      arrive_flag(ws, C_W, 4, OFF_FW + sp * FS, tag, l);
      STAMP2(42);
      load_taps();
      cnt_wait(C_W, 4);
      __builtin_amdgcn_s_sleep(SPQ);
    }
    STAMP(2);
    if (!mainwg) { cnt_wait(C_U, 4); }
    __builtin_amdgcn_s_setprio(0);
    const u32 fu_off = (l & 8) ? fw_off : OFF_FU + ((b >> 5) * 32 + 8 * gw + (l & 7)) * FS;
    poll_flags(ws, fu_off, tag);
    STAMP(3);
    u32x4 uu[2];
#pragma unroll
    for (int i = 0; i < 2; ++i) uu[i] = ld128(rs_ws, l * 16 + i * 1024, OFF_U + b * 8192 + gw * 2048);
    SB();
#pragma unroll
    for (int j = 0; j < NWB; ++j) load_w2(j);
    STAMP2(30);
    if (mainwg) cnt_wait(C_FREE + 4 * CPS, 4);
    asm volatile("" : "+v"(ha[0]), "+v"(ha[1]), "+v"(hn[0]), "+v"(hn[1]));
#pragma unroll
    for (int i = 0; i < 2; ++i) {
      const u32x4 x = ha[i];
      const u32 o0 = __builtin_amdgcn_alignbit(x.y, x.x, 16), o1 = __builtin_amdgcn_alignbit(x.z, x.y, 16), o2 = __builtin_amdgcn_alignbit(x.w, x.z, 16),
                o3 = __builtin_amdgcn_alignbit(hn[i].x, x.w, 16), o4 = __builtin_amdgcn_alignbit(hn[i].y, hn[i].x, 16);
      const u32 d = ((gw >> 1) ? L_HC1 : L_HC0) + 32 + (gw & 1) * 2048 + i * 1024 + l * 16;
      lds_st<u32x4>(d, x); lds_st<u32x4>(d + HCS, u32x4{o0, o1, o2, o3});
      lds_st<u32x4>(d + 2 * HCS, u32x4{x.y, x.z, x.w, hn[i].x}); lds_st<u32x4>(d + 3 * HCS, u32x4{o1, o2, o3, o4});
    }
    if (l < 19) {
      const int z = gw * 19 + l;
      lds_st<u32x4>(z < 16 ? ((z & 8) ? L_HC1 : L_HC0) + ((z >> 1) & 3) * HCS + (z & 1) * 16 : L_UR + 4096 + (z - 16) * 16 + (z >= 46 ? URS - 480 : 0), u32x4{0, 0, 0, 0});
    }
#pragma unroll
    for (int i = 0; i < 2; ++i) {
      const u32 a = L_UR + (2044 - 4 * (gw * 128 + i * 64 + l)) * 2;
      lds_st<u32x2>(a, u32x2{uu[i].x, uu[i].y}); lds_st<u32x2>(a + URS, u32x2{uu[i].z, uu[i].w});
    }
    cnt_add(C_S2);
    STAMP(4);
    cnt_wait(C_S2, mainwg ? 8 : 4);
    STAMP(5);
    {
      const int ch = gw & 1, kh = gw >> 1;
      f32x4 ca[8];
#pragma unroll
      for (int i = 0; i < 8; ++i) ca[i] = f32x4{0, 0, 0, 0};
      const u32 ab = (ch ? L_HC1 : L_HC0) + ((n16 + 1) & 3) * HCS + (((n16 + 1) & ~3) + 8 * g4) * 2 + kh * 256;
      const u32 bb = L_UR + ch * URS + (2032 - 1792 - 16 * n16 + 8 * g4) * 2 + kh * 256;
      const u32 pa = L_CP + gw * 8192 + (4 * n16 + (g4 ^ ((n16 >> 1) & 3))) * 16;
      u32x4 AL[2][4], BL[2][4], AH[4], BH[4];
      auto rdp = [&](u32x4& a, u32x4& bq, int k0, int i) AI { SB(); a = lds_2x64(ab + k0 * 64 + i * 512); bq = lds_ld<u32x4>(bb + k0 * 64 + (7 - i) * 512); SB(); };
#pragma unroll
      for (int i = 0; i < 4; ++i) rdp(AL[0][i], BL[0][i], 0, i);
      sfor<0, 4>([&](auto K) AI {
        constexpr int k0 = K.v;
        u32x4 (&A)[4] = AL[k0 & 1]; u32x4 (&B)[4] = BL[k0 & 1];
#pragma unroll
        for (int n = 0; n < 16; ++n) {
          constexpr int oi[16] = {0, 0, 1, 0, 1, 2, 0, 1, 2, 3, 1, 2, 3, 2, 3, 3}, ot[16] = {0, 1, 0, 2, 1, 0, 3, 2, 1, 0, 3, 2, 1, 3, 2, 3};
          ca[oi[n] + ot[n]] = mfma(A[oi[n]], B[ot[n]], ca[oi[n] + ot[n]]);
          if (n < 8 && (n & 1)) rdp(AH[n >> 1], BH[n >> 1], k0, 4 + (n >> 1));
          if (k0 == 3 && n >= 12) { SB(); lds_st<f32x4>(pa + (n - 12) * 1024, ca[n - 12]); SB(); }
        }
        int cn = 0;
#pragma unroll
        for (int T = 0; T < 4; ++T)
#pragma unroll
          for (int i = 0; i <= T; ++i) {
            ca[4 + T] = mfma(A[i], BH[T - i], ca[4 + T]); ca[4 + T] = mfma(AH[T - i], B[i], ca[4 + T]);
            if (k0 < 3 && cn >= 2 && cn < 6) rdp(AL[(k0 + 1) & 1][cn - 2], BL[(k0 + 1) & 1][cn - 2], k0 + 1, cn - 2);
            if (k0 == 3 && T > 0 && i == 1) { SB(); lds_st<f32x4>(pa + (3 + T) * 1024, ca[3 + T]); SB(); }
            ++cn;
          }
        SB();
      });
      lds_st<f32x4>(pa + 7 * 1024, ca[7]);
    }
    cnt_add(C_S3);
    STAMP(6);
#pragma unroll
    for (int j = NWB; j < 28; ++j) load_w2(j);
    cnt_wait(C_S3, 4);
    {
      const u32x2 hc = lds_ld<u32x2>(L_HC0 + 32), hb = lds_ld<u32x2>(L_HC1 + 32);
      f32x4 pr[2][4];
#pragma unroll
      for (int i = 0; i < 2; ++i) {
        const int t = gw * 128 + i * 64 + l;
        const u32 pa = L_CP + ((t & ~3) | ((t & 3) ^ ((t >> 3) & 3))) * 16;
#pragma unroll
        for (int p = 0; p < 4; ++p) pr[i][p] = lds_ld<f32x4>(pa + p * 8192);
      }
      SB();
      const f32x2 d2 = bf2(dd), h0 = {bflo(hc.x), bflo(hb.x)}, h1 = {bfhi(hc.x), bfhi(hb.x)}, h2 = {bflo(hc.y), bflo(hb.y)};
#pragma unroll
      for (int i = 0; i < 2; ++i) {
        const f32x2 a[4] = {f32x2{bfhi(uu[i].y), bfhi(uu[i].w)}, f32x2{bflo(uu[i].y), bflo(uu[i].w)}, f32x2{bfhi(uu[i].x), bfhi(uu[i].z)}, f32x2{bflo(uu[i].x), bflo(uu[i].z)}};
        f32x2 y[4], du[4]; u32 o[4];
        EACH4((y[j] = f32x2{sadd(pr[i][0][j], pr[i][2][j]), sadd(pr[i][1][j], pr[i][3][j])}));
        EACH4(du[j] = rb2(mul2s(a[j], d2)));
        y[0] = fma2s(a[0], h0, y[0]); y[1] = fma2s(a[1], h0, y[1]); y[2] = fma2s(a[2], h0, y[2]);
        y[1] = fma2s(a[0], h1, y[1]); y[2] = fma2s(a[1], h1, y[2]); y[2] = fma2s(a[0], h2, y[2]);
        EACH4(y[j] = add2s(rb2(y[j]), du[j]));
        EACH4(o[j] = pk2(y[j].x, y[j].y));
        st128(u32x4{o[0], o[1], o[2], o[3]}, rs_ws, g4 * 65536 + n16 * 16, OFF_Y + b * 256 + (gw * 8 + i * 4) * 65536);
      }
    }
    STAMP2(29);
    poll_flags(ws, fh_off, tag);
    waitv<0>();
    arrive_flag(ws, C_Y, 4, OFF_FY + b * FS, tag, l);
    STAMP(7);
  } else {
    {
      __builtin_amdgcn_s_setprio(3);
      const u32 so = (rt * 64 + gw * 16) * 1024 + ct * 128;
      const bool top = rt == 0 && gw == 0;
      sfor<0, 2>([&](auto I) AI { dma16g<NT_P, I.v>(mk_srd(kk, S_ * D_ * 2), vr, so + I.v * 7168, L_PK + 1024 + gw * 2048); });
      sfor<0, 2>([&](auto I) AI { dma16g<NT_P, I.v>(mk_srd(vv, S_ * D_ * 2), vr, so + I.v * 7168, L_PV + gw * 2048); });
      dma16m(mk_srd(kk, top ? 0 : S_ * D_ * 2), vr, top ? 0 : so - 8192, L_PK + gw * 2048, ~0ull << 40);
      dma16m(mk_srd(sk, D_ * 8), l * 16, ct * 512, L_SK, 0xffffffffull);
      load_tag();
      bar();
      waitv<0>();
      get_tag();
      STAMP2(40);
      const int cpl = l & 31, hh = l >> 5;
      const u32x4 sv = lds_ld<u32x4>(L_SK + cpl * 16);
      u32 a = L_PK + (gw * 16 + hh * 8) * 128 + cpl * 4;
      asm volatile("" : "+v"(a));
      u32 kr[11], vq[8];
#pragma unroll
      for (int i = 0; i < 11; ++i) kr[i] = lds_ld<u32>(a + 5 * 128 + i * 128);
#pragma unroll
      for (int i = 0; i < 8; ++i) vq[i] = lds_ld<u32>(a + (L_PV - L_PK) + i * 128);
      SB();
      const f32x2 kT[4] = {f32x2{bflo(sv.x), bflo(sv.z)}, f32x2{bfhi(sv.x), bfhi(sv.z)}, f32x2{bflo(sv.y), bflo(sv.w)}, f32x2{bfhi(sv.y), bfhi(sv.w)}};
      f32x2 kK[11];
#pragma unroll
      for (int i = 0; i < 3; ++i) kK[i] = bf2(kr[i]);
      u32x4 uo[2];
#pragma unroll
      for (int u = 0; u < 2; ++u) {
        f32x2 ka[4];
        EACH4(kK[4 * u + 3 + j] = bf2(kr[4 * u + 3 + j]));
        EACH4(ka[j] = kK[4 * u + j] * kT[3]); EACH4(ka[j] = fma2(kK[4 * u + j + 1], kT[2], ka[j]));
        EACH4(ka[j] = fma2(kK[4 * u + j + 2], kT[1], ka[j])); EACH4(ka[j] = fma2(kK[4 * u + j + 3], kT[0], ka[j]));
        EACH4(ka[j] = rb2(ka[j])); EACH4(ka[j] *= bf2(vq[4 * u + j]));
        uo[u] = u32x4{pk2(ka[3].x, ka[2].x), pk2(ka[1].x, ka[0].x), pk2(ka[3].y, ka[2].y), pk2(ka[1].y, ka[0].y)};
        SB();
      }
#pragma unroll
      for (int u = 0; u < 2; ++u) lds_st<u32x4>(L_PV + gw * 2048 + cpl * 64 + (((hh * 2 + u) ^ ((cpl >> 1) & 3)) << 4), uo[u]);
#pragma unroll
      for (int i = 0; i < 2; ++i) uo[i] = lds_ld<u32x4>(L_PV + gw * 2048 + i * 1024 + (l >> 2) * 64 + (((l & 3) ^ ((l >> 3) & 3)) << 4));
#pragma unroll
      for (int i = 0; i < 2; ++i) st128(uo[i], rs_ws, (l >> 2) * 8192 + (l & 3) * 16, OFF_U + ((ct * 32 + 16 * i) * 512 + rt * 16 + gw * 4) * 16);
      __builtin_amdgcn_s_setprio(APRIO);
      if (!mainwg) { waitv<0>(); arrive_flag(ws, C_U, 4, OFF_FU + (ct * 32 + rt) * FS, tag, l); STAMP2(42); cnt_wait(C_W, 4); __builtin_amdgcn_s_sleep(SPQ); }
    }
    if (mainwg) {
      const int th = gw >> 1, fh = gw & 1;
      f32x4 acc[4][4];
#pragma unroll
      for (int i = 0; i < 16; ++i) acc[i >> 2][i & 3] = f32x4{0, 0, 0, 0};
      const u32 xa = th * 16384 + n16 * 128 + ((g4 ^ (n16 & 7)) << 4);
      const u32 wa = g4 * 4096 + 2048 + (n16 >> 2) * 256 + (((4 * fh + 2 * th) ^ ((n16 >> 2) | ((g4 & 1) << 2))) << 5) + (l & 3) * 8;
      u32 ad[2][6];
#pragma unroll
      for (int h = 0; h < 2; ++h) {
#pragma unroll
        for (int e = 0; e < 4; ++e) ad[h][e] = (wa ^ (e * 32)) + h * 65536;
        ad[h][4] = xa + h * 65536; ad[h][5] = (xa ^ 64) + h * 65536;
        asm volatile("" : "+v"(ad[h][0]), "+v"(ad[h][1]), "+v"(ad[h][2]), "+v"(ad[h][3]), "+v"(ad[h][4]), "+v"(ad[h][5]));
      }
      u32x4 xf[2][4], wf[2][4];
      f32x4 cf[4], bf[4]; f32x2 rc[4];
      auto rd = [&](int hs, int p) AI {
        const int s = hs >> 1, ks = hs & 1, hi = (s >> 1) & 1, e = p >> 1;
        const u32 so = (s & 1) * SLOT;
        if (hs > 15) {
          if (p < 4) bf[p] = lds_ld<f32x4>(L_CC + fh * 256 + ((p * 64) ^ (th * 128)) + g4 * 16);
          else rc[p - 4] = lds_ld<f32x2>(L_RC + (th * 64 + (p - 4) * 16 + n16) * 8);
        } else if (!(p & 1)) { const u32 a = ad[hi][e] + so + ks * 16384; wf[ks][e] = lds_tr2(a, a + 1024); }
        else xf[ks][e] = lds_ld<u32x4>(ad[hi][4 + ks] + so + e * 4096);
      };
      u32 fullv;
      u32x4 ones = {0x3f803f80u, 0x3f803f80u, 0x3f803f80u, 0x3f803f80u};
      asm volatile("" : "+v"(ones));
#pragma unroll
      for (int i = 0; i < 4; ++i) cf[i] = f32x4{0, 0, 0, 0};
      auto half = [&](int hs) AI {
        const int ks = hs & 1;
        sfor<0, 16>([&](auto J) AI {
          constexpr int j = J.v, o = j < 4 ? (j & 1) * 4 + (j >> 1) : j < 9 ? (j == 4 ? 8 : j == 5 ? 9 : j == 6 ? 2 : j == 7 ? 6 : 10) : (j < 12 ? 12 + (j - 9) : (j - 12) * 4 + 3);
          acc[o >> 2][o & 3] = mfma(wf[ks][o >> 2], xf[ks][o & 3], acc[o >> 2][o & 3]);
          SB();
          if (j < 12 && j % 3 != 1) rd(hs + 1, (j / 3) * 2 + (j % 3) / 2);
          if (ks == 0 && j == 12) cnt_add(C_FREE + 2 * hs);
          if (ks == 0 && j == 13 && hs < 14) fullv = cnt_ld(C_FULL + 2 * hs + 4);
          SB();
        });
        cf[0] = mfma(wf[ks][0], ones, cf[0]); cf[1] = mfma(wf[ks][1], ones, cf[1]);
        SB();
      };
      STAMP2(34);
      cnt_wait(C_FULL, 4);
      STAMP2(35);
#pragma unroll
      for (int p = 0; p < 8; ++p) rd(0, p);
      SB();
      sfor<0, 8>([&](auto S) AI {
        constexpr int s = S.v;
        half(2 * s);
        if (s < 7 && (u32)__builtin_amdgcn_readfirstlane(fullv) < 4) cnt_wait(C_FULL + 4 * s + 4, 4);
        CFENCE(); SB();
        if (s == UDR) { waitv<0>(); arrive_flag(ws, C_U, 4, OFF_FU + (ct * 32 + rt) * FS, tag, l); }
        STAMP2(16 + s);
        half(2 * s + 1);
      });
      STAMP(2);
      __builtin_amdgcn_s_setprio(2);
      if (n16 == 0) { lds_st<f32x4>(L_CSX + gw * 128 + g4 * 16, cf[0]); lds_st<f32x4>(L_CSX + gw * 128 + 64 + g4 * 16, cf[1]); }
      cnt_add(C_SA + fh * 4);
      const float c1 = -2.f * 0.7978845608028654f * 1.4426950408889634f, c2 = c1 * 0.044715f;
      const u32 hso = OFF_H + ((rp * 8 + th * 4) * 56 + cc * 4 + fh * 2) * 1024;
#pragma unroll
      for (int u = 0; u < 8; ++u) {
        const int tt = u & 3, kp = u >> 2;
        if (u == 3) { cnt_wait(C_SA + fh * 4, 2); cf[2] = lds_ld<f32x4>(L_CSX + (gw ^ 2) * 128 + g4 * 16); cf[3] = lds_ld<f32x4>(L_CSX + (gw ^ 2) * 128 + 64 + g4 * 16); SB(); }
        f32x2 x[4], p[4]; u32 hp[4];
        EACH4(x[j] = fma2(HALF(cf[kp * 2 + (j >> 1)], j), sp2(rc[tt].y), HALF(bf[kp * 2 + (j >> 1)], j)));
        EACH4(x[j] = fma2(HALF(acc[kp * 2 + (j >> 1)][tt], j), sp2(rc[tt].x), x[j]));
        EACH4(p[j] = x[j] * x[j]);
        EACH4(p[j] = fma2(p[j], sp2(c2), sp2(c1)));
        EACH4(p[j] = p[j] * x[j]);
        EACH4(p[j] = exp2x2(p[j]));
        EACH4(p[j] = p[j] + sp2(1.f));
        EACH4(p[j] = rcpx2(p[j]));
        EACH4(p[j] = p[j] * x[j]);
        EACH4(hp[j] = pk2(p[j].x, p[j].y));
        st128(u32x4{hp[0], hp[1], hp[2], hp[3]}, rs_ws, l * 16, hso + tt * 56 * 1024 + ((kp * 1024) ^ (th * 1024)));
        SB();
      }
      cnt_add(C_S2);
      STAMP(4);
    }
    if (!mainwg) __builtin_amdgcn_s_sleep(SPQ);
    poll_flags(ws, fw_off, tag);
    waitv<0>();
    if (mainwg) arrive_flag(ws, C_H, 4, OFF_FH + (rp * 16 + cc) * FS, tag, l);
    __builtin_amdgcn_s_setprio(0);
    STAMP(5);
#pragma unroll
    for (int j = 0; j < 28; ++j) load_w2(j);
    STAMP(6);
    if (!mainwg) cnt_wait(C_S3, 4);
    poll_flags(ws, fh_off, tag);
    STAMP(7);
  }
  u32x4 hf[28];
  auto load_h = [&](int n) AI { if (n < 28) { hf[n] = ld128(rs_ws, l * 16, OFF_H + ((rt * 4 + n / 7) * 56 + 7 * w + n % 7) * 1024); SB(); } };
  u32 qv[4];
  const u32 b2v = *(const GAS u32*)(b2 + ct * 64 + (tid & 31) * 2), stf = __builtin_amdgcn_raw_buffer_load_b32(rs_ws, l * 16, OFF_ST + rt * 1024, 16);
#pragma unroll
  for (int e = 0; e < 4; ++e) qv[e] = *(const GAS u32*)(q + (rt * 64 + e * 16 + (tid >> 5)) * D_ + ct * 64 + (tid & 31) * 2);
  SB();
#pragma unroll
  for (int n = 0; n < HLA; ++n) load_h(n);
  if (w >= 4 || mainwg) cnt_wait(w < 4 ? C_FREE + 28 : C_Y, 4);
  STAMP(8);
  const int tk = tid >> 5, cq = tid & 31;
  const u32 fy_off = OFF_FY + (ct * 32 + 4 * w + (l & 3)) * FS;
  u64 fyv; u32x4 yl;
  f32x2 xn[4];
#pragma unroll
  for (int e = 0; e < 4; ++e) {
    const u32 sb = __builtin_bit_cast(u32, bperm((e * 16 + tk) * 4, __builtin_bit_cast(float, stf)));
    xn[e] = rb2(rb2(bf2(qv[e]) - sp2(bflo(sb))) * sp2(__builtin_amdgcn_rcpf(bfhi(sb))));
    asm volatile("" : "+v"(xn[e]));
  }
  SB();
  f32x4 a2[2][4];
  const u32 wa2 = (w < 4 ? 65536 : 0) + gw * 4096 + n16 * 256;
  auto pwrite = [&](int t, int nt) AI { lds_st<f32x4>(t * 16384 + wa2 + (((4 * nt + g4) ^ (n16 & 7)) << 4), a2[t & 1][nt]); SB(); };
#pragma unroll
  for (int t = 0; t < 4; ++t) {
#pragma unroll
    for (int i = 0; i < 7; ++i) {
#pragma unroll
      for (int nt = 0; nt < 4; ++nt) a2[t & 1][nt] = mfma(w2f[i * 4 + nt], hf[t * 7 + i], i ? a2[t & 1][nt] : f32x4{0, 0, 0, 0});
      SB();
      if (t > 0 && i < 4) pwrite(t - 1, i);
      if (t > 0 && i == 4) cnt_add(C_T + 4 * (t - 1));
      load_h(t * 7 + i + HLA);
      if (t * 7 + i + HLA == 28) { fyv = flag_ld(ws, fy_off); SB(); }
      if (t == 3 && i == 0) {
        STAMP2(32);
        flag_chk(fyv, ws, fy_off, tag);
        STAMP2(33);
        yl = ld128(rs_ws, tid * 16, OFF_Y + rt * 65536 + ct * 8192);
        SB();
      }
    }
    STAMP(9 + t);
  }
#pragma unroll
  for (int nt = 0; nt < 4; ++nt) pwrite(3, nt);
  cnt_wait(C_Y, 4);
#pragma unroll
  for (int e = 0; e < 4; ++e) lds_st<u32>(L_YS + ((tid >> 4) * 65 + (tid & 15) * 4 + e) * 4, yl[e]);
  cnt_add(C_T + 12);
  {
    u32 ra0 = tk * 256 + (((cq >> 1) ^ (tk & 7)) << 4) + (cq & 1) * 8, ra1 = ra0 + 65536;
    asm volatile("" : "+v"(ra1));
    f32x2 g[4];
    auto gate = [&](int e0, int n) AI {
      f32x2 pr[3][8], z[3];
#pragma unroll
      for (int e = 0; e < n; ++e)
#pragma unroll
        for (int i = 0; i < 8; ++i) pr[e][i] = *(const volatile LAS f32x2*)((i < 4 ? ra0 : ra1) + (e0 + e) * 16384 + (i & 3) * 4096);
      SB();
#pragma unroll
      for (int e = 0; e < n; ++e) z[e] = (((pr[e][0] + pr[e][1]) + (pr[e][2] + pr[e][3])) + ((pr[e][4] + pr[e][5]) + (pr[e][6] + pr[e][7]))) + bf2(b2v);
#pragma unroll
      for (int e = 0; e < n; ++e) { g[e0 + e] = z[e] * rcpx2(exp2x2(z[e] * sp2(-1.4426950408889634f)) + sp2(1.f)); asm volatile("" : "+v"(g[e0 + e])); }
      SB();
    };
    cnt_wait(C_T + 8, 8);
    gate(0, 3);
    STAMP2(36);
    cnt_wait(C_T + 12, 8);
    STAMP(13);
    u32 yv[4];
#pragma unroll
    for (int e = 0; e < 4; ++e) yv[e] = lds_ld<u32>(L_YS + (cq * 65 + e * 16 + tk) * 4);
    gate(3, 1);
    EACH4(xn[j] = rb2(xn[j] * bf2(yv[j])));
    EACH4(g[j] = fma2(xn[j], g[j], bf2(qv[j])));
    u16* op = out + (rt * 64 + tk) * D_ + ct * 64 + cq * 2;
    EACH4(*(GAS u32*)(op + j * 16 * D_) = pk2(g[j].x, g[j].y));
  }
  if (b == 0 && w == 0) {
#pragma unroll 1
    for (int i = 0; i < 4; ++i) poll_flags(ws, OFF_FY + (i * 64 + l) * FS, tag);
    if (l == 0) set_flag(ws, OFF_E, tag);
  }
  STAMP(14);
  STAMP_END;
}

extern "C" void kernel_launch(void* const* d_in, const int* in_sizes, int n_in, void* d_out, int out_size, void* d_ws, size_t ws_size,
                              hipStream_t stream) {
  char* ws = (char*)d_ws + (WS_TAIL ? ((ws_size - WS_BYTES) & ~(size_t)4095) : 0);
  fused_kernel<<<256, 512, 0, stream>>>((const u16*)d_in[0], (const u16*)d_in[1], (const u16*)d_in[2], (const u16*)d_in[3], (const u16*)d_in[4],
                                        (const u16*)d_in[5], (const u16*)d_in[7], (const u16*)d_in[8], (const u16*)d_in[9], (const u16*)d_in[10],
                                        (u16*)d_out, ws);
}
```

```cpp
#include <hip/hip_runtime.h>
typedef unsigned short u16;
typedef unsigned u32;
typedef unsigned long long u64;
typedef __bf16 bf16x8 __attribute__((ext_vector_type(8)));
typedef __bf16 bf16x2 __attribute__((ext_vector_type(2)));
typedef short s16x4 __attribute__((ext_vector_type(4)));
typedef float f32x4 __attribute__((ext_vector_type(4)));
typedef float f32x2 __attribute__((ext_vector_type(2)));
typedef unsigned u32x4 __attribute__((ext_vector_type(4)));
typedef unsigned u32x2 __attribute__((ext_vector_type(2)));
#define LAS __attribute__((address_space(3)))
#define GAS __attribute__((address_space(1)))
#define DI __device__ __forceinline__
#pragma clang diagnostic ignored "-Wint-to-pointer-cast"

#define FSTR 256
constexpr int S_ = 2048, D_ = 512, F_ = 1792;
constexpr u32 OFF_E = 0, OFF_CS = 16384, OFF_ST = 32768, FS = FSTR  , OFF_FU = 65536, OFF_FW = OFF_FU + 256 * FS, OFF_FH = OFF_FW + 256 * FS, OFF_FY = OFF_FH + 256 * FS;
constexpr u32 OFF_U = 1u << 20, OFF_Y = 3u << 20, OFF_W2T = 5u << 20, OFF_H = 7u << 20, WS_BYTES = 14u << 20;
constexpr u32 SLOT = 32768, L_PK = 131072, L_PV = 140288, L_PW = 148480, L_SK = 156928, L_CNT = 157696,
              L_RC = 157824  , L_CC = 158848  , L_CSX = 159872,
              L_HC0 = 131072, L_HC1 = 0, HCS = 4160, L_UR = 147712, URS = 4576, L_CP = 32768, L_YS = 131072, L_QS = 147712;
constexpr u32 C_FULL = L_CNT  , C_FREE = L_CNT + 48  , C_H = L_CNT + 80, C_W = L_CNT + 84, C_U = L_CNT + 88, C_Y = L_CNT + 92,
              C_S1 = L_CNT + 96, C_S2 = L_CNT + 100, C_S3 = L_CNT + 104, C_SA = L_CNT + 36  , C_T = L_CNT + 112  , L_B1 = 157440;
#define PSLEEP 8
#define NT_G 0
#define NT_P 1
#define WS_TAIL 0
#define BPRIO 1
#define NWB 28
#define WDR 1
#define SPQ 32
#define KVD -1
#define KROT 0
#define DEEP 0
#define PREPS 6
#define LPRIO 2
#define WGO 5
#define PFIRST 0
#define UDR 2
#define LFIRST 0
#define APRIO 1
#define CPS 5
#define HL0 5
#define YDR 0
#define NOPK 0
#define HLA 12

#define STAMP(i)
#define STAMP_INIT
#define STAMP_END
#define STAMP2(i)

template <int V> struct IC { static constexpr int v = V; };
template <int I, int N, class F> DI void sfor(F&& f) { if constexpr (I < N) { f(IC<I>{}); sfor<I + 1, N>(f); } }
DI u32x4 mk_srd(const void* p, u32 bytes) { u64 a = (u64)p; return u32x4{(u32)a, (u32)(a >> 32) & 0xffffu, bytes, 0x00020000u}; }
template <int NT> DI void dma16(u32x4 srd, u32 voff, u32 soff, u32 lds) {
  if (NT) asm volatile("s_mov_b32 m0, %3\n\ts_nop 0\n\tbuffer_load_dwordx4 %0, %1, %2 offen nt lds" ::"v"(voff), "s"(srd), "s"(soff), "s"(lds) : "memory");
  else asm volatile("s_mov_b32 m0, %3\n\ts_nop 0\n\tbuffer_load_dwordx4 %0, %1, %2 offen lds" ::"v"(voff), "s"(srd), "s"(soff), "s"(lds) : "memory");
}
template <int NT, int I> DI void dma16g(u32x4 srd, u32 voff, u32 soff, u32 lds) {
  if (I == 0) dma16<NT>(srd, voff, soff, lds);
  else if (NT) asm volatile("buffer_load_dwordx4 %0, %1, %2 offen offset:%3 nt lds" ::"v"(voff), "s"(srd), "s"(soff), "i"(I * 1024) : "memory");
  else asm volatile("buffer_load_dwordx4 %0, %1, %2 offen offset:%3 lds" ::"v"(voff), "s"(srd), "s"(soff), "i"(I * 1024) : "memory");
}
DI void dma16m(u32x4 srd, u32 voff, u32 soff, u32 lds, u64 mask) {
  asm volatile("s_mov_b32 m0, %3\n\ts_mov_b64 exec, %4\n\tbuffer_load_dwordx4 %0, %1, %2 offen lds\n\ts_mov_b64 exec, -1"
               ::"v"(voff), "s"(srd), "s"(soff), "s"(lds), "s"(mask) : "memory");
}
template <int N> DI void waitv() { asm volatile("s_waitcnt vmcnt(%0)" ::"i"(N) : "memory"); }
#define WAITL0() asm volatile("s_waitcnt lgkmcnt(0)" ::: "memory")
#define CFENCE() asm volatile("" ::: "memory")
#define SB() __builtin_amdgcn_sched_barrier(0)
DI void bar() { SB(); CFENCE(); __builtin_amdgcn_s_waitcnt(0xC07F); __builtin_amdgcn_s_barrier(); CFENCE(); SB(); }
DI float bflo(u32 x) { return __uint_as_float(x << 16); }
DI float bfhi(u32 x) { return __uint_as_float(x & 0xffff0000u); }
DI u32 pk2(float a, float b) { f32x2 p = {a, b}; return __builtin_bit_cast(u32, __builtin_convertvector(p, bf16x2)); }
DI float rb(float a) { return bflo(pk2(a, a)); }
DI f32x2 bf2(u32 x) { return f32x2{bflo(x), bfhi(x)}; }
DI f32x2 rb2(f32x2 v) { return bf2(pk2(v.x, v.y)); }
DI f32x2 sp2(float v) { return f32x2{v, v}; }
DI f32x2 exp2x2(f32x2 v) { return f32x2{__builtin_amdgcn_exp2f(v.x), __builtin_amdgcn_exp2f(v.y)}; }
DI f32x2 rcpx2(f32x2 v) { return f32x2{__builtin_amdgcn_rcpf(v.x), __builtin_amdgcn_rcpf(v.y)}; }
DI f32x2 fma2(f32x2 a, f32x2 b, f32x2 c) { return __builtin_elementwise_fma(a, b, c); }
DI float sfma(float a, float b, float c) { return __builtin_fmaf(a, b, c); }
DI float smul(float a, float b) { return a * b; }
DI float sadd(float a, float b) { return a + b; }
DI f32x2 fma2s(f32x2 a, f32x2 b, f32x2 c) { return fma2(a, b, c); }
DI f32x2 mul2s(f32x2 a, f32x2 b) { return a * b; }
DI f32x2 add2s(f32x2 a, f32x2 b) { return a + b; }
template <class T> DI T lds_ld(u32 off) { return *(const LAS T*)(off); }
template <class T> DI void lds_st(u32 off, T v) { *(LAS T*)(off) = v; }
DI u32x2 lds_tr(u32 off) { return __builtin_bit_cast(u32x2, __builtin_amdgcn_ds_read_tr16_b64_v4i16((LAS s16x4*)(off))); }
DI u32x4 lds_tr2(u32 o0, u32 o1) { u32x2 a = lds_tr(o0), b = lds_tr(o1); return u32x4{a.x, a.y, b.x, b.y}; }
DI u32x4 lds_2x64(u32 o) { u32x2 a = *(const volatile LAS u32x2*)(o), b = *(const volatile LAS u32x2*)(o + 8); return u32x4{a.x, a.y, b.x, b.y}; }
DI f32x4 mfma(u32x4 a, u32x4 b, f32x4 c) {
  return __builtin_amdgcn_mfma_f32_16x16x32_bf16(__builtin_bit_cast(bf16x8, a), __builtin_bit_cast(bf16x8, b), c, 0, 0, 0);
}
DI void st128(u32x4 v, __amdgpu_buffer_rsrc_t rs, u32 voff, u32 soff) {
  __builtin_amdgcn_raw_buffer_store_b128(v, rs, voff, soff, 16);
  asm volatile("s_nop 1" ::"v"(v) : "memory");
}
DI u32x4 ld128(__amdgpu_buffer_rsrc_t rs, u32 voff, u32 soff) { return __builtin_amdgcn_raw_buffer_load_b128(rs, voff, soff, 16); }
template <int C> DI float dpp_add(float v) {
  return v + __builtin_bit_cast(float, __builtin_amdgcn_update_dpp(0, __builtin_bit_cast(int, v), C, 0xf, 0xf, true));
}
DI float bperm(int src_lane4, float v) { return __builtin_bit_cast(float, __builtin_amdgcn_ds_bpermute(src_lane4, __builtin_bit_cast(int, v))); }
DI float sel(bool c, float a, float b) { return c ? a : b; }
DI float rl(float v, int i) { return __builtin_bit_cast(float, __builtin_amdgcn_readlane(__builtin_bit_cast(int, v), i)); }
DI float wave_sum(float v) {
  v = dpp_add<0xB1>(v); v = dpp_add<0x4E>(v); v = dpp_add<0x141>(v); v = dpp_add<0x140>(v);
  return (rl(v, 0) + rl(v, 16)) + (rl(v, 32) + rl(v, 48));
}
constexpr int SPIN_CAP = 1 << 16;
DI u64 flag_ld(const char* ws, u32 off) { return __hip_atomic_load((const GAS u64*)(ws + off), __ATOMIC_RELAXED, __HIP_MEMORY_SCOPE_AGENT); }
DI void poll_flags(const char* ws, u32 off, u64 tag) {
#pragma unroll 1
  for (int it = 0; it < SPIN_CAP; ++it) {
    if (__all(flag_ld(ws, off) == tag)) break;
    __builtin_amdgcn_s_sleep(PSLEEP);
  }
  CFENCE();
}
DI void flag_chk(u64 v, const char* ws, u32 off, u64 tag) { if (!__all(v == tag)) poll_flags(ws, off, tag); CFENCE(); }
DI void set_flag(char* ws, u32 off, u64 tag) { __hip_atomic_store((GAS u64*)(ws + off), tag, __ATOMIC_RELAXED, __HIP_MEMORY_SCOPE_AGENT); }
DI void arrive_flag(char* ws, u32 cnt, u32 n, u32 off, u64 tag, int l) {
  if (l == 0) {
    u32 old;
    asm volatile("ds_add_rtn_u32 %0, %1, %2\n\ts_waitcnt lgkmcnt(0)" : "=&v"(old) : "v"(cnt), "v"(1u) : "memory");
    if (old == n - 1) set_flag(ws, off, tag);
  }
  CFENCE();
}
DI void cnt_add(u32 cnt) { asm volatile("s_mov_b64 exec, 1\n\tds_add_u32 %0, %1\n\ts_mov_b64 exec, -1" ::"v"(cnt), "v"(1u) : "memory"); }
DI u32 cnt_ld(u32 cnt) { return *(const volatile LAS u32*)cnt; }
DI void cnt_wait(u32 cnt, u32 n) {
#pragma unroll 1
  for (int it = 0; it < (1 << 22); ++it) { if ((u32)__builtin_amdgcn_readfirstlane(cnt_ld(cnt)) >= n) break; __builtin_amdgcn_s_sleep(1); }
  CFENCE(); SB();
}
DI void group_sync(u32 cnt) { SB(); WAITL0(); cnt_add(cnt); cnt_wait(cnt, 4); }
#define AI __attribute__((always_inline))
#define EACH4(stmt) do { _Pragma("unroll") for (int j = 0; j < 4; ++j) { stmt; } SB(); } while (0)
#define HALF(v, j) (((j) & 1) ? (v).hi : (v).lo)

#define KATTR
__global__ __launch_bounds__(512, 2) KATTR void fused_kernel(const u16* __restrict__ q, const u16* __restrict__ kk, const u16* __restrict__ vv,
                                                       const u16* __restrict__ sk, const u16* __restrict__ ssk, const u16* __restrict__ sD,
                                                       const u16* __restrict__ w1, const u16* __restrict__ b1, const u16* __restrict__ w2,
                                                       const u16* __restrict__ b2, u16* __restrict__ out, char* __restrict__ ws) {
  __shared__ __attribute__((aligned(16))) char sm[163840];
  asm volatile("" ::"v"(sm) : "memory");
  asm volatile("" : "+s"(q), "+s"(kk), "+s"(vv), "+s"(sk), "+s"(ssk), "+s"(sD), "+s"(w1), "+s"(b1), "+s"(w2), "+s"(b2), "+s"(out), "+s"(ws));
  const int tid = threadIdx.x, l = tid & 63;
  const int w = __builtin_amdgcn_readfirstlane(tid >> 6);
  const int b = blockIdx.x, xcd = b & 7, r = b >> 3;
  const bool mainwg = r < 28;
  const int rp = (xcd & 3) * 4 + (r & 3), cc = (xcd >> 2) * 7 + (r >> 2);
  const int rt = (xcd & 3) * 8 + (xcd >> 2) * 4 + (r & 3), ct = r >> 2;
  const int gw = w & 3;
  const int n16 = l & 15, g4 = l >> 4;
  STAMP_INIT
  STAMP(0);
  if (tid < 8) lds_st<u32x4>(L_CNT + tid * 16, u32x4{0, 0, 0, 0});
  const u32x4 srd_q = mk_srd(q, S_ * D_ * 2);
  const u32x4 srd_w1m = mk_srd((const char*)w1 - F_ * 2, D_ * F_ * 2 + F_ * 2);
  u32x2 ev;
  u64 tag;
  auto load_tag = [&]() AI { asm volatile("s_mov_b64 exec, 1\n\tglobal_load_dwordx2 %0, %1, %2 sc1\n\ts_mov_b64 exec, -1" : "=v"(ev) : "v"(0u), "s"(ws) : "memory"); };
  auto get_tag = [&]() AI {
    SB();
    asm volatile("" : "+v"(ev));
    tag = (((u64)__builtin_amdgcn_readfirstlane(ev.y) << 32) | (u32)__builtin_amdgcn_readfirstlane(ev.x)) + 0x9E3779B97F4A7C15ull;
  };
  const __amdgpu_buffer_rsrc_t rs_ws = __builtin_amdgcn_make_buffer_rsrc(ws, 0, WS_BYTES, 0x00020000);
  const u32 vr = (l >> 3) * 1024 + (l & 7) * 16;
  u32x4 w2f[28];
  auto load_w2 = [&](int j) AI { w2f[j] = ld128(rs_ws, l * 16 + (j & 3) * 1024, OFF_W2T + (ct * 56 + 7 * w + (j >> 2)) * 4096); SB(); };
  const u32 fw_off = OFF_FW + (4 * w + (ct >> 1)) * FS;
  const u32 fh_off = OFF_FH + ((rt >> 1) * 16 + min(((7 * w) >> 2) + (l & 3), (7 * w + 6) >> 2)) * FS;

  const u32 sx32 = ((2 * g4 + (n16 >> 3)) & 3) << 5, tr0 = (4 * g4 + (n16 >> 2)) * 128 + (l & 3) * 8, vsw = ((l & 7) ^ ((l >> 3) & 6)) << 4;
  const int sp = (r - 28) * 8 + xcd;
  if (w >= 4) {
    const int t2 = tid & 255;
    u32x4 ha[2]; u32x2 hn[2];
    auto load_taps = [&]() AI {
      asm volatile("buffer_load_dwordx4 %0, %4, %5, %6 offen nt\n\tbuffer_load_dwordx4 %1, %4, %5, %6 offen offset:1024 nt\n\t"
                   "buffer_load_dwordx2 %2, %4, %5, %6 offen offset:16\n\tbuffer_load_dwordx2 %3, %4, %5, %6 offen offset:1040"
                   : "=&v"(ha[0]), "=&v"(ha[1]), "=&v"(hn[0]), "=&v"(hn[1]) : "v"(l * 16), "s"(mk_srd(ssk, S_ * D_ * 2)), "s"(b * 8192 + gw * 2048) : "memory");
    };
    __builtin_amdgcn_s_setprio(LPRIO);
    const u32 fu_off = (l & 8) ? fw_off : OFF_FU + ((b >> 5) * 32 + 8 * gw + (l & 7)) * FS;
    u64 fuv = 0;
    load_tag();
    const u32 vx0 = (l >> 3) * 1024 + (((l & 7) ^ (l >> 3)) << 4), vx1 = vx0 + 8192 - 1024;
    const u32 vwb = (g4 + 1) * (F_ * 2) - 2048, vws = (n16 ^ (g4 << 1)) << 4;
    const u32 vw[4] = {vwb + vws, vwb + vws + 4 * (F_ * 2) - 1024, vwb + (vws ^ 128), vwb + (vws ^ 128) + 4 * (F_ * 2) - 1024};
    auto stage = [&](int s) AI {
      const u32 sx = rp * 131072 + (gw & 1) * 65536 + s * 128, sw = ((gw & 1) * 32 + s * 64) * (F_ * 2) + cc * 256, dst = (s & 3) * SLOT + (gw & 1) * 16384;
#pragma unroll
      for (int i = 0; i < 4; ++i) {
        dma16g<NT_G, 0>(srd_q, vx0, sx + i * 16384, dst + i * 4096); dma16g<NT_G, 1>(srd_q, vx1, sx + i * 16384, 0);
        dma16g<NT_G, 2>(srd_w1m, vw[(i & 1) * 2], sw + i * 8 * (F_ * 2), 0); dma16g<NT_G, 3>(srd_w1m, vw[(i & 1) * 2 + 1], sw + i * 8 * (F_ * 2), 0);
      }
    };
    if (!LFIRST) bar();
    if (mainwg) { stage(gw >> 1); if (LFIRST) bar(); stage((gw >> 1) + 2); waitv<16>(); cnt_add(C_FULL + 4 * (gw >> 1)); }
    else {
#pragma unroll
      for (int p = 0; p < 4; ++p) {
        const int hp = gw * 4 + p;
        sfor<0, 4>([&](auto I) AI { dma16g<NT_P, I.v>(mk_srd(w2, hp < 14 ? D_ * F_ * 2 : 0), (l >> 3) * 1024 + vsw,
                                                     ((sp >> 2) * 7 + (hp >> 1)) * 32768 + (sp & 3) * 256 + (hp & 1) * 128 + I.v * 7168, hp * 4096); });
      }
      if (LFIRST) bar();
    }
    waitv<16>();
    get_tag();
    STAMP2(40);
    STAMP2(43);
    if (mainwg) {
      const int s0 = gw >> 1;
      waitv<0>(); cnt_add(C_FULL + 4 * s0 + 8);
      cnt_wait(C_FREE + 4 * s0, 4);
      stage(s0 + 4);
      waitv<0>(); cnt_add(C_FULL + 4 * s0 + 16);
      u32x4 g;
      asm volatile("buffer_load_dwordx4 %0, %1, %2, %3 offen sc1" : "=v"(g) : "v"(l * 16), "s"(mk_srd(ws, WS_BYTES)), "s"(OFF_ST + rp * 2048 + (gw & 1) * 1024) : "memory");
      dma16m(mk_srd(b1, F_ * 2), l * 16, cc * 256, L_B1, 0xffffull);
      cnt_wait(C_FREE + 4 * s0 + 8, 4);
      stage(s0 + 6);
      load_taps();
      waitv<20>();
      STAMP2(41);
      SB();
      asm volatile("" : "+v"(g));
      if (gw < 2) {
        auto g_ok = [&]() AI { return __all((g.y == (u32)tag) & (g.w == (u32)(tag >> 32))); };
        if (!g_ok()) {
#pragma unroll 1
          for (int it = 0; it < SPIN_CAP; ++it) { g = ld128(rs_ws, l * 16, OFF_ST + rp * 2048 + gw * 1024); if (g_ok()) break; __builtin_amdgcn_s_sleep(4); CFENCE(); }
        }
        const float i2 = __builtin_amdgcn_rcpf(bfhi(g.z));
        float ra = __builtin_amdgcn_rcpf(bfhi(g.x)) * i2;
        asm volatile("" : "+v"(ra));
        lds_st<f32x2>(L_RC + (gw * 64 + l) * 8, f32x2{ra, -bflo(g.x) * ra - bflo(g.z) * i2});
      } else lds_st<u32>(L_CC + ((gw & 1) * 64 + l) * 4, (u32)lds_ld<u16>(L_B1 + ((gw & 1) * 64 + l) * 2) << 16);
      fuv = flag_ld(ws, fu_off);
      waitv<5>();
      cnt_add(C_FULL + 4 * s0 + 24);
      STAMP2(42);
    } else {
      waitv<0>();
      SB();
#pragma unroll
      for (int p = 0; p < 4; ++p) {
        const int hp = gw * 4 + p;
        u32x4 wt[4];
#pragma unroll
        for (int nt = 0; nt < 4; ++nt) { const u32 a = hp * 4096 + tr0 + ((nt * 32) ^ sx32); wt[nt] = lds_tr2(a, a + 2048); }
#pragma unroll
        for (int nt = 0; nt < 4; ++nt)
          st128(wt[nt], rs_ws, l * 16, hp < 14 ? OFF_W2T + ((((sp & 3) * 2 + (hp & 1)) * 56 + (sp >> 2) * 7 + (hp >> 1)) * 4 + nt) * 1024 : 0x7f000000u);
      }
      STAMP2(41);
      waitv<0>();
      arrive_flag(ws, C_W, 4, OFF_FW + sp * FS, tag, l);
      STAMP2(42);
      load_taps();
      cnt_wait(C_W, 4);
      __builtin_amdgcn_s_sleep(SPQ);
    }
    STAMP(2);
    if (!mainwg) { cnt_wait(C_U, 4); }
    __builtin_amdgcn_s_setprio(0);
    waitv<0>();
    flag_chk(fuv, ws, fu_off, tag);
    STAMP(3);
    u32x4 uu[2];
#pragma unroll
    for (int i = 0; i < 2; ++i) uu[i] = ld128(rs_ws, l * 16 + i * 1024, OFF_U + b * 8192 + gw * 2048);
    SB();
#pragma unroll
    for (int j = 0; j < NWB; ++j) load_w2(j);
    STAMP2(30);
    if (mainwg) cnt_wait(C_FREE + 4 * CPS, 4);
    asm volatile("" : "+v"(ha[0]), "+v"(ha[1]), "+v"(hn[0]), "+v"(hn[1]));
#pragma unroll
    for (int i = 0; i < 2; ++i) {
      const u32x4 x = ha[i];
      const u32 o0 = __builtin_amdgcn_alignbit(x.y, x.x, 16), o1 = __builtin_amdgcn_alignbit(x.z, x.y, 16), o2 = __builtin_amdgcn_alignbit(x.w, x.z, 16),
                o3 = __builtin_amdgcn_alignbit(hn[i].x, x.w, 16), o4 = __builtin_amdgcn_alignbit(hn[i].y, hn[i].x, 16);
      const u32 d = ((gw >> 1) ? L_HC1 : L_HC0) + 32 + (gw & 1) * 2048 + i * 1024 + l * 16;
      lds_st<u32x4>(d, x); lds_st<u32x4>(d + HCS, u32x4{o0, o1, o2, o3});
      lds_st<u32x4>(d + 2 * HCS, u32x4{x.y, x.z, x.w, hn[i].x}); lds_st<u32x4>(d + 3 * HCS, u32x4{o1, o2, o3, o4});
    }
    if (l < 19) {
      const int z = gw * 19 + l;
      lds_st<u32x4>(z < 16 ? ((z & 8) ? L_HC1 : L_HC0) + ((z >> 1) & 3) * HCS + (z & 1) * 16 : L_UR + 4096 + (z - 16) * 16 + (z >= 46 ? URS - 480 : 0), u32x4{0, 0, 0, 0});
    }
#pragma unroll
    for (int i = 0; i < 2; ++i) {
      const u32 a = L_UR + (2044 - 4 * (gw * 128 + i * 64 + l)) * 2;
      lds_st<u32x2>(a, u32x2{uu[i].x, uu[i].y}); lds_st<u32x2>(a + URS, u32x2{uu[i].z, uu[i].w});
    }
    cnt_add(C_S2);
    STAMP(4);
    cnt_wait(C_S2, mainwg && !NOPK ? 8 : 4);
    STAMP(5);
    {
      const int ch = gw & 1, kh = gw >> 1;
      f32x4 ca[8];
#pragma unroll
      for (int i = 0; i < 8; ++i) ca[i] = f32x4{0, 0, 0, 0};
      const u32 ab = (ch ? L_HC1 : L_HC0) + ((n16 + 1) & 3) * HCS + (((n16 + 1) & ~3) + 8 * g4) * 2 + kh * 256;
      const u32 bb = L_UR + ch * URS + (2032 - 1792 - 16 * n16 + 8 * g4) * 2 + kh * 256;
      const u32 pa = L_CP + gw * 8192 + (4 * n16 + (g4 ^ ((n16 >> 1) & 3))) * 16;
      u32x4 AL[2][4], BL[2][4], AH[4], BH[4];
      auto rdp = [&](u32x4& a, u32x4& bq, int k0, int i) AI { SB(); a = lds_2x64(ab + k0 * 64 + i * 512); bq = lds_ld<u32x4>(bb + k0 * 64 + (7 - i) * 512); SB(); };
#pragma unroll
      for (int i = 0; i < 4; ++i) rdp(AL[0][i], BL[0][i], 0, i);
      sfor<0, 4>([&](auto K) AI {
        constexpr int k0 = K.v;
        u32x4 (&A)[4] = AL[k0 & 1]; u32x4 (&B)[4] = BL[k0 & 1];
#pragma unroll
        for (int n = 0; n < 16; ++n) {
          constexpr int oi[16] = {0, 0, 1, 0, 1, 2, 0, 1, 2, 3, 1, 2, 3, 2, 3, 3}, ot[16] = {0, 1, 0, 2, 1, 0, 3, 2, 1, 0, 3, 2, 1, 3, 2, 3};
          ca[oi[n] + ot[n]] = mfma(A[oi[n]], B[ot[n]], ca[oi[n] + ot[n]]);
          if (n < 8 && (n & 1)) rdp(AH[n >> 1], BH[n >> 1], k0, 4 + (n >> 1));
          if (k0 == 3 && n >= 12) { SB(); lds_st<f32x4>(pa + (n - 12) * 1024, ca[n - 12]); SB(); }
        }
        int cn = 0;
#pragma unroll
        for (int T = 0; T < 4; ++T)
#pragma unroll
          for (int i = 0; i <= T; ++i) {
            ca[4 + T] = mfma(A[i], BH[T - i], ca[4 + T]); ca[4 + T] = mfma(AH[T - i], B[i], ca[4 + T]);
            if (k0 < 3 && cn >= 2 && cn < 6) rdp(AL[(k0 + 1) & 1][cn - 2], BL[(k0 + 1) & 1][cn - 2], k0 + 1, cn - 2);
            if (k0 == 3 && T > 0 && i == 1) { SB(); lds_st<f32x4>(pa + (3 + T) * 1024, ca[3 + T]); SB(); }
            ++cn;
          }
        SB();
      });
      lds_st<f32x4>(pa + 7 * 1024, ca[7]);
    }
    cnt_add(C_S3);
    STAMP(6);
#pragma unroll
    for (int j = NWB; j < 28; ++j) load_w2(j);
  } else {
    {
      __builtin_amdgcn_s_setprio(3);
      const u32 so = (rt * 64 + gw * 16) * 1024 + ct * 128;
      const bool top = rt == 0 && gw == 0;
      sfor<0, 2>([&](auto I) AI { dma16g<NT_P, I.v>(mk_srd(kk, S_ * D_ * 2), vr, so + I.v * 7168, L_PK + 1024 + gw * 2048); });
      sfor<0, 2>([&](auto I) AI { dma16g<NT_P, I.v>(mk_srd(vv, S_ * D_ * 2), vr, so + I.v * 7168, L_PV + gw * 2048); });
      dma16m(mk_srd(kk, top ? 0 : S_ * D_ * 2), vr, top ? 0 : so - 8192, L_PK + gw * 2048, ~0ull << 40);
      dma16m(mk_srd(sk, D_ * 8), l * 16, ct * 512, L_SK, 0xffffffffull);
    const int srow = rt * 64 + ct * 8 + gw * 2;
    u32x4 x0[2];
      load_tag();
    asm volatile("buffer_load_dwordx4 %0, %2, %3, %4 offen\n\tbuffer_load_dwordx4 %1, %2, %3, %4 offen offset:1024"
                 : "=&v"(x0[0]), "=&v"(x0[1]) : "v"(l * 16), "s"(srd_q), "s"(srow * 1024) : "memory");
      bar();
      waitv<0>();
      get_tag();
      STAMP2(40);
      const int cpl = l & 31, hh = l >> 5;
      const u32x4 sv = lds_ld<u32x4>(L_SK + cpl * 16);
      u32 a = L_PK + (gw * 16 + hh * 8) * 128 + cpl * 4;
      asm volatile("" : "+v"(a));
      u32 kr[11], vq[8];
#pragma unroll
      for (int i = 0; i < 11; ++i) kr[i] = lds_ld<u32>(a + 5 * 128 + i * 128);
#pragma unroll
      for (int i = 0; i < 8; ++i) vq[i] = lds_ld<u32>(a + (L_PV - L_PK) + i * 128);
      SB();
      const f32x2 kT[4] = {f32x2{bflo(sv.x), bflo(sv.z)}, f32x2{bfhi(sv.x), bfhi(sv.z)}, f32x2{bflo(sv.y), bflo(sv.w)}, f32x2{bfhi(sv.y), bfhi(sv.w)}};
      f32x2 kK[11];
#pragma unroll
      for (int i = 0; i < 3; ++i) kK[i] = bf2(kr[i]);
      u32x4 uo[2];
#pragma unroll
      for (int u = 0; u < 2; ++u) {
        f32x2 ka[4];
        EACH4(kK[4 * u + 3 + j] = bf2(kr[4 * u + 3 + j]));
        EACH4(ka[j] = kK[4 * u + j] * kT[3]); EACH4(ka[j] = fma2(kK[4 * u + j + 1], kT[2], ka[j]));
        EACH4(ka[j] = fma2(kK[4 * u + j + 2], kT[1], ka[j])); EACH4(ka[j] = fma2(kK[4 * u + j + 3], kT[0], ka[j]));
        EACH4(ka[j] = rb2(ka[j])); EACH4(ka[j] *= bf2(vq[4 * u + j]));
        uo[u] = u32x4{pk2(ka[3].x, ka[2].x), pk2(ka[1].x, ka[0].x), pk2(ka[3].y, ka[2].y), pk2(ka[1].y, ka[0].y)};
        SB();
      }
#pragma unroll
      for (int u = 0; u < 2; ++u) lds_st<u32x4>(L_PV + gw * 2048 + cpl * 64 + (((hh * 2 + u) ^ ((cpl >> 1) & 3)) << 4), uo[u]);
#pragma unroll
      for (int i = 0; i < 2; ++i) uo[i] = lds_ld<u32x4>(L_PV + gw * 2048 + i * 1024 + (l >> 2) * 64 + (((l & 3) ^ ((l >> 3) & 3)) << 4));
#pragma unroll
      for (int i = 0; i < 2; ++i) st128(uo[i], rs_ws, (l >> 2) * 8192 + (l & 3) * 16, OFF_U + ((ct * 32 + 16 * i) * 512 + rt * 16 + gw * 4) * 16);
    {
      asm volatile("" : "+v"(x0[0]), "+v"(x0[1]));
      float c0[2], sa[2], sq[2];
#pragma unroll
      for (int i = 0; i < 2; ++i) {
        c0[i] = bflo(__builtin_amdgcn_readfirstlane(x0[i].x));
        const f32x2 d[4] = {bf2(x0[i].x) - sp2(c0[i]), bf2(x0[i].y) - sp2(c0[i]), bf2(x0[i].z) - sp2(c0[i]), bf2(x0[i].w) - sp2(c0[i])};
        const f32x2 s2 = (d[0] + d[1]) + (d[2] + d[3]), q2 = fma2(d[0], d[0], d[1] * d[1]) + fma2(d[2], d[2], d[3] * d[3]);
        sa[i] = s2.x + s2.y; sq[i] = q2.x + q2.y;
      }
#define RED4(C) do { sa[0] = dpp_add<C>(sa[0]); sq[0] = dpp_add<C>(sq[0]); sa[1] = dpp_add<C>(sa[1]); sq[1] = dpp_add<C>(sq[1]); SB(); } while (0)
      SB(); RED4(0xB1); RED4(0x4E); RED4(0x141); RED4(0x140); RED4(0x142); RED4(0x143);
      const bool odd = l & 1;
      const float md = sel(odd, sa[1], sa[0]) * (1.f / D_), mean = sel(odd, c0[1], c0[0]) + md, var = fmaxf(sel(odd, sq[1], sq[0]) * (1.f / D_) - md * md, 0.f);
      const float m1 = rb(mean), sd1 = rb(__builtin_amdgcn_sqrtf(rb(rb(var) + 1e-5f)));
      const float i1 = __builtin_amdgcn_rcpf(sd1);
      const float m2 = (mean - m1) * i1, sd2 = __builtin_amdgcn_sqrtf(rb(rb(var * i1 * i1) + 1e-5f));
      if (l >= 62) st128(u32x4{pk2(m1, sd1), (u32)tag, pk2(m2, sd2), (u32)(tag >> 32)}, rs_ws, (l - 62) * 16, OFF_ST + srow * 16);
    }

      __builtin_amdgcn_s_setprio(APRIO);
      if (!mainwg) { waitv<0>(); arrive_flag(ws, C_U, 4, OFF_FU + (ct * 32 + rt) * FS, tag, l); STAMP2(42); cnt_wait(C_W, 4); __builtin_amdgcn_s_sleep(SPQ); }
    }
    if (mainwg) {
      const int th = gw >> 1, fh = gw & 1;
      f32x4 acc[4][4];
#pragma unroll
      for (int i = 0; i < 16; ++i) acc[i >> 2][i & 3] = f32x4{0, 0, 0, 0};
      const u32 xa = th * 16384 + n16 * 128 + ((g4 ^ (n16 & 7)) << 4);
      const u32 wa = g4 * 4096 + 2048 + (n16 >> 2) * 256 + (((4 * fh + 2 * th) ^ ((n16 >> 2) | ((g4 & 1) << 2))) << 5) + (l & 3) * 8;
      u32 ad[2][6];
#pragma unroll
      for (int h = 0; h < 2; ++h) {
#pragma unroll
        for (int e = 0; e < 4; ++e) ad[h][e] = (wa ^ (e * 32)) + h * 65536;
        ad[h][4] = xa + h * 65536; ad[h][5] = (xa ^ 64) + h * 65536;
        asm volatile("" : "+v"(ad[h][0]), "+v"(ad[h][1]), "+v"(ad[h][2]), "+v"(ad[h][3]), "+v"(ad[h][4]), "+v"(ad[h][5]));
      }
      u32x4 xf[2][4], wf[2][4];
      f32x4 cf[4], bf[4]; f32x2 rc[4];
      auto rd = [&](int hs, int p) AI {
        const int s = hs >> 1, ks = hs & 1, hi = (s >> 1) & 1, e = p >> 1;
        const u32 so = (s & 1) * SLOT;
        if (hs > 15) {
          if (p < 4) bf[p] = lds_ld<f32x4>(L_CC + fh * 256 + ((p * 64) ^ (th * 128)) + g4 * 16);
          else rc[p - 4] = lds_ld<f32x2>(L_RC + (th * 64 + (p - 4) * 16 + n16) * 8);
        } else if (!(p & 1)) { const u32 a = ad[hi][e] + so + ks * 16384; wf[ks][e] = lds_tr2(a, a + 1024); }
        else xf[ks][e] = lds_ld<u32x4>(ad[hi][4 + ks] + so + e * 4096);
      };
      u32 fullv;
      u32x4 ones = {0x3f803f80u, 0x3f803f80u, 0x3f803f80u, 0x3f803f80u};
      asm volatile("" : "+v"(ones));
#pragma unroll
      for (int i = 0; i < 4; ++i) cf[i] = f32x4{0, 0, 0, 0};
      auto half = [&](int hs) AI {
        const int ks = hs & 1;
        sfor<0, 16>([&](auto J) AI {
          constexpr int j = J.v, o = j < 4 ? (j & 1) * 4 + (j >> 1) : j < 9 ? (j == 4 ? 8 : j == 5 ? 9 : j == 6 ? 2 : j == 7 ? 6 : 10) : (j < 12 ? 12 + (j - 9) : (j - 12) * 4 + 3);
          acc[o >> 2][o & 3] = mfma(wf[ks][o >> 2], xf[ks][o & 3], acc[o >> 2][o & 3]);
          SB();
          if (j < 12 && j % 3 != 1) rd(hs + 1, (j / 3) * 2 + (j % 3) / 2);
          if (ks == 0 && j == 12) cnt_add(C_FREE + 2 * hs);
          if (ks == 0 && j == 13 && hs < 14) fullv = cnt_ld(C_FULL + 2 * hs + 4);
          SB();
        });
        cf[0] = mfma(wf[ks][0], ones, cf[0]); cf[1] = mfma(wf[ks][1], ones, cf[1]);
        SB();
      };
      STAMP2(34);
      cnt_wait(C_FULL, 2);
      STAMP2(35);
#pragma unroll
      for (int p = 0; p < 8; ++p) rd(0, p);
      SB();
      sfor<0, 8>([&](auto S) AI {
        constexpr int s = S.v;
        half(2 * s);
        if (s < 7 && (u32)__builtin_amdgcn_readfirstlane(fullv) < 2) cnt_wait(C_FULL + 4 * s + 4, 2);
        CFENCE(); SB();
        if (s == UDR) { waitv<0>(); arrive_flag(ws, C_U, 4, OFF_FU + (ct * 32 + rt) * FS, tag, l); }
        STAMP2(16 + s);
        half(2 * s + 1);
      });
      STAMP(2);
      __builtin_amdgcn_s_setprio(2);
      if (n16 == 0) { lds_st<f32x4>(L_CSX + gw * 128 + g4 * 16, cf[0]); lds_st<f32x4>(L_CSX + gw * 128 + 64 + g4 * 16, cf[1]); }
      cnt_add(C_SA + fh * 4);
      const float c1 = -2.f * 0.7978845608028654f * 1.4426950408889634f, c2 = c1 * 0.044715f;
      const u32 hso = OFF_H + ((rp * 8 + th * 4) * 56 + cc * 4 + fh * 2) * 1024;
#pragma unroll
      for (int u = 0; u < 8; ++u) {
        const int tt = u & 3, kp = u >> 2;
        if (u == 3) { cnt_wait(C_SA + fh * 4, 2); cf[2] = lds_ld<f32x4>(L_CSX + (gw ^ 2) * 128 + g4 * 16); cf[3] = lds_ld<f32x4>(L_CSX + (gw ^ 2) * 128 + 64 + g4 * 16); SB(); }
        f32x2 x[4], p[4]; u32 hp[4];
        EACH4(x[j] = fma2(HALF(cf[kp * 2 + (j >> 1)], j), sp2(rc[tt].y), HALF(bf[kp * 2 + (j >> 1)], j)));
        EACH4(x[j] = fma2(HALF(acc[kp * 2 + (j >> 1)][tt], j), sp2(rc[tt].x), x[j]));
        EACH4(p[j] = x[j] * x[j]);
        EACH4(p[j] = fma2(p[j], sp2(c2), sp2(c1)));
        EACH4(p[j] = p[j] * x[j]);
        EACH4(p[j] = exp2x2(p[j]));
        EACH4(p[j] = p[j] + sp2(1.f));
        EACH4(p[j] = rcpx2(p[j]));
        EACH4(p[j] = p[j] * x[j]);
        EACH4(hp[j] = pk2(p[j].x, p[j].y));
        st128(u32x4{hp[0], hp[1], hp[2], hp[3]}, rs_ws, l * 16, hso + tt * 56 * 1024 + ((kp * 1024) ^ (th * 1024)));
        SB();
      }
      if (!NOPK) cnt_add(C_S2);
      STAMP(4);
    }
    if (!mainwg) __builtin_amdgcn_s_sleep(SPQ);
    poll_flags(ws, fw_off, tag);
    waitv<0>();
    if (mainwg) arrive_flag(ws, C_H, 4, OFF_FH + (rp * 16 + cc) * FS, tag, l);
    __builtin_amdgcn_s_setprio(0);
    STAMP(5);
#pragma unroll
    for (int j = 0; j < 28; ++j) load_w2(j);
    STAMP(6);
    if (!mainwg) cnt_wait(C_S3, 4);
  }
  u32 qv[4];
  const u32 b2v = *(const GAS u32*)(b2 + ct * 64 + (tid & 31) * 2), dd = *(const volatile GAS u32*)(sD + 2 * b);
#pragma unroll
  for (int e = 0; e < 4; ++e) qv[e] = *(const GAS u32*)(q + (rt * 64 + e * 16 + (tid >> 5)) * D_ + ct * 64 + (tid & 31) * 2);
  SB();
  poll_flags(ws, fh_off, tag);
  STAMP(7);
  u32x4 hf[28];
  auto load_h = [&](int n) AI { if (n < 28) { hf[n] = ld128(rs_ws, l * 16, OFF_H + ((rt * 4 + n / 7) * 56 + 7 * w + n % 7) * 1024); SB(); } };
  const u32 stf = __builtin_amdgcn_raw_buffer_load_b32(rs_ws, l * 16, OFF_ST + rt * 1024, 16);
#pragma unroll
  for (int n = 0; n < HL0; ++n) load_h(n);
  cnt_wait(C_S3, 4);
  STAMP2(29);
  {
    const u32x2 hc = lds_ld<u32x2>(L_HC0 + 32), hb = lds_ld<u32x2>(L_HC1 + 32);
    const u32x2 u0 = lds_ld<u32x2>(L_UR + (2044 - 4 * tid) * 2), u1 = lds_ld<u32x2>(L_UR + URS + (2044 - 4 * tid) * 2);
    const u32 pa = L_CP + ((tid & ~3) | ((tid & 3) ^ ((tid >> 3) & 3))) * 16;
    f32x4 pr[4];
#pragma unroll
    for (int p = 0; p < 4; ++p) pr[p] = lds_ld<f32x4>(pa + p * 8192);
    SB();
    const f32x2 d2 = bf2(dd), h0 = {bflo(hc.x), bflo(hb.x)}, h1 = {bfhi(hc.x), bfhi(hb.x)}, h2 = {bflo(hc.y), bflo(hb.y)};
    const f32x2 a[4] = {f32x2{bfhi(u0.y), bfhi(u1.y)}, f32x2{bflo(u0.y), bflo(u1.y)}, f32x2{bfhi(u0.x), bfhi(u1.x)}, f32x2{bflo(u0.x), bflo(u1.x)}};
    const f32x4 y0 = pr[0] + pr[2], y1 = pr[1] + pr[3];
    f32x2 y[4]; u32 o[4];
    EACH4((y[j] = f32x2{y0[j], y1[j]}));
    y[0] = fma2(a[0], h0, y[0]); y[1] = fma2(a[1], h0, y[1]); y[2] = fma2(a[2], h0, y[2]);
    y[1] = fma2(a[0], h1, y[1]); y[2] = fma2(a[1], h1, y[2]); y[2] = fma2(a[0], h2, y[2]);
    EACH4(y[j] = rb2(y[j]) + rb2(a[j] * d2));
    EACH4(o[j] = pk2(y[j].x, y[j].y));
    st128(u32x4{o[0], o[1], o[2], o[3]}, rs_ws, (tid >> 4) * 65536 + (tid & 15) * 16, OFF_Y + b * 256);
  }
  cnt_add(C_S1);
#pragma unroll
  for (int n = HL0; n < HLA; ++n) load_h(n);
  STAMP(8);
  const int tk = tid >> 5, cq = tid & 31;
  const u32 fy_off = OFF_FY + (ct * 32 + 4 * w + (l & 3)) * FS;
  u64 fyv; u32x4 yl;
  f32x2 xn[4];
#pragma unroll
  for (int e = 0; e < 4; ++e) {
    const u32 sb = __builtin_bit_cast(u32, bperm((e * 16 + tk) * 4, __builtin_bit_cast(float, stf)));
    xn[e] = rb2(rb2(bf2(qv[e]) - sp2(bflo(sb))) * sp2(__builtin_amdgcn_rcpf(bfhi(sb))));
    asm volatile("" : "+v"(xn[e]));
  }
  SB();
  u32 ra0 = tk * 256 + (((cq >> 1) ^ (tk & 7)) << 4) + (cq & 1) * 8, ra1 = ra0 + 65536;
  asm volatile("" : "+v"(ra1));
  f32x2 g[4], pr[8], zz;
  auto gate = [&](int e, int part) AI {
    if (part == 0) {
      cnt_wait(C_T + 4 * e, 8);
#pragma unroll
      for (int i = 0; i < 8; ++i) pr[i] = *(const volatile LAS f32x2*)((i < 4 ? ra0 : ra1) + e * 16384 + (i & 3) * 4096);
    } else if (part == 1) {
      zz = (((pr[0] + pr[1]) + (pr[2] + pr[3])) + ((pr[4] + pr[5]) + (pr[6] + pr[7]))) + bf2(b2v);
      asm volatile("" : "+v"(zz));
    } else { g[e] = zz * rcpx2(exp2x2(zz * sp2(-1.4426950408889634f)) + sp2(1.f)); asm volatile("" : "+v"(g[e])); }
    SB();
  };
  f32x4 a2[2][4];
  const u32 wa2 = (w < 4 ? 65536 : 0) + gw * 4096 + n16 * 256;
  auto pwrite = [&](int t, int nt) AI { lds_st<f32x4>(t * 16384 + wa2 + (((4 * nt + g4) ^ (n16 & 7)) << 4), a2[t & 1][nt]); SB(); };
#pragma unroll
  for (int t = 0; t < 4; ++t) {
#pragma unroll
    for (int i = 0; i < 7; ++i) {
      if (t == 0 && i == YDR) { waitv<0>(); arrive_flag(ws, C_Y, 8, OFF_FY + b * FS, tag, l); SB(); }
      if (t == 1 && i == 0) cnt_wait(C_S1, 8);
#pragma unroll
      for (int nt = 0; nt < 4; ++nt) a2[t & 1][nt] = mfma(w2f[i * 4 + nt], hf[t * 7 + i], i ? a2[t & 1][nt] : f32x4{0, 0, 0, 0});
      SB();
      if (t > 0 && i < 4) pwrite(t - 1, i);
      if (t > 0 && i == 4) cnt_add(C_T + 4 * (t - 1));
      load_h(t * 7 + i + HLA);
      if (t >= 2 && i >= 4) gate(t - 2, i - 4);
      if (t * 7 + i + HLA == 28) { fyv = flag_ld(ws, fy_off); SB(); }
      if (t == 3 && i == 0) {
        STAMP2(32);
        flag_chk(fyv, ws, fy_off, tag);
        STAMP2(33);
        yl = ld128(rs_ws, tid * 16, OFF_Y + rt * 65536 + ct * 8192);
        SB();
      }
    }
    STAMP(9 + t);
  }
#pragma unroll
  for (int nt = 0; nt < 4; ++nt) pwrite(3, nt);
#pragma unroll
  for (int e = 0; e < 4; ++e) lds_st<u32>(L_YS + ((tid >> 4) * 65 + (tid & 15) * 4 + e) * 4, yl[e]);
  cnt_add(C_T + 12);
  gate(2, 0); gate(2, 1); gate(2, 2);
  STAMP2(36);
  gate(3, 0);
  STAMP(13);
  {
    u32 yv[4];
#pragma unroll
    for (int e = 0; e < 4; ++e) yv[e] = lds_ld<u32>(L_YS + (cq * 65 + e * 16 + tk) * 4);
    SB();
    gate(3, 1); gate(3, 2);
    EACH4(xn[j] = rb2(xn[j] * bf2(yv[j])));
    EACH4(g[j] = fma2(xn[j], g[j], bf2(qv[j])));
    u16* op = out + (rt * 64 + tk) * D_ + ct * 64 + cq * 2;
    EACH4(*(GAS u32*)(op + j * 16 * D_) = pk2(g[j].x, g[j].y));
  }
  if (b == 0 && w == 0) {
#pragma unroll 1
    for (int i = 0; i < 4; ++i) poll_flags(ws, OFF_FY + (i * 64 + l) * FS, tag);
    if (l == 0) set_flag(ws, OFF_E, tag);
  }
  STAMP(14);
  STAMP_END;
}

extern "C" void kernel_launch(void* const* d_in, const int* in_sizes, int n_in, void* d_out, int out_size, void* d_ws, size_t ws_size,
                              hipStream_t stream) {
  char* ws = (char*)d_ws + (WS_TAIL ? ((ws_size - WS_BYTES) & ~(size_t)4095) : 0);
  fused_kernel<<<256, 512, 0, stream>>>((const u16*)d_in[0], (const u16*)d_in[1], (const u16*)d_in[2], (const u16*)d_in[3], (const u16*)d_in[4],
                                        (const u16*)d_in[5], (const u16*)d_in[7], (const u16*)d_in[8], (const u16*)d_in[9], (const u16*)d_in[10],
                                        (u16*)d_out, ws);
}
```

```cpp
#include <hip/hip_runtime.h>
typedef unsigned short u16;
typedef unsigned u32;
typedef unsigned long long u64;
typedef __bf16 bf16x8 __attribute__((ext_vector_type(8)));
typedef __bf16 bf16x2 __attribute__((ext_vector_type(2)));
typedef short s16x4 __attribute__((ext_vector_type(4)));
typedef float f32x4 __attribute__((ext_vector_type(4)));
typedef float f32x2 __attribute__((ext_vector_type(2)));
typedef unsigned u32x4 __attribute__((ext_vector_type(4)));
typedef unsigned u32x2 __attribute__((ext_vector_type(2)));
#define LAS __attribute__((address_space(3)))
#define GAS __attribute__((address_space(1)))
#define DI __device__ __forceinline__
#pragma clang diagnostic ignored "-Wint-to-pointer-cast"

#define FSTR 256
constexpr int S_ = 2048, D_ = 512, F_ = 1792;
constexpr u32 OFF_E = 0, OFF_CS = 16384, OFF_ST = 32768, FS = FSTR  , OFF_FU = 65536, OFF_FW = OFF_FU + 256 * FS, OFF_FH = OFF_FW + 256 * FS, OFF_FY = OFF_FH + 256 * FS;
constexpr u32 OFF_U = 1u << 20, OFF_Y = 3u << 20, OFF_W2T = 5u << 20, OFF_H = 7u << 20, WS_BYTES = 14u << 20;
constexpr u32 SLOT = 32768, L_PK = 131072, L_PV = 140288, L_PW = 148480, L_SK = 156928, L_CNT = 157696,
              L_RC = 157824  , L_CC = 158848  , L_CSX = 159872,
              L_HC0 = 131072, L_HC1 = 0, HCS = 4160, L_UR = 147712, URS = 4576, L_CP = 32768, L_YS = 131072, L_QS = 147712;
constexpr u32 C_FULL = L_CNT  , C_FREE = L_CNT + 48  , C_H = L_CNT + 80, C_W = L_CNT + 84, C_U = L_CNT + 88, C_Y = L_CNT + 92,
              C_S1 = L_CNT + 96, C_S2 = L_CNT + 100, C_S3 = L_CNT + 104, C_SA = L_CNT + 36  , C_T = L_CNT + 112  , L_B1 = 157440;
#define PSLEEP 8
#define NT_G 0
#define NT_P 1
#define WS_TAIL 0
#define BPRIO 1
#define NWB 28
#define WDR 1
#define SPQ 32
#define KVD -1
#define KROT 2
#define DEEP 0
#define PREPS 6
#define LPRIO 2
#define WGO 5
#define PFIRST 0
#define UDR 2
#define LFIRST 0
#define APRIO 1
#define CPS 5
#define HL0 5
#define YDR 0
#define NOPK 0
#define CPF 1
#define HLA 12

#define STAMP(i)
#define STAMP_INIT
#define STAMP_END
#define STAMP2(i)

template <int V> struct IC { static constexpr int v = V; };
template <int I, int N, class F> DI void sfor(F&& f) { if constexpr (I < N) { f(IC<I>{}); sfor<I + 1, N>(f); } }
DI u32x4 mk_srd(const void* p, u32 bytes) { u64 a = (u64)p; return u32x4{(u32)a, (u32)(a >> 32) & 0xffffu, bytes, 0x00020000u}; }
template <int NT> DI void dma16(u32x4 srd, u32 voff, u32 soff, u32 lds) {
  if (NT) asm volatile("s_mov_b32 m0, %3\n\ts_nop 0\n\tbuffer_load_dwordx4 %0, %1, %2 offen nt lds" ::"v"(voff), "s"(srd), "s"(soff), "s"(lds) : "memory");
  else asm volatile("s_mov_b32 m0, %3\n\ts_nop 0\n\tbuffer_load_dwordx4 %0, %1, %2 offen lds" ::"v"(voff), "s"(srd), "s"(soff), "s"(lds) : "memory");
}
template <int NT, int I> DI void dma16g(u32x4 srd, u32 voff, u32 soff, u32 lds) {
  if (I == 0) dma16<NT>(srd, voff, soff, lds);
  else if (NT) asm volatile("buffer_load_dwordx4 %0, %1, %2 offen offset:%3 nt lds" ::"v"(voff), "s"(srd), "s"(soff), "i"(I * 1024) : "memory");
  else asm volatile("buffer_load_dwordx4 %0, %1, %2 offen offset:%3 lds" ::"v"(voff), "s"(srd), "s"(soff), "i"(I * 1024) : "memory");
}
DI void dma16m(u32x4 srd, u32 voff, u32 soff, u32 lds, u64 mask) {
  asm volatile("s_mov_b32 m0, %3\n\ts_mov_b64 exec, %4\n\tbuffer_load_dwordx4 %0, %1, %2 offen lds\n\ts_mov_b64 exec, -1"
               ::"v"(voff), "s"(srd), "s"(soff), "s"(lds), "s"(mask) : "memory");
}
template <int N> DI void waitv() { asm volatile("s_waitcnt vmcnt(%0)" ::"i"(N) : "memory"); }
#define WAITL0() asm volatile("s_waitcnt lgkmcnt(0)" ::: "memory")
#define CFENCE() asm volatile("" ::: "memory")
#define SB() __builtin_amdgcn_sched_barrier(0)
DI void bar() { SB(); CFENCE(); __builtin_amdgcn_s_waitcnt(0xC07F); __builtin_amdgcn_s_barrier(); CFENCE(); SB(); }
DI float bflo(u32 x) { return __uint_as_float(x << 16); }
DI float bfhi(u32 x) { return __uint_as_float(x & 0xffff0000u); }
DI u32 pk2(float a, float b) { f32x2 p = {a, b}; return __builtin_bit_cast(u32, __builtin_convertvector(p, bf16x2)); }
DI float rb(float a) { return bflo(pk2(a, a)); }
DI f32x2 bf2(u32 x) { return f32x2{bflo(x), bfhi(x)}; }
DI f32x2 rb2(f32x2 v) { return bf2(pk2(v.x, v.y)); }
DI f32x2 sp2(float v) { return f32x2{v, v}; }
DI f32x2 exp2x2(f32x2 v) { return f32x2{__builtin_amdgcn_exp2f(v.x), __builtin_amdgcn_exp2f(v.y)}; }
DI f32x2 rcpx2(f32x2 v) { return f32x2{__builtin_amdgcn_rcpf(v.x), __builtin_amdgcn_rcpf(v.y)}; }
DI f32x2 fma2(f32x2 a, f32x2 b, f32x2 c) { return __builtin_elementwise_fma(a, b, c); }
DI float sfma(float a, float b, float c) { return __builtin_fmaf(a, b, c); }
DI float smul(float a, float b) { return a * b; }
DI float sadd(float a, float b) { return a + b; }
DI f32x2 fma2s(f32x2 a, f32x2 b, f32x2 c) { return fma2(a, b, c); }
DI f32x2 mul2s(f32x2 a, f32x2 b) { return a * b; }
DI f32x2 add2s(f32x2 a, f32x2 b) { return a + b; }
template <class T> DI T lds_ld(u32 off) { return *(const LAS T*)(off); }
template <class T> DI void lds_st(u32 off, T v) { *(LAS T*)(off) = v; }
DI u32x2 lds_tr(u32 off) { return __builtin_bit_cast(u32x2, __builtin_amdgcn_ds_read_tr16_b64_v4i16((LAS s16x4*)(off))); }
DI u32x4 lds_tr2(u32 o0, u32 o1) { u32x2 a = lds_tr(o0), b = lds_tr(o1); return u32x4{a.x, a.y, b.x, b.y}; }
DI u32x4 lds_2x64(u32 o) { u32x2 a = *(const volatile LAS u32x2*)(o), b = *(const volatile LAS u32x2*)(o + 8); return u32x4{a.x, a.y, b.x, b.y}; }
DI f32x4 mfma(u32x4 a, u32x4 b, f32x4 c) {
  return __builtin_amdgcn_mfma_f32_16x16x32_bf16(__builtin_bit_cast(bf16x8, a), __builtin_bit_cast(bf16x8, b), c, 0, 0, 0);
}
DI void st128(u32x4 v, __amdgpu_buffer_rsrc_t rs, u32 voff, u32 soff) {
  __builtin_amdgcn_raw_buffer_store_b128(v, rs, voff, soff, 16);
  asm volatile("s_nop 1" ::"v"(v) : "memory");
}
DI u32x4 ld128(__amdgpu_buffer_rsrc_t rs, u32 voff, u32 soff) { return __builtin_amdgcn_raw_buffer_load_b128(rs, voff, soff, 16); }
template <int C> DI float dpp_add(float v) {
  return v + __builtin_bit_cast(float, __builtin_amdgcn_update_dpp(0, __builtin_bit_cast(int, v), C, 0xf, 0xf, true));
}
DI float bperm(int src_lane4, float v) { return __builtin_bit_cast(float, __builtin_amdgcn_ds_bpermute(src_lane4, __builtin_bit_cast(int, v))); }
DI float sel(bool c, float a, float b) { return c ? a : b; }
DI float rl(float v, int i) { return __builtin_bit_cast(float, __builtin_amdgcn_readlane(__builtin_bit_cast(int, v), i)); }
DI float wave_sum(float v) {
  v = dpp_add<0xB1>(v); v = dpp_add<0x4E>(v); v = dpp_add<0x141>(v); v = dpp_add<0x140>(v);
  return (rl(v, 0) + rl(v, 16)) + (rl(v, 32) + rl(v, 48));
}
constexpr int SPIN_CAP = 1 << 16;
DI u64 flag_ld(const char* ws, u32 off) { return __hip_atomic_load((const GAS u64*)(ws + off), __ATOMIC_RELAXED, __HIP_MEMORY_SCOPE_AGENT); }
DI void poll_flags(const char* ws, u32 off, u64 tag) {
#pragma unroll 1
  for (int it = 0; it < SPIN_CAP; ++it) {
    if (__all(flag_ld(ws, off) == tag)) break;
    __builtin_amdgcn_s_sleep(PSLEEP);
  }
  CFENCE();
}
DI void flag_chk(u64 v, const char* ws, u32 off, u64 tag) { if (!__all(v == tag)) poll_flags(ws, off, tag); CFENCE(); }
DI void set_flag(char* ws, u32 off, u64 tag) { __hip_atomic_store((GAS u64*)(ws + off), tag, __ATOMIC_RELAXED, __HIP_MEMORY_SCOPE_AGENT); }
DI void arrive_flag(char* ws, u32 cnt, u32 n, u32 off, u64 tag, int l) {
  if (l == 0) {
    u32 old;
    asm volatile("ds_add_rtn_u32 %0, %1, %2\n\ts_waitcnt lgkmcnt(0)" : "=&v"(old) : "v"(cnt), "v"(1u) : "memory");
    if (old == n - 1) set_flag(ws, off, tag);
  }
  CFENCE();
}
DI void cnt_add(u32 cnt) { asm volatile("s_mov_b64 exec, 1\n\tds_add_u32 %0, %1\n\ts_mov_b64 exec, -1" ::"v"(cnt), "v"(1u) : "memory"); }
DI u32 cnt_ld(u32 cnt) { return *(const volatile LAS u32*)cnt; }
DI void cnt_wait(u32 cnt, u32 n) {
#pragma unroll 1
  for (int it = 0; it < (1 << 22); ++it) { if ((u32)__builtin_amdgcn_readfirstlane(cnt_ld(cnt)) >= n) break; __builtin_amdgcn_s_sleep(1); }
  CFENCE(); SB();
}
DI void group_sync(u32 cnt) { SB(); WAITL0(); cnt_add(cnt); cnt_wait(cnt, 4); }
#define AI __attribute__((always_inline))
#define EACH4(stmt) do { _Pragma("unroll") for (int j = 0; j < 4; ++j) { stmt; } SB(); } while (0)
#define HALF(v, j) (((j) & 1) ? (v).hi : (v).lo)

#define KATTR
__global__ __launch_bounds__(512, 2) KATTR void fused_kernel(const u16* __restrict__ q, const u16* __restrict__ kk, const u16* __restrict__ vv,
                                                       const u16* __restrict__ sk, const u16* __restrict__ ssk, const u16* __restrict__ sD,
                                                       const u16* __restrict__ w1, const u16* __restrict__ b1, const u16* __restrict__ w2,
                                                       const u16* __restrict__ b2, u16* __restrict__ out, char* __restrict__ ws) {
  __shared__ __attribute__((aligned(16))) char sm[163840];
  asm volatile("" ::"v"(sm) : "memory");
  asm volatile("" : "+s"(q), "+s"(kk), "+s"(vv), "+s"(sk), "+s"(ssk), "+s"(sD), "+s"(w1), "+s"(b1), "+s"(w2), "+s"(b2), "+s"(out), "+s"(ws));
  const int tid = threadIdx.x, l = tid & 63;
  const int w = __builtin_amdgcn_readfirstlane(tid >> 6);
  const int b = blockIdx.x, xcd = b & 7, r = b >> 3;
  const bool mainwg = r < 28;
  const int rp = (xcd & 3) * 4 + (r & 3), cc = (xcd >> 2) * 7 + (r >> 2);
  const int rt = (xcd & 3) * 8 + (xcd >> 2) * 4 + (r & 3), ct = r >> 2;
  const int gw = w & 3;
  const int n16 = l & 15, g4 = l >> 4;
  STAMP_INIT
  STAMP(0);
  if (CPF && w == 7) {
    u64 pc; u32 dummy;
    asm volatile("s_getpc_b64 %0" : "=s"(pc));
    asm volatile("global_load_dword %0, %1, %2" : "=v"(dummy) : "v"((u32)((r & 3) * 64 + l) * 128u - 128u * ((r & 3) == 3 && l >= 24 ? l - 24 : 0)), "s"(pc) : "memory");
  }
  if (tid < 8) lds_st<u32x4>(L_CNT + tid * 16, u32x4{0, 0, 0, 0});
  const u32x4 srd_q = mk_srd(q, S_ * D_ * 2);
  const u32x4 srd_w1m = mk_srd((const char*)w1 - F_ * 2, D_ * F_ * 2 + F_ * 2);
  u32x2 ev;
  u64 tag;
  auto load_tag = [&]() AI { asm volatile("s_mov_b64 exec, 1\n\tglobal_load_dwordx2 %0, %1, %2 sc1\n\ts_mov_b64 exec, -1" : "=v"(ev) : "v"(0u), "s"(ws) : "memory"); };
  auto get_tag = [&]() AI {
    SB();
    asm volatile("" : "+v"(ev));
    tag = (((u64)__builtin_amdgcn_readfirstlane(ev.y) << 32) | (u32)__builtin_amdgcn_readfirstlane(ev.x)) + 0x9E3779B97F4A7C15ull;
  };
  const __amdgpu_buffer_rsrc_t rs_ws = __builtin_amdgcn_make_buffer_rsrc(ws, 0, WS_BYTES, 0x00020000);
  const u32 vr = (l >> 3) * 1024 + (l & 7) * 16;
  u32x4 w2f[28];
  auto load_w2 = [&](int j) AI { w2f[j] = ld128(rs_ws, l * 16 + (j & 3) * 1024, OFF_W2T + (ct * 56 + 7 * w + (j >> 2)) * 4096); SB(); };
  const u32 fw_off = OFF_FW + (4 * w + (ct >> 1)) * FS;
  const u32 fh_off = OFF_FH + ((rt >> 1) * 16 + min(((7 * w) >> 2) + (l & 3), (7 * w + 6) >> 2)) * FS;

  const u32 sx32 = ((2 * g4 + (n16 >> 3)) & 3) << 5, tr0 = (4 * g4 + (n16 >> 2)) * 128 + (l & 3) * 8, vsw = ((l & 7) ^ ((l >> 3) & 6)) << 4;
  const int sp = (r - 28) * 8 + xcd;
  if (w >= 4) {
    const int t2 = tid & 255;
    u32x4 ha[2]; u32x2 hn[2];
    auto load_taps = [&]() AI {
      asm volatile("buffer_load_dwordx4 %0, %4, %5, %6 offen nt\n\tbuffer_load_dwordx4 %1, %4, %5, %6 offen offset:1024 nt\n\t"
                   "buffer_load_dwordx2 %2, %4, %5, %6 offen offset:16\n\tbuffer_load_dwordx2 %3, %4, %5, %6 offen offset:1040"
                   : "=&v"(ha[0]), "=&v"(ha[1]), "=&v"(hn[0]), "=&v"(hn[1]) : "v"(l * 16), "s"(mk_srd(ssk, S_ * D_ * 2)), "s"(b * 8192 + gw * 2048) : "memory");
    };
    __builtin_amdgcn_s_setprio(LPRIO);
    const u32 fu_off = (l & 8) ? fw_off : OFF_FU + ((b >> 5) * 32 + 8 * gw + (l & 7)) * FS;
    u64 fuv = 0;
    load_tag();
    const u32 vx0 = (l >> 3) * 1024 + (((l & 7) ^ (l >> 3)) << 4), vx1 = vx0 + 8192 - 1024;
    const u32 vwb = (g4 + 1) * (F_ * 2) - 2048, vws = (n16 ^ (g4 << 1)) << 4;
    const u32 vw[4] = {vwb + vws, vwb + vws + 4 * (F_ * 2) - 1024, vwb + (vws ^ 128), vwb + (vws ^ 128) + 4 * (F_ * 2) - 1024};
    const int kc0 = KROT == 1 ? ((r >> 2) + 2 * (r & 3)) & 7 : KROT == 2 ? (((r >> 2) + r) & 1) * 4 : 0;
    auto stage = [&](int s) AI {
      const int kc = (s + kc0) & 7;
      const u32 sx = rp * 131072 + (gw & 1) * 65536 + kc * 128, sw = ((gw & 1) * 32 + kc * 64) * (F_ * 2) + cc * 256, dst = (s & 3) * SLOT + (gw & 1) * 16384;
#pragma unroll
      for (int i = 0; i < 4; ++i) {
        dma16g<NT_G, 0>(srd_q, vx0, sx + i * 16384, dst + i * 4096); dma16g<NT_G, 1>(srd_q, vx1, sx + i * 16384, 0);
        dma16g<NT_G, 2>(srd_w1m, vw[(i & 1) * 2], sw + i * 8 * (F_ * 2), 0); dma16g<NT_G, 3>(srd_w1m, vw[(i & 1) * 2 + 1], sw + i * 8 * (F_ * 2), 0);
      }
    };
    if (!LFIRST) bar();
    if (mainwg) { stage(gw >> 1); if (LFIRST) bar(); stage((gw >> 1) + 2); waitv<16>(); cnt_add(C_FULL + 4 * (gw >> 1)); }
    else {
#pragma unroll
      for (int p = 0; p < 4; ++p) {
        const int hp = gw * 4 + p;
        sfor<0, 4>([&](auto I) AI { dma16g<NT_P, I.v>(mk_srd(w2, hp < 14 ? D_ * F_ * 2 : 0), (l >> 3) * 1024 + vsw,
                                                     ((sp >> 2) * 7 + (hp >> 1)) * 32768 + (sp & 3) * 256 + (hp & 1) * 128 + I.v * 7168, hp * 4096); });
      }
      if (LFIRST) bar();
    }
    waitv<16>();
    get_tag();
    STAMP2(40);
    STAMP2(43);
    if (mainwg) {
      const int s0 = gw >> 1;
      waitv<0>(); cnt_add(C_FULL + 4 * s0 + 8);
      cnt_wait(C_FREE + 4 * s0, 4);
      stage(s0 + 4);
      waitv<0>(); cnt_add(C_FULL + 4 * s0 + 16);
      u32x4 g;
      asm volatile("buffer_load_dwordx4 %0, %1, %2, %3 offen sc1" : "=v"(g) : "v"(l * 16), "s"(mk_srd(ws, WS_BYTES)), "s"(OFF_ST + rp * 2048 + (gw & 1) * 1024) : "memory");
      dma16m(mk_srd(b1, F_ * 2), l * 16, cc * 256, L_B1, 0xffffull);
      cnt_wait(C_FREE + 4 * s0 + 8, 4);
      stage(s0 + 6);
      load_taps();
      waitv<20>();
      STAMP2(41);
      SB();
      asm volatile("" : "+v"(g));
      if (gw < 2) {
        auto g_ok = [&]() AI { return __all((g.y == (u32)tag) & (g.w == (u32)(tag >> 32))); };
        if (!g_ok()) {
#pragma unroll 1
          for (int it = 0; it < SPIN_CAP; ++it) { g = ld128(rs_ws, l * 16, OFF_ST + rp * 2048 + gw * 1024); if (g_ok()) break; __builtin_amdgcn_s_sleep(4); CFENCE(); }
        }
        const float i2 = __builtin_amdgcn_rcpf(bfhi(g.z));
        float ra = __builtin_amdgcn_rcpf(bfhi(g.x)) * i2;
        asm volatile("" : "+v"(ra));
        lds_st<f32x2>(L_RC + (gw * 64 + l) * 8, f32x2{ra, -bflo(g.x) * ra - bflo(g.z) * i2});
      } else lds_st<u32>(L_CC + ((gw & 1) * 64 + l) * 4, (u32)lds_ld<u16>(L_B1 + ((gw & 1) * 64 + l) * 2) << 16);
      fuv = flag_ld(ws, fu_off);
      waitv<5>();
      cnt_add(C_FULL + 4 * s0 + 24);
      STAMP2(42);
    } else {
      waitv<0>();
      SB();
#pragma unroll
      for (int p = 0; p < 4; ++p) {
        const int hp = gw * 4 + p;
        u32x4 wt[4];
#pragma unroll
        for (int nt = 0; nt < 4; ++nt) { const u32 a = hp * 4096 + tr0 + ((nt * 32) ^ sx32); wt[nt] = lds_tr2(a, a + 2048); }
#pragma unroll
        for (int nt = 0; nt < 4; ++nt)
          st128(wt[nt], rs_ws, l * 16, hp < 14 ? OFF_W2T + ((((sp & 3) * 2 + (hp & 1)) * 56 + (sp >> 2) * 7 + (hp >> 1)) * 4 + nt) * 1024 : 0x7f000000u);
      }
      STAMP2(41);
      waitv<0>();
      arrive_flag(ws, C_W, 4, OFF_FW + sp * FS, tag, l);
      STAMP2(42);
      load_taps();
      cnt_wait(C_W, 4);
      __builtin_amdgcn_s_sleep(SPQ);
    }
    STAMP(2);
    if (!mainwg) { cnt_wait(C_U, 4); }
    __builtin_amdgcn_s_setprio(0);
    waitv<0>();
    flag_chk(fuv, ws, fu_off, tag);
    STAMP(3);
    u32x4 uu[2];
#pragma unroll
    for (int i = 0; i < 2; ++i) uu[i] = ld128(rs_ws, l * 16 + i * 1024, OFF_U + b * 8192 + gw * 2048);
    SB();
#pragma unroll
    for (int j = 0; j < NWB; ++j) load_w2(j);
    STAMP2(30);
    if (mainwg) cnt_wait(C_FREE + 4 * CPS, 4);
    asm volatile("" : "+v"(ha[0]), "+v"(ha[1]), "+v"(hn[0]), "+v"(hn[1]));
#pragma unroll
    for (int i = 0; i < 2; ++i) {
      const u32x4 x = ha[i];
      const u32 o0 = __builtin_amdgcn_alignbit(x.y, x.x, 16), o1 = __builtin_amdgcn_alignbit(x.z, x.y, 16), o2 = __builtin_amdgcn_alignbit(x.w, x.z, 16),
                o3 = __builtin_amdgcn_alignbit(hn[i].x, x.w, 16), o4 = __builtin_amdgcn_alignbit(hn[i].y, hn[i].x, 16);
      const u32 d = ((gw >> 1) ? L_HC1 : L_HC0) + 32 + (gw & 1) * 2048 + i * 1024 + l * 16;
      lds_st<u32x4>(d, x); lds_st<u32x4>(d + HCS, u32x4{o0, o1, o2, o3});
      lds_st<u32x4>(d + 2 * HCS, u32x4{x.y, x.z, x.w, hn[i].x}); lds_st<u32x4>(d + 3 * HCS, u32x4{o1, o2, o3, o4});
    }
    if (l < 19) {
      const int z = gw * 19 + l;
      lds_st<u32x4>(z < 16 ? ((z & 8) ? L_HC1 : L_HC0) + ((z >> 1) & 3) * HCS + (z & 1) * 16 : L_UR + 4096 + (z - 16) * 16 + (z >= 46 ? URS - 480 : 0), u32x4{0, 0, 0, 0});
    }
#pragma unroll
    for (int i = 0; i < 2; ++i) {
      const u32 a = L_UR + (2044 - 4 * (gw * 128 + i * 64 + l)) * 2;
      lds_st<u32x2>(a, u32x2{uu[i].x, uu[i].y}); lds_st<u32x2>(a + URS, u32x2{uu[i].z, uu[i].w});
    }
    cnt_add(C_S2);
    STAMP(4);
    cnt_wait(C_S2, mainwg && !NOPK ? 8 : 4);
    STAMP(5);
    {
      const int ch = gw & 1, kh = gw >> 1;
      f32x4 ca[8];
#pragma unroll
      for (int i = 0; i < 8; ++i) ca[i] = f32x4{0, 0, 0, 0};
      const u32 ab = (ch ? L_HC1 : L_HC0) + ((n16 + 1) & 3) * HCS + (((n16 + 1) & ~3) + 8 * g4) * 2 + kh * 256;
      const u32 bb = L_UR + ch * URS + (2032 - 1792 - 16 * n16 + 8 * g4) * 2 + kh * 256;
      const u32 pa = L_CP + gw * 8192 + (4 * n16 + (g4 ^ ((n16 >> 1) & 3))) * 16;
      u32x4 AL[2][4], BL[2][4], AH[4], BH[4];
      auto rdp = [&](u32x4& a, u32x4& bq, int k0, int i) AI { SB(); a = lds_2x64(ab + k0 * 64 + i * 512); bq = lds_ld<u32x4>(bb + k0 * 64 + (7 - i) * 512); SB(); };
#pragma unroll
      for (int i = 0; i < 4; ++i) rdp(AL[0][i], BL[0][i], 0, i);
      sfor<0, 4>([&](auto K) AI {
        constexpr int k0 = K.v;
        u32x4 (&A)[4] = AL[k0 & 1]; u32x4 (&B)[4] = BL[k0 & 1];
#pragma unroll
        for (int n = 0; n < 16; ++n) {
          constexpr int oi[16] = {0, 0, 1, 0, 1, 2, 0, 1, 2, 3, 1, 2, 3, 2, 3, 3}, ot[16] = {0, 1, 0, 2, 1, 0, 3, 2, 1, 0, 3, 2, 1, 3, 2, 3};
          ca[oi[n] + ot[n]] = mfma(A[oi[n]], B[ot[n]], ca[oi[n] + ot[n]]);
          if (n < 8 && (n & 1)) rdp(AH[n >> 1], BH[n >> 1], k0, 4 + (n >> 1));
          if (k0 == 3 && n >= 12) { SB(); lds_st<f32x4>(pa + (n - 12) * 1024, ca[n - 12]); SB(); }
        }
        int cn = 0;
#pragma unroll
        for (int T = 0; T < 4; ++T)
#pragma unroll
          for (int i = 0; i <= T; ++i) {
            ca[4 + T] = mfma(A[i], BH[T - i], ca[4 + T]); ca[4 + T] = mfma(AH[T - i], B[i], ca[4 + T]);
            if (k0 < 3 && cn >= 2 && cn < 6) rdp(AL[(k0 + 1) & 1][cn - 2], BL[(k0 + 1) & 1][cn - 2], k0 + 1, cn - 2);
            if (k0 == 3 && T > 0 && i == 1) { SB(); lds_st<f32x4>(pa + (3 + T) * 1024, ca[3 + T]); SB(); }
            ++cn;
          }
        SB();
      });
      lds_st<f32x4>(pa + 7 * 1024, ca[7]);
    }
    cnt_add(C_S3);
    STAMP(6);
#pragma unroll
    for (int j = NWB; j < 28; ++j) load_w2(j);
  } else {
    {
      __builtin_amdgcn_s_setprio(3);
      const u32 so = (rt * 64 + gw * 16) * 1024 + ct * 128;
      const bool top = rt == 0 && gw == 0;
      sfor<0, 2>([&](auto I) AI { dma16g<NT_P, I.v>(mk_srd(kk, S_ * D_ * 2), vr, so + I.v * 7168, L_PK + 1024 + gw * 2048); });
      sfor<0, 2>([&](auto I) AI { dma16g<NT_P, I.v>(mk_srd(vv, S_ * D_ * 2), vr, so + I.v * 7168, L_PV + gw * 2048); });
      dma16m(mk_srd(kk, top ? 0 : S_ * D_ * 2), vr, top ? 0 : so - 8192, L_PK + gw * 2048, ~0ull << 40);
      dma16m(mk_srd(sk, D_ * 8), l * 16, ct * 512, L_SK, 0xffffffffull);
    const int srow = rt * 64 + ct * 8 + gw * 2;
    u32x4 x0[2];
      load_tag();
    asm volatile("buffer_load_dwordx4 %0, %2, %3, %4 offen\n\tbuffer_load_dwordx4 %1, %2, %3, %4 offen offset:1024"
                 : "=&v"(x0[0]), "=&v"(x0[1]) : "v"(l * 16), "s"(srd_q), "s"(srow * 1024) : "memory");
      bar();
      waitv<0>();
      get_tag();
      STAMP2(40);
      const int cpl = l & 31, hh = l >> 5;
      const u32x4 sv = lds_ld<u32x4>(L_SK + cpl * 16);
      u32 a = L_PK + (gw * 16 + hh * 8) * 128 + cpl * 4;
      asm volatile("" : "+v"(a));
      u32 kr[11], vq[8];
#pragma unroll
      for (int i = 0; i < 11; ++i) kr[i] = lds_ld<u32>(a + 5 * 128 + i * 128);
#pragma unroll
      for (int i = 0; i < 8; ++i) vq[i] = lds_ld<u32>(a + (L_PV - L_PK) + i * 128);
      SB();
      const f32x2 kT[4] = {f32x2{bflo(sv.x), bflo(sv.z)}, f32x2{bfhi(sv.x), bfhi(sv.z)}, f32x2{bflo(sv.y), bflo(sv.w)}, f32x2{bfhi(sv.y), bfhi(sv.w)}};
      f32x2 kK[11];
#pragma unroll
      for (int i = 0; i < 3; ++i) kK[i] = bf2(kr[i]);
      u32x4 uo[2];
#pragma unroll
      for (int u = 0; u < 2; ++u) {
        f32x2 ka[4];
        EACH4(kK[4 * u + 3 + j] = bf2(kr[4 * u + 3 + j]));
        EACH4(ka[j] = kK[4 * u + j] * kT[3]); EACH4(ka[j] = fma2(kK[4 * u + j + 1], kT[2], ka[j]));
        EACH4(ka[j] = fma2(kK[4 * u + j + 2], kT[1], ka[j])); EACH4(ka[j] = fma2(kK[4 * u + j + 3], kT[0], ka[j]));
        EACH4(ka[j] = rb2(ka[j])); EACH4(ka[j] *= bf2(vq[4 * u + j]));
        uo[u] = u32x4{pk2(ka[3].x, ka[2].x), pk2(ka[1].x, ka[0].x), pk2(ka[3].y, ka[2].y), pk2(ka[1].y, ka[0].y)};
        SB();
      }
#pragma unroll
      for (int u = 0; u < 2; ++u) lds_st<u32x4>(L_PV + gw * 2048 + cpl * 64 + (((hh * 2 + u) ^ ((cpl >> 1) & 3)) << 4), uo[u]);
#pragma unroll
      for (int i = 0; i < 2; ++i) uo[i] = lds_ld<u32x4>(L_PV + gw * 2048 + i * 1024 + (l >> 2) * 64 + (((l & 3) ^ ((l >> 3) & 3)) << 4));
#pragma unroll
      for (int i = 0; i < 2; ++i) st128(uo[i], rs_ws, (l >> 2) * 8192 + (l & 3) * 16, OFF_U + ((ct * 32 + 16 * i) * 512 + rt * 16 + gw * 4) * 16);
    {
      asm volatile("" : "+v"(x0[0]), "+v"(x0[1]));
      float c0[2], sa[2], sq[2];
#pragma unroll
      for (int i = 0; i < 2; ++i) {
        c0[i] = bflo(__builtin_amdgcn_readfirstlane(x0[i].x));
        const f32x2 d[4] = {bf2(x0[i].x) - sp2(c0[i]), bf2(x0[i].y) - sp2(c0[i]), bf2(x0[i].z) - sp2(c0[i]), bf2(x0[i].w) - sp2(c0[i])};
        const f32x2 s2 = (d[0] + d[1]) + (d[2] + d[3]), q2 = fma2(d[0], d[0], d[1] * d[1]) + fma2(d[2], d[2], d[3] * d[3]);
        sa[i] = s2.x + s2.y; sq[i] = q2.x + q2.y;
      }
#define RED4(C) do { sa[0] = dpp_add<C>(sa[0]); sq[0] = dpp_add<C>(sq[0]); sa[1] = dpp_add<C>(sa[1]); sq[1] = dpp_add<C>(sq[1]); SB(); } while (0)
      SB(); RED4(0xB1); RED4(0x4E); RED4(0x141); RED4(0x140); RED4(0x142); RED4(0x143);
      const bool odd = l & 1;
      const float md = sel(odd, sa[1], sa[0]) * (1.f / D_), mean = sel(odd, c0[1], c0[0]) + md, var = fmaxf(sel(odd, sq[1], sq[0]) * (1.f / D_) - md * md, 0.f);
      const float m1 = rb(mean), sd1 = rb(__builtin_amdgcn_sqrtf(rb(rb(var) + 1e-5f)));
      const float i1 = __builtin_amdgcn_rcpf(sd1);
      const float m2 = (mean - m1) * i1, sd2 = __builtin_amdgcn_sqrtf(rb(rb(var * i1 * i1) + 1e-5f));
      if (l >= 62) st128(u32x4{pk2(m1, sd1), (u32)tag, pk2(m2, sd2), (u32)(tag >> 32)}, rs_ws, (l - 62) * 16, OFF_ST + srow * 16);
    }

      __builtin_amdgcn_s_setprio(APRIO);
      if (!mainwg) { waitv<0>(); arrive_flag(ws, C_U, 4, OFF_FU + (ct * 32 + rt) * FS, tag, l); STAMP2(42); cnt_wait(C_W, 4); __builtin_amdgcn_s_sleep(SPQ); }
    }
    if (mainwg) {
      const int th = gw >> 1, fh = gw & 1;
      f32x4 acc[4][4];
#pragma unroll
      for (int i = 0; i < 16; ++i) acc[i >> 2][i & 3] = f32x4{0, 0, 0, 0};
      const u32 xa = th * 16384 + n16 * 128 + ((g4 ^ (n16 & 7)) << 4);
      const u32 wa = g4 * 4096 + 2048 + (n16 >> 2) * 256 + (((4 * fh + 2 * th) ^ ((n16 >> 2) | ((g4 & 1) << 2))) << 5) + (l & 3) * 8;
      u32 ad[2][6];
#pragma unroll
      for (int h = 0; h < 2; ++h) {
#pragma unroll
        for (int e = 0; e < 4; ++e) ad[h][e] = (wa ^ (e * 32)) + h * 65536;
        ad[h][4] = xa + h * 65536; ad[h][5] = (xa ^ 64) + h * 65536;
        asm volatile("" : "+v"(ad[h][0]), "+v"(ad[h][1]), "+v"(ad[h][2]), "+v"(ad[h][3]), "+v"(ad[h][4]), "+v"(ad[h][5]));
      }
      u32x4 xf[2][4], wf[2][4];
      f32x4 cf[4], bf[4]; f32x2 rc[4];
      auto rd = [&](int hs, int p) AI {
        const int s = hs >> 1, ks = hs & 1, hi = (s >> 1) & 1, e = p >> 1;
        const u32 so = (s & 1) * SLOT;
        if (hs > 15) {
          if (p < 4) bf[p] = lds_ld<f32x4>(L_CC + fh * 256 + ((p * 64) ^ (th * 128)) + g4 * 16);
          else rc[p - 4] = lds_ld<f32x2>(L_RC + (th * 64 + (p - 4) * 16 + n16) * 8);
        } else if (!(p & 1)) { const u32 a = ad[hi][e] + so + ks * 16384; wf[ks][e] = lds_tr2(a, a + 1024); }
        else xf[ks][e] = lds_ld<u32x4>(ad[hi][4 + ks] + so + e * 4096);
      };
      u32 fullv;
      u32x4 ones = {0x3f803f80u, 0x3f803f80u, 0x3f803f80u, 0x3f803f80u};
      asm volatile("" : "+v"(ones));
#pragma unroll
      for (int i = 0; i < 4; ++i) cf[i] = f32x4{0, 0, 0, 0};
      auto half = [&](int hs) AI {
        const int ks = hs & 1;
        sfor<0, 16>([&](auto J) AI {
          constexpr int j = J.v, o = j < 4 ? (j & 1) * 4 + (j >> 1) : j < 9 ? (j == 4 ? 8 : j == 5 ? 9 : j == 6 ? 2 : j == 7 ? 6 : 10) : (j < 12 ? 12 + (j - 9) : (j - 12) * 4 + 3);
          acc[o >> 2][o & 3] = mfma(wf[ks][o >> 2], xf[ks][o & 3], acc[o >> 2][o & 3]);
          SB();
          if (j < 12 && j % 3 != 1) rd(hs + 1, (j / 3) * 2 + (j % 3) / 2);
          if (ks == 0 && j == 12) cnt_add(C_FREE + 2 * hs);
          if (ks == 0 && j == 13 && hs < 14) fullv = cnt_ld(C_FULL + 2 * hs + 4);
          SB();
        });
        cf[0] = mfma(wf[ks][0], ones, cf[0]); cf[1] = mfma(wf[ks][1], ones, cf[1]);
        SB();
      };
      STAMP2(34);
      cnt_wait(C_FULL, 2);
      STAMP2(35);
#pragma unroll
      for (int p = 0; p < 8; ++p) rd(0, p);
      SB();
      sfor<0, 8>([&](auto S) AI {
        constexpr int s = S.v;
        half(2 * s);
        if (s < 7 && (u32)__builtin_amdgcn_readfirstlane(fullv) < 2) cnt_wait(C_FULL + 4 * s + 4, 2);
        CFENCE(); SB();
        if (s == UDR) { waitv<0>(); arrive_flag(ws, C_U, 4, OFF_FU + (ct * 32 + rt) * FS, tag, l); }
        STAMP2(16 + s);
        half(2 * s + 1);
      });
      STAMP(2);
      __builtin_amdgcn_s_setprio(2);
      if (n16 == 0) { lds_st<f32x4>(L_CSX + gw * 128 + g4 * 16, cf[0]); lds_st<f32x4>(L_CSX + gw * 128 + 64 + g4 * 16, cf[1]); }
      cnt_add(C_SA + fh * 4);
      const float c1 = -2.f * 0.7978845608028654f * 1.4426950408889634f, c2 = c1 * 0.044715f;
      const u32 hso = OFF_H + ((rp * 8 + th * 4) * 56 + cc * 4 + fh * 2) * 1024;
#pragma unroll
      for (int u = 0; u < 8; ++u) {
        const int tt = u & 3, kp = u >> 2;
        if (u == 3) { cnt_wait(C_SA + fh * 4, 2); cf[2] = lds_ld<f32x4>(L_CSX + (gw ^ 2) * 128 + g4 * 16); cf[3] = lds_ld<f32x4>(L_CSX + (gw ^ 2) * 128 + 64 + g4 * 16); SB(); }
        f32x2 x[4], p[4]; u32 hp[4];
        EACH4(x[j] = fma2(HALF(cf[kp * 2 + (j >> 1)], j), sp2(rc[tt].y), HALF(bf[kp * 2 + (j >> 1)], j)));
        EACH4(x[j] = fma2(HALF(acc[kp * 2 + (j >> 1)][tt], j), sp2(rc[tt].x), x[j]));
        EACH4(p[j] = x[j] * x[j]);
        EACH4(p[j] = fma2(p[j], sp2(c2), sp2(c1)));
        EACH4(p[j] = p[j] * x[j]);
        EACH4(p[j] = exp2x2(p[j]));
        EACH4(p[j] = p[j] + sp2(1.f));
        EACH4(p[j] = rcpx2(p[j]));
        EACH4(p[j] = p[j] * x[j]);
        EACH4(hp[j] = pk2(p[j].x, p[j].y));
        st128(u32x4{hp[0], hp[1], hp[2], hp[3]}, rs_ws, l * 16, hso + tt * 56 * 1024 + ((kp * 1024) ^ (th * 1024)));
        SB();
      }
      if (!NOPK) cnt_add(C_S2);
      STAMP(4);
    }
    if (!mainwg) __builtin_amdgcn_s_sleep(SPQ);
    poll_flags(ws, fw_off, tag);
    waitv<0>();
    if (mainwg) arrive_flag(ws, C_H, 4, OFF_FH + (rp * 16 + cc) * FS, tag, l);
    __builtin_amdgcn_s_setprio(0);
    STAMP(5);
#pragma unroll
    for (int j = 0; j < 28; ++j) load_w2(j);
    STAMP(6);
    if (!mainwg) cnt_wait(C_S3, 4);
  }
  u32 qv[4];
  const u32 b2v = *(const GAS u32*)(b2 + ct * 64 + (tid & 31) * 2), dd = *(const volatile GAS u32*)(sD + 2 * b);
#pragma unroll
  for (int e = 0; e < 4; ++e) qv[e] = *(const GAS u32*)(q + (rt * 64 + e * 16 + (tid >> 5)) * D_ + ct * 64 + (tid & 31) * 2);
  SB();
  poll_flags(ws, fh_off, tag);
  STAMP(7);
  u32x4 hf[28];
  auto load_h = [&](int n) AI { if (n < 28) { hf[n] = ld128(rs_ws, l * 16, OFF_H + ((rt * 4 + n / 7) * 56 + 7 * w + n % 7) * 1024); SB(); } };
  const u32 stf = __builtin_amdgcn_raw_buffer_load_b32(rs_ws, l * 16, OFF_ST + rt * 1024, 16);
#pragma unroll
  for (int n = 0; n < HL0; ++n) load_h(n);
  cnt_wait(C_S3, 4);
  STAMP2(29);
  {
    const u32x2 hc = lds_ld<u32x2>(L_HC0 + 32), hb = lds_ld<u32x2>(L_HC1 + 32);
    const u32x2 u0 = lds_ld<u32x2>(L_UR + (2044 - 4 * tid) * 2), u1 = lds_ld<u32x2>(L_UR + URS + (2044 - 4 * tid) * 2);
    const u32 pa = L_CP + ((tid & ~3) | ((tid & 3) ^ ((tid >> 3) & 3))) * 16;
    f32x4 pr[4];
#pragma unroll
    for (int p = 0; p < 4; ++p) pr[p] = lds_ld<f32x4>(pa + p * 8192);
    SB();
    const f32x2 d2 = bf2(dd), h0 = {bflo(hc.x), bflo(hb.x)}, h1 = {bfhi(hc.x), bfhi(hb.x)}, h2 = {bflo(hc.y), bflo(hb.y)};
    const f32x2 a[4] = {f32x2{bfhi(u0.y), bfhi(u1.y)}, f32x2{bflo(u0.y), bflo(u1.y)}, f32x2{bfhi(u0.x), bfhi(u1.x)}, f32x2{bflo(u0.x), bflo(u1.x)}};
    const f32x4 y0 = pr[0] + pr[2], y1 = pr[1] + pr[3];
    f32x2 y[4]; u32 o[4];
    EACH4((y[j] = f32x2{y0[j], y1[j]}));
    y[0] = fma2(a[0], h0, y[0]); y[1] = fma2(a[1], h0, y[1]); y[2] = fma2(a[2], h0, y[2]);
    y[1] = fma2(a[0], h1, y[1]); y[2] = fma2(a[1], h1, y[2]); y[2] = fma2(a[0], h2, y[2]);
    EACH4(y[j] = rb2(y[j]) + rb2(a[j] * d2));
    EACH4(o[j] = pk2(y[j].x, y[j].y));
    st128(u32x4{o[0], o[1], o[2], o[3]}, rs_ws, (tid >> 4) * 65536 + (tid & 15) * 16, OFF_Y + b * 256);
  }
  cnt_add(C_S1);
#pragma unroll
  for (int n = HL0; n < HLA; ++n) load_h(n);
  STAMP(8);
  const int tk = tid >> 5, cq = tid & 31;
  const u32 fy_off = OFF_FY + (ct * 32 + 4 * w + (l & 3)) * FS;
  u64 fyv; u32x4 yl;
  f32x2 xn[4];
#pragma unroll
  for (int e = 0; e < 4; ++e) {
    const u32 sb = __builtin_bit_cast(u32, bperm((e * 16 + tk) * 4, __builtin_bit_cast(float, stf)));
    xn[e] = rb2(rb2(bf2(qv[e]) - sp2(bflo(sb))) * sp2(__builtin_amdgcn_rcpf(bfhi(sb))));
    asm volatile("" : "+v"(xn[e]));
  }
  SB();
  u32 ra0 = tk * 256 + (((cq >> 1) ^ (tk & 7)) << 4) + (cq & 1) * 8, ra1 = ra0 + 65536;
  asm volatile("" : "+v"(ra1));
  f32x2 g[4], pr[8], zz;
  auto gate = [&](int e, int part) AI {
    if (part == 0) {
      cnt_wait(C_T + 4 * e, 8);
#pragma unroll
      for (int i = 0; i < 8; ++i) pr[i] = *(const volatile LAS f32x2*)((i < 4 ? ra0 : ra1) + e * 16384 + (i & 3) * 4096);
    } else if (part == 1) {
      zz = (((pr[0] + pr[1]) + (pr[2] + pr[3])) + ((pr[4] + pr[5]) + (pr[6] + pr[7]))) + bf2(b2v);
      asm volatile("" : "+v"(zz));
    } else { g[e] = zz * rcpx2(exp2x2(zz * sp2(-1.4426950408889634f)) + sp2(1.f)); asm volatile("" : "+v"(g[e])); }
    SB();
  };
  f32x4 a2[2][4];
  const u32 wa2 = (w < 4 ? 65536 : 0) + gw * 4096 + n16 * 256;
  auto pwrite = [&](int t, int nt) AI { lds_st<f32x4>(t * 16384 + wa2 + (((4 * nt + g4) ^ (n16 & 7)) << 4), a2[t & 1][nt]); SB(); };
#pragma unroll
  for (int t = 0; t < 4; ++t) {
#pragma unroll
    for (int i = 0; i < 7; ++i) {
      if (t == 0 && i == YDR) { waitv<0>(); arrive_flag(ws, C_Y, 8, OFF_FY + b * FS, tag, l); SB(); }
      if (t == 1 && i == 0) cnt_wait(C_S1, 8);
#pragma unroll
      for (int nt = 0; nt < 4; ++nt) a2[t & 1][nt] = mfma(w2f[i * 4 + nt], hf[t * 7 + i], i ? a2[t & 1][nt] : f32x4{0, 0, 0, 0});
      SB();
      if (t > 0 && i < 4) pwrite(t - 1, i);
      if (t > 0 && i == 4) cnt_add(C_T + 4 * (t - 1));
      load_h(t * 7 + i + HLA);
      if (t >= 2 && i >= 4) gate(t - 2, i - 4);
      if (t * 7 + i + HLA == 28) { fyv = flag_ld(ws, fy_off); SB(); }
      if (t == 3 && i == 0) {
        STAMP2(32);
        flag_chk(fyv, ws, fy_off, tag);
        STAMP2(33);
        yl = ld128(rs_ws, tid * 16, OFF_Y + rt * 65536 + ct * 8192);
        SB();
      }
    }
    STAMP(9 + t);
  }
#pragma unroll
  for (int nt = 0; nt < 4; ++nt) pwrite(3, nt);
#pragma unroll
  for (int e = 0; e < 4; ++e) lds_st<u32>(L_YS + ((tid >> 4) * 65 + (tid & 15) * 4 + e) * 4, yl[e]);
  cnt_add(C_T + 12);
  gate(2, 0); gate(2, 1); gate(2, 2);
  STAMP2(36);
  gate(3, 0);
  STAMP(13);
  {
    u32 yv[4];
#pragma unroll
    for (int e = 0; e < 4; ++e) yv[e] = lds_ld<u32>(L_YS + (cq * 65 + e * 16 + tk) * 4);
    SB();
    gate(3, 1); gate(3, 2);
    EACH4(xn[j] = rb2(xn[j] * bf2(yv[j])));
    EACH4(g[j] = fma2(xn[j], g[j], bf2(qv[j])));
    u16* op = out + (rt * 64 + tk) * D_ + ct * 64 + cq * 2;
    EACH4(*(GAS u32*)(op + j * 16 * D_) = pk2(g[j].x, g[j].y));
  }
  if (b == 0 && w == 0) {
#pragma unroll 1
    for (int i = 0; i < 4; ++i) poll_flags(ws, OFF_FY + (i * 64 + l) * FS, tag);
    if (l == 0) set_flag(ws, OFF_E, tag);
  }
  STAMP(14);
  STAMP_END;
}

extern "C" void kernel_launch(void* const* d_in, const int* in_sizes, int n_in, void* d_out, int out_size, void* d_ws, size_t ws_size,
                              hipStream_t stream) {
  char* ws = (char*)d_ws + (WS_TAIL ? ((ws_size - WS_BYTES) & ~(size_t)4095) : 0);
  fused_kernel<<<256, 512, 0, stream>>>((const u16*)d_in[0], (const u16*)d_in[1], (const u16*)d_in[2], (const u16*)d_in[3], (const u16*)d_in[4],
                                        (const u16*)d_in[5], (const u16*)d_in[7], (const u16*)d_in[8], (const u16*)d_in[9], (const u16*)d_in[10],
                                        (u16*)d_out, ws);
}
```

```cpp
#include <hip/hip_runtime.h>
typedef unsigned short u16;
typedef unsigned u32;
typedef unsigned long long u64;
typedef __bf16 bf16x8 __attribute__((ext_vector_type(8)));
typedef __bf16 bf16x2 __attribute__((ext_vector_type(2)));
typedef short s16x4 __attribute__((ext_vector_type(4)));
typedef float f32x4 __attribute__((ext_vector_type(4)));
typedef float f32x2 __attribute__((ext_vector_type(2)));
typedef unsigned u32x4 __attribute__((ext_vector_type(4)));
typedef unsigned u32x2 __attribute__((ext_vector_type(2)));
#define LAS __attribute__((address_space(3)))
#define GAS __attribute__((address_space(1)))
#define DI __device__ __forceinline__
#pragma clang diagnostic ignored "-Wint-to-pointer-cast"

#define FSTR 256
constexpr int S_ = 2048, D_ = 512, F_ = 1792;
constexpr u32 OFF_E = 0, OFF_CS = 16384, OFF_ST = 32768, FS = FSTR  , OFF_FU = 65536, OFF_FW = OFF_FU + 256 * FS, OFF_FH = OFF_FW + 256 * FS, OFF_FY = OFF_FH + 256 * FS;
constexpr u32 OFF_U = 1u << 20, OFF_Y = 3u << 20, OFF_W2T = 5u << 20, OFF_H = 7u << 20, WS_BYTES = 14u << 20;
constexpr u32 SLOT = 32768, L_PK = 131072, L_PV = 140288, L_PW = 148480, L_SK = 156928, L_CNT = 157696,
              L_RC = 157824  , L_CC = 158848  , L_CSX = 159872,
              L_HC0 = 131072, L_HC1 = 0, HCS = 4160, L_UR = 147712, URS = 4576, L_CP = 32768, L_YS = 131072, L_QS = 147712;
constexpr u32 C_FULL = L_CNT  , C_FREE = L_CNT + 48  , C_H = L_CNT + 80, C_W = L_CNT + 84, C_U = L_CNT + 88, C_Y = L_CNT + 92,
              C_S1 = L_CNT + 96, C_S2 = L_CNT + 100, C_S3 = L_CNT + 104, C_SA = L_CNT + 36  , C_T = L_CNT + 112  , C_YS = L_CNT + 108, L_B1 = 157440;
#define PSLEEP 8
#define NT_G 0
#define NT_P 1
#define WS_TAIL 0
#define BPRIO 1
#define NWB 28
#define WDR 1
#define SPQ 32
#define KVD -1
#define KROT 2
#define DEEP 0
#define PREPS 6
#define LPRIO 2
#define WGO 5
#define PFIRST 0
#define UDR 2
#define LFIRST 0
#define APRIO 1
#define CPS 5
#define HL0 8
#define YDR 0
#define NOPK 0
#define CPF 1
#define XNF 0
#define HLA 10

#define STAMP(i)
#define STAMP_INIT
#define STAMP_END
#define STAMP2(i)

template <int V> struct IC { static constexpr int v = V; };
template <int I, int N, class F> DI void sfor(F&& f) { if constexpr (I < N) { f(IC<I>{}); sfor<I + 1, N>(f); } }
DI u32x4 mk_srd(const void* p, u32 bytes) { u64 a = (u64)p; return u32x4{(u32)a, (u32)(a >> 32) & 0xffffu, bytes, 0x00020000u}; }
template <int NT> DI void dma16(u32x4 srd, u32 voff, u32 soff, u32 lds) {
  if (NT) asm volatile("s_mov_b32 m0, %3\n\ts_nop 0\n\tbuffer_load_dwordx4 %0, %1, %2 offen nt lds" ::"v"(voff), "s"(srd), "s"(soff), "s"(lds) : "memory");
  else asm volatile("s_mov_b32 m0, %3\n\ts_nop 0\n\tbuffer_load_dwordx4 %0, %1, %2 offen lds" ::"v"(voff), "s"(srd), "s"(soff), "s"(lds) : "memory");
}
template <int NT, int I> DI void dma16g(u32x4 srd, u32 voff, u32 soff, u32 lds) {
  if (I == 0) dma16<NT>(srd, voff, soff, lds);
  else if (NT) asm volatile("buffer_load_dwordx4 %0, %1, %2 offen offset:%3 nt lds" ::"v"(voff), "s"(srd), "s"(soff), "i"(I * 1024) : "memory");
  else asm volatile("buffer_load_dwordx4 %0, %1, %2 offen offset:%3 lds" ::"v"(voff), "s"(srd), "s"(soff), "i"(I * 1024) : "memory");
}
DI void dma16m(u32x4 srd, u32 voff, u32 soff, u32 lds, u64 mask) {
  asm volatile("s_mov_b32 m0, %3\n\ts_mov_b64 exec, %4\n\tbuffer_load_dwordx4 %0, %1, %2 offen lds\n\ts_mov_b64 exec, -1"
               ::"v"(voff), "s"(srd), "s"(soff), "s"(lds), "s"(mask) : "memory");
}
template <int N> DI void waitv() { asm volatile("s_waitcnt vmcnt(%0)" ::"i"(N) : "memory"); }
#define WAITL0() asm volatile("s_waitcnt lgkmcnt(0)" ::: "memory")
#define CFENCE() asm volatile("" ::: "memory")
#define SB() __builtin_amdgcn_sched_barrier(0)
DI void bar() { SB(); CFENCE(); __builtin_amdgcn_s_waitcnt(0xC07F); __builtin_amdgcn_s_barrier(); CFENCE(); SB(); }
DI float bflo(u32 x) { return __uint_as_float(x << 16); }
DI float bfhi(u32 x) { return __uint_as_float(x & 0xffff0000u); }
DI u32 pk2(float a, float b) { f32x2 p = {a, b}; return __builtin_bit_cast(u32, __builtin_convertvector(p, bf16x2)); }
DI float rb(float a) { return bflo(pk2(a, a)); }
DI f32x2 bf2(u32 x) { return f32x2{bflo(x), bfhi(x)}; }
DI f32x2 rb2(f32x2 v) { return bf2(pk2(v.x, v.y)); }
DI f32x2 sp2(float v) { return f32x2{v, v}; }
DI f32x2 exp2x2(f32x2 v) { return f32x2{__builtin_amdgcn_exp2f(v.x), __builtin_amdgcn_exp2f(v.y)}; }
DI f32x2 rcpx2(f32x2 v) { return f32x2{__builtin_amdgcn_rcpf(v.x), __builtin_amdgcn_rcpf(v.y)}; }
DI f32x2 fma2(f32x2 a, f32x2 b, f32x2 c) { return __builtin_elementwise_fma(a, b, c); }
DI float sfma(float a, float b, float c) { return __builtin_fmaf(a, b, c); }
DI float smul(float a, float b) { return a * b; }
DI float sadd(float a, float b) { return a + b; }
DI f32x2 fma2s(f32x2 a, f32x2 b, f32x2 c) { return fma2(a, b, c); }
DI f32x2 mul2s(f32x2 a, f32x2 b) { return a * b; }
DI f32x2 add2s(f32x2 a, f32x2 b) { return a + b; }
template <class T> DI T lds_ld(u32 off) { return *(const LAS T*)(off); }
template <class T> DI void lds_st(u32 off, T v) { *(LAS T*)(off) = v; }
DI u32x2 lds_tr(u32 off) { return __builtin_bit_cast(u32x2, __builtin_amdgcn_ds_read_tr16_b64_v4i16((LAS s16x4*)(off))); }
DI u32x4 lds_tr2(u32 o0, u32 o1) { u32x2 a = lds_tr(o0), b = lds_tr(o1); return u32x4{a.x, a.y, b.x, b.y}; }
DI u32x4 lds_2x64(u32 o) { u32x2 a = *(const volatile LAS u32x2*)(o), b = *(const volatile LAS u32x2*)(o + 8); return u32x4{a.x, a.y, b.x, b.y}; }
DI f32x4 mfma(u32x4 a, u32x4 b, f32x4 c) {
  return __builtin_amdgcn_mfma_f32_16x16x32_bf16(__builtin_bit_cast(bf16x8, a), __builtin_bit_cast(bf16x8, b), c, 0, 0, 0);
}
DI void st128(u32x4 v, __amdgpu_buffer_rsrc_t rs, u32 voff, u32 soff) {
  __builtin_amdgcn_raw_buffer_store_b128(v, rs, voff, soff, 16);
  asm volatile("s_nop 1" ::"v"(v) : "memory");
}
DI u32x4 ld128(__amdgpu_buffer_rsrc_t rs, u32 voff, u32 soff) { return __builtin_amdgcn_raw_buffer_load_b128(rs, voff, soff, 16); }
template <int C> DI float dpp_add(float v) {
  return v + __builtin_bit_cast(float, __builtin_amdgcn_update_dpp(0, __builtin_bit_cast(int, v), C, 0xf, 0xf, true));
}
DI float bperm(int src_lane4, float v) { return __builtin_bit_cast(float, __builtin_amdgcn_ds_bpermute(src_lane4, __builtin_bit_cast(int, v))); }
DI float sel(bool c, float a, float b) { return c ? a : b; }
DI float rl(float v, int i) { return __builtin_bit_cast(float, __builtin_amdgcn_readlane(__builtin_bit_cast(int, v), i)); }
DI float wave_sum(float v) {
  v = dpp_add<0xB1>(v); v = dpp_add<0x4E>(v); v = dpp_add<0x141>(v); v = dpp_add<0x140>(v);
  return (rl(v, 0) + rl(v, 16)) + (rl(v, 32) + rl(v, 48));
}
constexpr int SPIN_CAP = 1 << 16;
DI u64 flag_ld(const char* ws, u32 off) { return __hip_atomic_load((const GAS u64*)(ws + off), __ATOMIC_RELAXED, __HIP_MEMORY_SCOPE_AGENT); }
DI void poll_flags(const char* ws, u32 off, u64 tag) {
#pragma unroll 1
  for (int it = 0; it < SPIN_CAP; ++it) {
    if (__all(flag_ld(ws, off) == tag)) break;
    __builtin_amdgcn_s_sleep(PSLEEP);
  }
  CFENCE();
}
DI void flag_chk(u64 v, const char* ws, u32 off, u64 tag) { if (!__all(v == tag)) poll_flags(ws, off, tag); CFENCE(); }
DI void set_flag(char* ws, u32 off, u64 tag) { __hip_atomic_store((GAS u64*)(ws + off), tag, __ATOMIC_RELAXED, __HIP_MEMORY_SCOPE_AGENT); }
DI void arrive_flag(char* ws, u32 cnt, u32 n, u32 off, u64 tag, int l) {
  if (l == 0) {
    u32 old;
    asm volatile("ds_add_rtn_u32 %0, %1, %2\n\ts_waitcnt lgkmcnt(0)" : "=&v"(old) : "v"(cnt), "v"(1u) : "memory");
    if (old == n - 1) set_flag(ws, off, tag);
  }
  CFENCE();
}
DI void cnt_add(u32 cnt) { asm volatile("s_mov_b64 exec, 1\n\tds_add_u32 %0, %1\n\ts_mov_b64 exec, -1" ::"v"(cnt), "v"(1u) : "memory"); }
DI u32 cnt_ld(u32 cnt) { return *(const volatile LAS u32*)cnt; }
DI void cnt_wait(u32 cnt, u32 n) {
#pragma unroll 1
  for (int it = 0; it < (1 << 22); ++it) { if ((u32)__builtin_amdgcn_readfirstlane(cnt_ld(cnt)) >= n) break; __builtin_amdgcn_s_sleep(1); }
  CFENCE(); SB();
}
DI void group_sync(u32 cnt) { SB(); WAITL0(); cnt_add(cnt); cnt_wait(cnt, 4); }
#define AI __attribute__((always_inline))
#define EACH4(stmt) do { _Pragma("unroll") for (int j = 0; j < 4; ++j) { stmt; } SB(); } while (0)
#define HALF(v, j) (((j) & 1) ? (v).hi : (v).lo)

#define KATTR
__global__ __launch_bounds__(512, 2) KATTR void fused_kernel(const u16* __restrict__ q, const u16* __restrict__ kk, const u16* __restrict__ vv,
                                                       const u16* __restrict__ sk, const u16* __restrict__ ssk, const u16* __restrict__ sD,
                                                       const u16* __restrict__ w1, const u16* __restrict__ b1, const u16* __restrict__ w2,
                                                       const u16* __restrict__ b2, u16* __restrict__ out, char* __restrict__ ws) {
  __shared__ __attribute__((aligned(16))) char sm[163840];
  asm volatile("" ::"v"(sm) : "memory");
  const int tid = threadIdx.x, l = tid & 63;
  const int w = __builtin_amdgcn_readfirstlane(tid >> 6);
  const int b = blockIdx.x, xcd = b & 7, r = b >> 3;
  const bool mainwg = r < 28;
  const int rp = (xcd & 3) * 4 + (r & 3), cc = (xcd >> 2) * 7 + (r >> 2);
  const int rt = (xcd & 3) * 8 + (xcd >> 2) * 4 + (r & 3), ct = r >> 2;
  const int gw = w & 3;
  const int n16 = l & 15, g4 = l >> 4;
  STAMP_INIT
  STAMP(0);
  if (CPF && w == 7) {
    u64 pc; u32 dummy;
    asm volatile("s_getpc_b64 %0" : "=s"(pc));
    asm volatile("global_load_dword %0, %1, %2" : "=v"(dummy) : "v"((u32)((r & 3) * 64 + l) * 128u - 128u * ((r & 3) == 3 && l >= 24 ? l - 24 : 0)), "s"(pc) : "memory");
  }
  if (tid < 8) lds_st<u32x4>(L_CNT + tid * 16, u32x4{0, 0, 0, 0});
  int rp_ = rp, cc_ = cc, rt_ = rt;
  u32 vr = (l >> 3) * 1024 + (l & 7) * 16;
  asm volatile("" : "+s"(rp_), "+s"(cc_), "+s"(rt_), "+v"(vr));
  SB();
  asm volatile("" : "+s"(q), "+s"(kk), "+s"(vv), "+s"(sk), "+s"(ssk), "+s"(sD), "+s"(w1), "+s"(b1), "+s"(w2), "+s"(b2), "+s"(out), "+s"(ws));
  const u32x4 srd_q = mk_srd(q, S_ * D_ * 2);
  const u32x4 srd_w1m = mk_srd((const char*)w1 - F_ * 2, D_ * F_ * 2 + F_ * 2);
  u32x2 ev;
  u64 tag;
  auto load_tag = [&]() AI { asm volatile("s_mov_b64 exec, 1\n\tglobal_load_dwordx2 %0, %1, %2 sc1\n\ts_mov_b64 exec, -1" : "=v"(ev) : "v"(0u), "s"(ws) : "memory"); };
  auto get_tag = [&]() AI {
    SB();
    asm volatile("" : "+v"(ev));
    tag = (((u64)__builtin_amdgcn_readfirstlane(ev.y) << 32) | (u32)__builtin_amdgcn_readfirstlane(ev.x)) + 0x9E3779B97F4A7C15ull;
  };
  const __amdgpu_buffer_rsrc_t rs_ws = __builtin_amdgcn_make_buffer_rsrc(ws, 0, WS_BYTES, 0x00020000);
  u32x4 w2f[28];
  auto load_w2 = [&](int j) AI { w2f[j] = ld128(rs_ws, l * 16 + (j & 3) * 1024, OFF_W2T + (ct * 56 + 7 * w + (j >> 2)) * 4096); SB(); };
  const u32 fw_off = OFF_FW + (4 * w + (ct >> 1)) * FS;
  const u32 fh_off = OFF_FH + ((rt >> 1) * 16 + min(((7 * w) >> 2) + (l & 3), (7 * w + 6) >> 2)) * FS;

  const u32 sx32 = ((2 * g4 + (n16 >> 3)) & 3) << 5, tr0 = (4 * g4 + (n16 >> 2)) * 128 + (l & 3) * 8, vsw = ((l & 7) ^ ((l >> 3) & 6)) << 4;
  u64 fhv;
  const int sp = (r - 28) * 8 + xcd;
  if (w >= 4) {
    u32x4 ha[2]; u32x2 hn[2]; u32 dd;
    auto load_taps = [&]() AI {
      asm volatile("buffer_load_dwordx4 %0, %4, %5, %6 offen nt\n\tbuffer_load_dwordx4 %1, %4, %5, %6 offen offset:1024 nt\n\t"
                   "buffer_load_dwordx2 %2, %4, %5, %6 offen offset:16\n\tbuffer_load_dwordx2 %3, %4, %5, %6 offen offset:1040"
                   : "=&v"(ha[0]), "=&v"(ha[1]), "=&v"(hn[0]), "=&v"(hn[1]) : "v"(l * 16), "s"(mk_srd(ssk, S_ * D_ * 2)), "s"(b * 8192 + gw * 2048) : "memory");
      asm volatile("buffer_load_ushort %0, off, %1, %2" : "=v"(dd) : "s"(mk_srd(sD, D_ * 2)), "s"(b * 4 + (gw >> 1) * 2) : "memory");
    };
    __builtin_amdgcn_s_setprio(LPRIO);
    const u32 fu_off = (l & 8) ? fw_off : OFF_FU + ((b >> 5) * 32 + 8 * gw + (l & 7)) * FS;
    u64 fuv = 0;
    load_tag();
    const u32 vx0 = (l >> 3) * 1024 + (((l & 7) ^ (l >> 3)) << 4), vx1 = vx0 + 8192 - 1024;
    const u32 vwb = (g4 + 1) * (F_ * 2) - 2048, vws = (n16 ^ (g4 << 1)) << 4;
    const u32 vw[4] = {vwb + vws, vwb + vws + 4 * (F_ * 2) - 1024, vwb + (vws ^ 128), vwb + (vws ^ 128) + 4 * (F_ * 2) - 1024};
    const int kc0 = KROT == 1 ? ((r >> 2) + 2 * (r & 3)) & 7 : KROT == 2 ? (((r >> 2) + r) & 1) * 4 : 0;
    auto stage = [&](int s) AI {
      const int kc = (s + kc0) & 7;
      const u32 sx = rp * 131072 + (gw & 1) * 65536 + kc * 128, sw = ((gw & 1) * 32 + kc * 64) * (F_ * 2) + cc * 256, dst = (s & 3) * SLOT + (gw & 1) * 16384;
#pragma unroll
      for (int i = 0; i < 4; ++i) {
        dma16g<NT_G, 0>(srd_q, vx0, sx + i * 16384, dst + i * 4096); dma16g<NT_G, 1>(srd_q, vx1, sx + i * 16384, 0);
        dma16g<NT_G, 2>(srd_w1m, vw[(i & 1) * 2], sw + i * 8 * (F_ * 2), 0); dma16g<NT_G, 3>(srd_w1m, vw[(i & 1) * 2 + 1], sw + i * 8 * (F_ * 2), 0);
      }
    };
    if (!LFIRST) bar();
    if (mainwg) { stage(gw >> 1); if (LFIRST) bar(); stage((gw >> 1) + 2); waitv<16>(); cnt_add(C_FULL + 4 * (gw >> 1)); }
    else {
#pragma unroll
      for (int p = 0; p < 4; ++p) {
        const int hp = gw * 4 + p;
        sfor<0, 4>([&](auto I) AI { dma16g<NT_P, I.v>(mk_srd(w2, hp < 14 ? D_ * F_ * 2 : 0), (l >> 3) * 1024 + vsw,
                                                     ((sp >> 2) * 7 + (hp >> 1)) * 32768 + (sp & 3) * 256 + (hp & 1) * 128 + I.v * 7168, hp * 4096); });
      }
      if (LFIRST) bar();
    }
    waitv<16>();
    get_tag();
    STAMP2(40);
    STAMP2(43);
    if (mainwg) {
      const int s0 = gw >> 1;
      waitv<0>(); cnt_add(C_FULL + 4 * s0 + 8);
      cnt_wait(C_FREE + 4 * s0, 4);
      stage(s0 + 4);
      waitv<0>(); cnt_add(C_FULL + 4 * s0 + 16);
      u32x4 g;
      asm volatile("buffer_load_dwordx4 %0, %1, %2, %3 offen sc1" : "=v"(g) : "v"(l * 16), "s"(mk_srd(ws, WS_BYTES)), "s"(OFF_ST + rp * 2048 + (gw & 1) * 1024) : "memory");
      dma16m(mk_srd(b1, F_ * 2), l * 16, cc * 256, L_B1, 0xffffull);
      cnt_wait(C_FREE + 4 * s0 + 8, 4);
      stage(s0 + 6);
      load_taps();
      waitv<21>();
      STAMP2(41);
      SB();
      asm volatile("" : "+v"(g));
      if (gw < 2) {
        auto g_ok = [&]() AI { return __all((g.y == (u32)tag) & (g.w == (u32)(tag >> 32))); };
        if (!g_ok()) {
#pragma unroll 1
          for (int it = 0; it < SPIN_CAP; ++it) { g = ld128(rs_ws, l * 16, OFF_ST + rp * 2048 + gw * 1024); if (g_ok()) break; __builtin_amdgcn_s_sleep(4); CFENCE(); }
        }
        const float i2 = __builtin_amdgcn_rcpf(bfhi(g.z));
        float ra = __builtin_amdgcn_rcpf(bfhi(g.x)) * i2;
        asm volatile("" : "+v"(ra));
        lds_st<f32x2>(L_RC + (gw * 64 + l) * 8, f32x2{ra, -bflo(g.x) * ra - bflo(g.z) * i2});
      } else lds_st<u32>(L_CC + ((gw & 1) * 64 + l) * 4, (u32)lds_ld<u16>(L_B1 + ((gw & 1) * 64 + l) * 2) << 16);
      fuv = flag_ld(ws, fu_off);
      waitv<6>();
      cnt_add(C_FULL + 4 * s0 + 24);
      STAMP2(42);
    } else {
      waitv<0>();
      SB();
#pragma unroll
      for (int p = 0; p < 4; ++p) {
        const int hp = gw * 4 + p;
        u32x4 wt[4];
#pragma unroll
        for (int nt = 0; nt < 4; ++nt) { const u32 a = hp * 4096 + tr0 + ((nt * 32) ^ sx32); wt[nt] = lds_tr2(a, a + 2048); }
#pragma unroll
        for (int nt = 0; nt < 4; ++nt)
          st128(wt[nt], rs_ws, l * 16, hp < 14 ? OFF_W2T + ((((sp & 3) * 2 + (hp & 1)) * 56 + (sp >> 2) * 7 + (hp >> 1)) * 4 + nt) * 1024 : 0x7f000000u);
      }
      STAMP2(41);
      waitv<0>();
      arrive_flag(ws, C_W, 4, OFF_FW + sp * FS, tag, l);
      STAMP2(42);
      load_taps();
      cnt_wait(C_W, 4);
      __builtin_amdgcn_s_sleep(SPQ);
    }
    STAMP(2);
    if (!mainwg) { cnt_wait(C_U, 4); }
    __builtin_amdgcn_s_setprio(0);
    waitv<0>();
    flag_chk(fuv, ws, fu_off, tag);
    STAMP(3);
    u32x4 uu[2];
#pragma unroll
    for (int i = 0; i < 2; ++i) uu[i] = ld128(rs_ws, l * 16 + i * 1024, OFF_U + b * 8192 + gw * 2048);
    SB();
#pragma unroll
    for (int j = 0; j < NWB; ++j) load_w2(j);
    STAMP2(30);
    if (mainwg) cnt_wait(C_FREE + 4 * CPS, 4);
    asm volatile("" : "+v"(ha[0]), "+v"(ha[1]), "+v"(hn[0]), "+v"(hn[1]), "+v"(dd));
    if (!(gw & 1)) {
      if (l == 0) ha[0].x = (ha[0].x & 0xffff0000u) | (pk2(bflo(ha[0].x) + bflo(dd), 0.f) & 0xffffu);
      const u32 h01 = __builtin_amdgcn_readfirstlane(ha[0].x), h23 = __builtin_amdgcn_readfirstlane(ha[0].y);
      const int c = l >> 1;
      const u32 z = c == 3 ? h01 << 16 : 0u, y = c == 1 ? h01 << 16 : c == 2 ? h01 : c == 3 ? (h01 >> 16) | (h23 << 16) : 0u;
      if (l < 8) lds_st<u32x4>(((gw >> 1) ? L_HC1 : L_HC0) + c * HCS + (l & 1) * 16, (l & 1) ? u32x4{0, 0, z, y} : u32x4{0, 0, 0, 0});
    } else if (l < 30) lds_st<u32x4>(L_UR + (gw >> 1) * URS + 4096 + l * 16, u32x4{0, 0, 0, 0});
#pragma unroll
    for (int i = 0; i < 2; ++i) {
      const u32x4 x = ha[i];
      const u32 o0 = __builtin_amdgcn_alignbit(x.y, x.x, 16), o1 = __builtin_amdgcn_alignbit(x.z, x.y, 16), o2 = __builtin_amdgcn_alignbit(x.w, x.z, 16),
                o3 = __builtin_amdgcn_alignbit(hn[i].x, x.w, 16), o4 = __builtin_amdgcn_alignbit(hn[i].y, hn[i].x, 16);
      const u32 d = ((gw >> 1) ? L_HC1 : L_HC0) + 32 + (gw & 1) * 2048 + i * 1024 + l * 16;
      lds_st<u32x4>(d, x); lds_st<u32x4>(d + HCS, u32x4{o0, o1, o2, o3});
      lds_st<u32x4>(d + 2 * HCS, u32x4{x.y, x.z, x.w, hn[i].x}); lds_st<u32x4>(d + 3 * HCS, u32x4{o1, o2, o3, o4});
    }
#pragma unroll
    for (int i = 0; i < 2; ++i) {
      const u32 a = L_UR + (2044 - 4 * (gw * 128 + i * 64 + l)) * 2;
      lds_st<u32x2>(a, u32x2{uu[i].x, uu[i].y}); lds_st<u32x2>(a + URS, u32x2{uu[i].z, uu[i].w});
    }
    cnt_add(C_S2);
    STAMP(4);
    cnt_wait(C_S2, mainwg && !NOPK ? 8 : 4);
    STAMP(5);
    {
      const int ch = gw & 1, kh = gw >> 1;
      f32x4 ca[8];
#pragma unroll
      for (int i = 0; i < 8; ++i) ca[i] = f32x4{0, 0, 0, 0};
      const u32 ab = (ch ? L_HC1 : L_HC0) + ((n16 + 1) & 3) * HCS + (((n16 + 1) & ~3) + 8 * g4) * 2 + kh * 256;
      const u32 bb = L_UR + ch * URS + (2032 - 1792 - 16 * n16 + 8 * g4) * 2 + kh * 256;
      const u32 pa = L_CP + gw * 8192 + (4 * n16 + (g4 ^ ((n16 >> 1) & 3))) * 16;
      u32x4 AL[2][4], BL[2][4], AH[4], BH[4];
      auto rdp = [&](u32x4& a, u32x4& bq, int k0, int i) AI { SB(); a = lds_2x64(ab + k0 * 64 + i * 512); bq = lds_ld<u32x4>(bb + k0 * 64 + (7 - i) * 512); SB(); };
#pragma unroll
      for (int i = 0; i < 4; ++i) rdp(AL[0][i], BL[0][i], 0, i);
      sfor<0, 4>([&](auto K) AI {
        constexpr int k0 = K.v;
        u32x4 (&A)[4] = AL[k0 & 1]; u32x4 (&B)[4] = BL[k0 & 1];
#pragma unroll
        for (int n = 0; n < 16; ++n) {
          constexpr int oi[16] = {0, 0, 1, 0, 1, 2, 0, 1, 2, 3, 1, 2, 3, 2, 3, 3}, ot[16] = {0, 1, 0, 2, 1, 0, 3, 2, 1, 0, 3, 2, 1, 3, 2, 3};
          ca[oi[n] + ot[n]] = mfma(A[oi[n]], B[ot[n]], ca[oi[n] + ot[n]]);
          if (n < 8 && (n & 1)) rdp(AH[n >> 1], BH[n >> 1], k0, 4 + (n >> 1));
          if (k0 == 3 && n >= 12) { SB(); lds_st<f32x4>(pa + (n - 12) * 1024, ca[n - 12]); SB(); }
        }
        int cn = 0;
#pragma unroll
        for (int T = 0; T < 4; ++T)
#pragma unroll
          for (int i = 0; i <= T; ++i) {
            ca[4 + T] = mfma(A[i], BH[T - i], ca[4 + T]); ca[4 + T] = mfma(AH[T - i], B[i], ca[4 + T]);
            if (k0 < 3 && cn >= 2 && cn < 6) rdp(AL[(k0 + 1) & 1][cn - 2], BL[(k0 + 1) & 1][cn - 2], k0 + 1, cn - 2);
            if (k0 == 3 && T > 0 && i == 1) { SB(); lds_st<f32x4>(pa + (3 + T) * 1024, ca[3 + T]); SB(); }
            ++cn;
          }
        SB();
      });
      lds_st<f32x4>(pa + 7 * 1024, ca[7]);
    }
    cnt_add(C_S3);
    STAMP(6);
    cnt_wait(C_S3, 4);
    {
      const int t2 = tid & 255;
      f32x4 pr[2][4];
#pragma unroll
      for (int i = 0; i < 2; ++i)
#pragma unroll
        for (int p = 0; p < 4; ++p) pr[i][p] = lds_ld<f32x4>(L_CP + ((t2 & ~3) | ((t2 & 3) ^ ((t2 >> 3) & 3))) * 16 + i * 4096 + p * 8192);
      cnt_add(C_S1);
      SB();
#pragma unroll
      for (int i = 0; i < 2; ++i) {
        const f32x4 y0 = pr[i][0] + pr[i][2], y1 = pr[i][1] + pr[i][3];
        st128(u32x4{pk2(y0[0], y1[0]), pk2(y0[1], y1[1]), pk2(y0[2], y1[2]), pk2(y0[3], y1[3])}, rs_ws, (t2 >> 4) * 65536 + (t2 & 15) * 16, OFF_Y + b * 256 + i * 16 * 65536);
      }
    }
    STAMP2(29);
    fhv = flag_ld(ws, fh_off);
    waitv<0>();
    arrive_flag(ws, C_Y, 4, OFF_FY + b * FS, tag, l);
  } else {
    {
      __builtin_amdgcn_s_setprio(3);
      const u32 so = (rt * 64 + gw * 16) * 1024 + ct * 128;
      const bool top = rt == 0 && gw == 0;
      sfor<0, 2>([&](auto I) AI { dma16g<NT_P, I.v>(mk_srd(kk, S_ * D_ * 2), vr, so + I.v * 7168, L_PK + 1024 + gw * 2048); });
      sfor<0, 2>([&](auto I) AI { dma16g<NT_P, I.v>(mk_srd(vv, S_ * D_ * 2), vr, so + I.v * 7168, L_PV + gw * 2048); });
      dma16m(mk_srd(kk, top ? 0 : S_ * D_ * 2), vr, top ? 0 : so - 8192, L_PK + gw * 2048, ~0ull << 40);
      dma16m(mk_srd(sk, D_ * 8), l * 16, ct * 512, L_SK, 0xffffffffull);
    const int srow = rt * 64 + ct * 8 + gw * 2;
    u32x4 x0[2];
      load_tag();
    asm volatile("buffer_load_dwordx4 %0, %2, %3, %4 offen\n\tbuffer_load_dwordx4 %1, %2, %3, %4 offen offset:1024"
                 : "=&v"(x0[0]), "=&v"(x0[1]) : "v"(l * 16), "s"(srd_q), "s"(srow * 1024) : "memory");
      bar();
      waitv<0>();
      get_tag();
      STAMP2(40);
      const int cpl = l & 31, hh = l >> 5;
      const u32x4 sv = lds_ld<u32x4>(L_SK + cpl * 16);
      u32 a = L_PK + (gw * 16 + hh * 8) * 128 + cpl * 4;
      asm volatile("" : "+v"(a));
      u32 kr[11], vq[8];
#pragma unroll
      for (int i = 0; i < 11; ++i) kr[i] = lds_ld<u32>(a + 5 * 128 + i * 128);
#pragma unroll
      for (int i = 0; i < 8; ++i) vq[i] = lds_ld<u32>(a + (L_PV - L_PK) + i * 128);
      SB();
      const f32x2 kT[4] = {f32x2{bflo(sv.x), bflo(sv.z)}, f32x2{bfhi(sv.x), bfhi(sv.z)}, f32x2{bflo(sv.y), bflo(sv.w)}, f32x2{bfhi(sv.y), bfhi(sv.w)}};
      f32x2 kK[11];
#pragma unroll
      for (int i = 0; i < 3; ++i) kK[i] = bf2(kr[i]);
      u32x4 uo[2];
#pragma unroll
      for (int u = 0; u < 2; ++u) {
        f32x2 ka[4];
        EACH4(kK[4 * u + 3 + j] = bf2(kr[4 * u + 3 + j]));
        EACH4(ka[j] = kK[4 * u + j] * kT[3]); EACH4(ka[j] = fma2(kK[4 * u + j + 1], kT[2], ka[j]));
        EACH4(ka[j] = fma2(kK[4 * u + j + 2], kT[1], ka[j])); EACH4(ka[j] = fma2(kK[4 * u + j + 3], kT[0], ka[j]));
        EACH4(ka[j] = rb2(ka[j])); EACH4(ka[j] *= bf2(vq[4 * u + j]));
        uo[u] = u32x4{pk2(ka[3].x, ka[2].x), pk2(ka[1].x, ka[0].x), pk2(ka[3].y, ka[2].y), pk2(ka[1].y, ka[0].y)};
        SB();
      }
#pragma unroll
      for (int u = 0; u < 2; ++u) lds_st<u32x4>(L_PV + gw * 2048 + cpl * 64 + (((hh * 2 + u) ^ ((cpl >> 1) & 3)) << 4), uo[u]);
#pragma unroll
      for (int i = 0; i < 2; ++i) uo[i] = lds_ld<u32x4>(L_PV + gw * 2048 + i * 1024 + (l >> 2) * 64 + (((l & 3) ^ ((l >> 3) & 3)) << 4));
#pragma unroll
      for (int i = 0; i < 2; ++i) st128(uo[i], rs_ws, (l >> 2) * 8192 + (l & 3) * 16, OFF_U + ((ct * 32 + 16 * i) * 512 + rt * 16 + gw * 4) * 16);
    {
      asm volatile("" : "+v"(x0[0]), "+v"(x0[1]));
      float c0[2], sa[2], sq[2];
#pragma unroll
      for (int i = 0; i < 2; ++i) {
        c0[i] = bflo(__builtin_amdgcn_readfirstlane(x0[i].x));
        const f32x2 d[4] = {bf2(x0[i].x) - sp2(c0[i]), bf2(x0[i].y) - sp2(c0[i]), bf2(x0[i].z) - sp2(c0[i]), bf2(x0[i].w) - sp2(c0[i])};
        const f32x2 s2 = (d[0] + d[1]) + (d[2] + d[3]), q2 = fma2(d[0], d[0], d[1] * d[1]) + fma2(d[2], d[2], d[3] * d[3]);
        sa[i] = s2.x + s2.y; sq[i] = q2.x + q2.y;
      }
#define RED4(C) do { sa[0] = dpp_add<C>(sa[0]); sq[0] = dpp_add<C>(sq[0]); sa[1] = dpp_add<C>(sa[1]); sq[1] = dpp_add<C>(sq[1]); SB(); } while (0)
      SB(); RED4(0xB1); RED4(0x4E); RED4(0x141); RED4(0x140); RED4(0x142); RED4(0x143);
      const bool odd = l & 1;
      const float md = sel(odd, sa[1], sa[0]) * (1.f / D_), mean = sel(odd, c0[1], c0[0]) + md, var = fmaxf(sel(odd, sq[1], sq[0]) * (1.f / D_) - md * md, 0.f);
      const float m1 = rb(mean), sd1 = rb(__builtin_amdgcn_sqrtf(rb(rb(var) + 1e-5f)));
      const float i1 = __builtin_amdgcn_rcpf(sd1);
      const float m2 = (mean - m1) * i1, sd2 = __builtin_amdgcn_sqrtf(rb(rb(var * i1 * i1) + 1e-5f));
      if (l >= 62) st128(u32x4{pk2(m1, sd1), (u32)tag, pk2(m2, sd2), (u32)(tag >> 32)}, rs_ws, (l - 62) * 16, OFF_ST + srow * 16);
    }

      __builtin_amdgcn_s_setprio(APRIO);
      if (!mainwg) { waitv<0>(); arrive_flag(ws, C_U, 4, OFF_FU + (ct * 32 + rt) * FS, tag, l); STAMP2(42); cnt_wait(C_W, 4); __builtin_amdgcn_s_sleep(SPQ); }
    }
    if (mainwg) {
      const int th = gw >> 1, fh = gw & 1;
      f32x4 acc[4][4];
#pragma unroll
      for (int i = 0; i < 16; ++i) acc[i >> 2][i & 3] = f32x4{0, 0, 0, 0};
      const u32 xa = th * 16384 + n16 * 128 + ((g4 ^ (n16 & 7)) << 4);
      const u32 wa = g4 * 4096 + 2048 + (n16 >> 2) * 256 + (((4 * fh + 2 * th) ^ ((n16 >> 2) | ((g4 & 1) << 2))) << 5) + (l & 3) * 8;
      u32 ad[2][6];
#pragma unroll
      for (int h = 0; h < 2; ++h) {
#pragma unroll
        for (int e = 0; e < 4; ++e) ad[h][e] = (wa ^ (e * 32)) + h * 65536;
        ad[h][4] = xa + h * 65536; ad[h][5] = (xa ^ 64) + h * 65536;
        asm volatile("" : "+v"(ad[h][0]), "+v"(ad[h][1]), "+v"(ad[h][2]), "+v"(ad[h][3]), "+v"(ad[h][4]), "+v"(ad[h][5]));
      }
      u32x4 xf[2][4], wf[2][4];
      f32x4 cf[4], bf[4]; f32x2 rc[4];
      auto rd = [&](int hs, int p) AI {
        const int s = hs >> 1, ks = hs & 1, hi = (s >> 1) & 1, e = p >> 1;
        const u32 so = (s & 1) * SLOT;
        if (hs > 15) {
          if (p < 4) bf[p] = lds_ld<f32x4>(L_CC + fh * 256 + ((p * 64) ^ (th * 128)) + g4 * 16);
          else rc[p - 4] = lds_ld<f32x2>(L_RC + (th * 64 + (p - 4) * 16 + n16) * 8);
        } else if (!(p & 1)) { const u32 a = ad[hi][e] + so + ks * 16384; wf[ks][e] = lds_tr2(a, a + 1024); }
        else xf[ks][e] = lds_ld<u32x4>(ad[hi][4 + ks] + so + e * 4096);
      };
      u32 fullv;
      u32x4 ones = {0x3f803f80u, 0x3f803f80u, 0x3f803f80u, 0x3f803f80u};
      asm volatile("" : "+v"(ones));
#pragma unroll
      for (int i = 0; i < 4; ++i) cf[i] = f32x4{0, 0, 0, 0};
      auto half = [&](int hs) AI {
        const int ks = hs & 1;
        sfor<0, 16>([&](auto J) AI {
          constexpr int j = J.v, o = j < 4 ? (j & 1) * 4 + (j >> 1) : j < 9 ? (j == 4 ? 8 : j == 5 ? 9 : j == 6 ? 2 : j == 7 ? 6 : 10) : (j < 12 ? 12 + (j - 9) : (j - 12) * 4 + 3);
          acc[o >> 2][o & 3] = mfma(wf[ks][o >> 2], xf[ks][o & 3], acc[o >> 2][o & 3]);
          SB();
          if (j < 12 && j % 3 != 1) rd(hs + 1, (j / 3) * 2 + (j % 3) / 2);
          if (ks == 0 && j == 12) cnt_add(C_FREE + 2 * hs);
          if (ks == 0 && j == 13 && hs < 14) fullv = cnt_ld(C_FULL + 2 * hs + 4);
          SB();
        });
        cf[0] = mfma(wf[ks][0], ones, cf[0]); cf[1] = mfma(wf[ks][1], ones, cf[1]);
        SB();
      };
      STAMP2(34);
      cnt_wait(C_FULL, 2);
      STAMP2(35);
#pragma unroll
      for (int p = 0; p < 8; ++p) rd(0, p);
      SB();
      sfor<0, 8>([&](auto S) AI {
        constexpr int s = S.v;
        half(2 * s);
        if (s < 7 && (u32)__builtin_amdgcn_readfirstlane(fullv) < 2) cnt_wait(C_FULL + 4 * s + 4, 2);
        CFENCE(); SB();
        if (s == UDR) { waitv<0>(); arrive_flag(ws, C_U, 4, OFF_FU + (ct * 32 + rt) * FS, tag, l); }
        STAMP2(16 + s);
        half(2 * s + 1);
      });
      STAMP(2);
      __builtin_amdgcn_s_setprio(2);
      if (n16 == 0) { lds_st<f32x4>(L_CSX + gw * 128 + g4 * 16, cf[0]); lds_st<f32x4>(L_CSX + gw * 128 + 64 + g4 * 16, cf[1]); }
      cnt_add(C_SA + fh * 4);
      const float c1 = -2.f * 0.7978845608028654f * 1.4426950408889634f, c2 = c1 * 0.044715f;
      const u32 hso = OFF_H + ((rp * 8 + th * 4) * 56 + cc * 4 + fh * 2) * 1024;
#pragma unroll
      for (int u = 0; u < 8; ++u) {
        const int tt = u & 3, kp = u >> 2;
        if (u == 3) { cnt_wait(C_SA + fh * 4, 2); cf[2] = lds_ld<f32x4>(L_CSX + (gw ^ 2) * 128 + g4 * 16); cf[3] = lds_ld<f32x4>(L_CSX + (gw ^ 2) * 128 + 64 + g4 * 16); SB(); }
        f32x2 x[4], p[4]; u32 hp[4];
        EACH4(x[j] = fma2(HALF(cf[kp * 2 + (j >> 1)], j), sp2(rc[tt].y), HALF(bf[kp * 2 + (j >> 1)], j)));
        EACH4(x[j] = fma2(HALF(acc[kp * 2 + (j >> 1)][tt], j), sp2(rc[tt].x), x[j]));
        EACH4(p[j] = x[j] * x[j]);
        EACH4(p[j] = fma2(p[j], sp2(c2), sp2(c1)));
        EACH4(p[j] = p[j] * x[j]);
        EACH4(p[j] = exp2x2(p[j]));
        EACH4(p[j] = p[j] + sp2(1.f));
        EACH4(p[j] = rcpx2(p[j]));
        EACH4(p[j] = p[j] * x[j]);
        EACH4(hp[j] = pk2(p[j].x, p[j].y));
        st128(u32x4{hp[0], hp[1], hp[2], hp[3]}, rs_ws, l * 16, hso + tt * 56 * 1024 + ((kp * 1024) ^ (th * 1024)));
        SB();
      }
      if (!NOPK) cnt_add(C_S2);
      STAMP(4);
    }
    if (!mainwg) __builtin_amdgcn_s_sleep(SPQ);
    poll_flags(ws, fw_off, tag);
    waitv<0>();
    if (mainwg) arrive_flag(ws, C_H, 4, OFF_FH + (rp * 16 + cc) * FS, tag, l);
    __builtin_amdgcn_s_setprio(0);
    STAMP(5);
#pragma unroll
    for (int j = 0; j < 28; ++j) load_w2(j);
    STAMP(6);
    if (!mainwg) {
      if (b == 255 && w == 0) {
#pragma unroll 1
        for (int i = 0; i < 5; ++i) poll_flags(ws, i < 4 ? OFF_FU + (i * 64 + l) * FS : OFF_FW + (l & 31) * FS, tag);
        if (l == 0) set_flag(ws, OFF_E, tag);
      }
      cnt_wait(C_S3, 4);
    }
    fhv = flag_ld(ws, fh_off);
  }
  flag_chk(fhv, ws, fh_off, tag);
  STAMP(7);
  u32x4 hf[28];
  auto load_h = [&](int n) AI { if (n < 28) { hf[n] = ld128(rs_ws, l * 16, OFF_H + ((rt * 4 + n / 7) * 56 + 7 * w + n % 7) * 1024); SB(); } };
  const int t2 = tid & 255;
#pragma unroll
  for (int n = 0; n < HLA; ++n) load_h(n);
  STAMP(8);
  const int tk = t2 >> 4;
  const u32 fy_off = OFF_FY + (ct * 32 + 8 * gw + (l & 7)) * FS;
  u64 fyv; u32x2 qv[4], b2v; u32 stf;
  f32x4 a2[2][4];
  const u32 wa2 = (w < 4 ? 65536 : 0) + gw * 4096 + n16 * 256;
  auto pwrite = [&](int t, int nt) AI { lds_st<f32x4>(t * 16384 + wa2 + (((4 * nt + g4) ^ (n16 & 7)) << 4), a2[t & 1][nt]); SB(); };
#pragma unroll
  for (int t = 0; t < 4; ++t) {
#pragma unroll
    for (int i = 0; i < 7; ++i) {
      if (t == 1 && i == 0 && (w >= 4 || mainwg)) cnt_wait(w < 4 ? C_FREE + 28 : C_S1, 4);
#pragma unroll
      for (int nt = 0; nt < 4; ++nt) a2[t & 1][nt] = mfma(w2f[i * 4 + nt], hf[t * 7 + i], i ? a2[t & 1][nt] : f32x4{0, 0, 0, 0});
      SB();
      if (t > 0 && i < 4) pwrite(t - 1, i);
      if (t > 0 && i == 4) cnt_add(C_T + 4 * (t - 1));
      load_h(t * 7 + i + HLA);
      if (t == 3 && i == 0) {
        const __amdgpu_buffer_rsrc_t rq = __builtin_amdgcn_make_buffer_rsrc((void*)q, 0, w < 4 ? S_ * D_ * 2 : 0, 0x00020000);
#pragma unroll
        for (int e = 0; e < 4; ++e) qv[e] = __builtin_amdgcn_raw_buffer_load_b64(rq, tk * 1024 + n16 * 8, (rt * 64 + e * 16) * 1024 + ct * 128, 0);
        stf = __builtin_amdgcn_raw_buffer_load_b32(rs_ws, l * 16, OFF_ST + rt * 1024, 16);
        b2v = *(const GAS u32x2*)(b2 + ct * 64 + n16 * 4);
        CFENCE(); SB();
      }
      if (t == 3 && i == 4) { fyv = flag_ld(ws, fy_off); SB(); }
    }
    STAMP(9 + t);
  }
#pragma unroll
  for (int nt = 0; nt < 4; ++nt) pwrite(3, nt);
  cnt_add(C_T + 12);
  if (w < 4) {
    u32 ra0 = tk * 256 + ((n16 ^ (tk & 7)) << 4), ra1 = ra0 + 65536;
    asm volatile("" : "+v"(ra1));
    f32x4 pr[8];
    auto rdp = [&](int e) AI {
      cnt_wait(C_T + 4 * e, 8);
#pragma unroll
      for (int i = 0; i < 8; ++i) pr[i] = lds_ld<f32x4>((i < 4 ? ra0 : ra1) + e * 16384 + (i & 3) * 4096);
      SB();
    };
    STAMP2(32);
    flag_chk(fyv, ws, fy_off, tag);
    STAMP2(33);
    u32x4 yl[2];
#pragma unroll
    for (int i = 0; i < 2; ++i) yl[i] = ld128(rs_ws, l * 16 + i * 1024, OFF_Y + rt * 65536 + ct * 8192 + gw * 2048);
    SB();
    f32x2 xn[4][2];
    {
      u32 sb[4];
      EACH4(sb[j] = __builtin_bit_cast(u32, bperm((j * 16 + tk) * 4, __builtin_bit_cast(float, stf))));
      float iv[4];
      EACH4(iv[j] = __builtin_amdgcn_rcpf(bfhi(sb[j])));
      EACH4((xn[j][0] = bf2(qv[j].x) - sp2(bflo(sb[j])), xn[j][1] = bf2(qv[j].y) - sp2(bflo(sb[j]))));
      EACH4((xn[j][0] *= sp2(iv[j]), xn[j][1] *= sp2(iv[j])));
    }
    const f32x2 bb[2] = {bf2(b2v.x), bf2(b2v.y)};
    cnt_wait(C_S3, 4);
#pragma unroll
    for (int i = 0; i < 2; ++i)
#pragma unroll
      for (int e = 0; e < 2; ++e) lds_st<u32x2>(L_YS + ((gw * 8 + i * 4 + g4) * 66 + n16 * 4 + e * 2) * 4, e ? yl[i].hi : yl[i].lo);
    cnt_add(C_YS);
    cnt_wait(C_YS, 4);
    u32 yv[4][2];
    EACH4((yv[j][0] = lds_ld<u32>(L_YS + (n16 * 132 + j * 16 + tk) * 4), yv[j][1] = lds_ld<u32>(L_YS + (n16 * 132 + 66 + j * 16 + tk) * 4)));
    rdp(0);
    EACH4((xn[j][0] *= bf2(yv[j][0]), xn[j][1] *= bf2(yv[j][1])));
    STAMP2(36);
    u16* op = out + (rt * 64 + tk) * D_ + ct * 64 + n16 * 4;
#pragma unroll
    for (int e = 0; e < 4; ++e) {
      const f32x4 z4 = ((pr[0] + pr[1]) + (pr[2] + pr[3])) + ((pr[4] + pr[5]) + (pr[6] + pr[7]));
      f32x2 z[2] = {z4.lo + bb[0], z4.hi + bb[1]}, p[2];
      asm volatile("" : "+v"(z[0]), "+v"(z[1]));
      SB();
      if (e < 3) rdp(e + 1);
      if (e == 2) STAMP(13);
#pragma unroll
      for (int j = 0; j < 2; ++j) p[j] = exp2x2(z[j] * sp2(-1.4426950408889634f));
#pragma unroll
      for (int j = 0; j < 2; ++j) { z[j] *= xn[e][j]; p[j] = rcpx2(p[j] + sp2(1.f)); }
#pragma unroll
      for (int j = 0; j < 2; ++j) p[j] = fma2(z[j], p[j], bf2(qv[e][j]));
      *(GAS u32x2*)(op + e * 16 * D_) = u32x2{pk2(p[0].x, p[0].y), pk2(p[1].x, p[1].y)};
      SB();
    }
  }
  STAMP(14);
  STAMP_END;
}

extern "C" void kernel_launch(void* const* d_in, const int* in_sizes, int n_in, void* d_out, int out_size, void* d_ws, size_t ws_size,
                              hipStream_t stream) {
  char* ws = (char*)d_ws + (WS_TAIL ? ((ws_size - WS_BYTES) & ~(size_t)4095) : 0);
  fused_kernel<<<256, 512, 0, stream>>>((const u16*)d_in[0], (const u16*)d_in[1], (const u16*)d_in[2], (const u16*)d_in[3], (const u16*)d_in[4],
                                        (const u16*)d_in[5], (const u16*)d_in[7], (const u16*)d_in[8], (const u16*)d_in[9], (const u16*)d_in[10],
                                        (u16*)d_out, ws);
}
```

```cpp
#include <hip/hip_runtime.h>
typedef unsigned short u16;
typedef unsigned u32;
typedef unsigned long long u64;
typedef __bf16 bf16x8 __attribute__((ext_vector_type(8)));
typedef __bf16 bf16x2 __attribute__((ext_vector_type(2)));
typedef short s16x4 __attribute__((ext_vector_type(4)));
typedef float f32x4 __attribute__((ext_vector_type(4)));
typedef float f32x2 __attribute__((ext_vector_type(2)));
typedef unsigned u32x4 __attribute__((ext_vector_type(4)));
typedef unsigned u32x2 __attribute__((ext_vector_type(2)));
#define LAS __attribute__((address_space(3)))
#define GAS __attribute__((address_space(1)))
#define DI __device__ __forceinline__
#pragma clang diagnostic ignored "-Wint-to-pointer-cast"

#define FSTR 256
constexpr int S_ = 2048, D_ = 512, F_ = 1792;
constexpr u32 OFF_E = 0, OFF_CS = 16384, OFF_ST = 32768, FS = FSTR  , OFF_FU = 65536, OFF_FW = OFF_FU + 256 * FS, OFF_FH = OFF_FW + 256 * FS, OFF_FY = OFF_FH + 256 * FS;
constexpr u32 OFF_U = 1u << 20, OFF_Y = 3u << 20, OFF_W2T = 5u << 20, OFF_H = 7u << 20, WS_BYTES = 14u << 20;
constexpr u32 SLOT = 32768, L_PK = 131072, L_PV = 140288, L_PW = 148480, L_SK = 156928, L_CNT = 157696,
              L_RC = 157824  , L_CC = 158848  , L_CSX = 159872,
              L_HC0 = 131072, L_HC1 = 0, HCS = 4160, L_UR = 147712, URS = 4576, L_CP = 32768, L_YS = 131072, L_QS = 147712;
constexpr u32 C_FULL = L_CNT  , C_FREE = L_CNT + 48  , C_H = L_CNT + 80, C_W = L_CNT + 84, C_U = L_CNT + 88, C_Y = L_CNT + 92,
              C_S1 = L_CNT + 96, C_S2 = L_CNT + 100, C_S3 = L_CNT + 104, C_SA = L_CNT + 36  , C_T = L_CNT + 112  , L_B1 = 157440;
#define PSLEEP 8
#define NT_G 0
#define NT_P 1
#define WS_TAIL 0
#define BPRIO 1
#define NWB 28
#define WDR 1
#define SPQ 32
#define KVD -1
#define KROT 0
#define DEEP 0
#define PREPS 6
#define LPRIO 2
#define WGO 5
#define PFIRST 0
#define UDR 2
#define LFIRST 0
#define APRIO 1
#define CPS 5
#define HL0 5
#define YDR 0
#define NOPK 0
#define HLA 12

#define STAMP(i)
#define STAMP_INIT
#define STAMP_END
#define STAMP2(i)

template <int V> struct IC { static constexpr int v = V; };
template <int I, int N, class F> DI void sfor(F&& f) { if constexpr (I < N) { f(IC<I>{}); sfor<I + 1, N>(f); } }
DI u32x4 mk_srd(const void* p, u32 bytes) { u64 a = (u64)p; return u32x4{(u32)a, (u32)(a >> 32) & 0xffffu, bytes, 0x00020000u}; }
template <int NT> DI void dma16(u32x4 srd, u32 voff, u32 soff, u32 lds) {
  if (NT) asm volatile("s_mov_b32 m0, %3\n\ts_nop 0\n\tbuffer_load_dwordx4 %0, %1, %2 offen nt lds" ::"v"(voff), "s"(srd), "s"(soff), "s"(lds) : "memory");
  else asm volatile("s_mov_b32 m0, %3\n\ts_nop 0\n\tbuffer_load_dwordx4 %0, %1, %2 offen lds" ::"v"(voff), "s"(srd), "s"(soff), "s"(lds) : "memory");
}
template <int NT, int I> DI void dma16g(u32x4 srd, u32 voff, u32 soff, u32 lds) {
  if (I == 0) dma16<NT>(srd, voff, soff, lds);
  else if (NT) asm volatile("buffer_load_dwordx4 %0, %1, %2 offen offset:%3 nt lds" ::"v"(voff), "s"(srd), "s"(soff), "i"(I * 1024) : "memory");
  else asm volatile("buffer_load_dwordx4 %0, %1, %2 offen offset:%3 lds" ::"v"(voff), "s"(srd), "s"(soff), "i"(I * 1024) : "memory");
}
DI void dma16m(u32x4 srd, u32 voff, u32 soff, u32 lds, u64 mask) {
  asm volatile("s_mov_b32 m0, %3\n\ts_mov_b64 exec, %4\n\tbuffer_load_dwordx4 %0, %1, %2 offen lds\n\ts_mov_b64 exec, -1"
               ::"v"(voff), "s"(srd), "s"(soff), "s"(lds), "s"(mask) : "memory");
}
template <int N> DI void waitv() { asm volatile("s_waitcnt vmcnt(%0)" ::"i"(N) : "memory"); }
#define WAITL0() asm volatile("s_waitcnt lgkmcnt(0)" ::: "memory")
#define CFENCE() asm volatile("" ::: "memory")
#define SB() __builtin_amdgcn_sched_barrier(0)
DI void bar() { SB(); CFENCE(); __builtin_amdgcn_s_waitcnt(0xC07F); __builtin_amdgcn_s_barrier(); CFENCE(); SB(); }
DI float bflo(u32 x) { return __uint_as_float(x << 16); }
DI float bfhi(u32 x) { return __uint_as_float(x & 0xffff0000u); }
DI u32 pk2(float a, float b) { f32x2 p = {a, b}; return __builtin_bit_cast(u32, __builtin_convertvector(p, bf16x2)); }
DI float rb(float a) { return bflo(pk2(a, a)); }
DI f32x2 bf2(u32 x) { return f32x2{bflo(x), bfhi(x)}; }
DI f32x2 rb2(f32x2 v) { return bf2(pk2(v.x, v.y)); }
DI f32x2 sp2(float v) { return f32x2{v, v}; }
DI f32x2 exp2x2(f32x2 v) { return f32x2{__builtin_amdgcn_exp2f(v.x), __builtin_amdgcn_exp2f(v.y)}; }
DI f32x2 rcpx2(f32x2 v) { return f32x2{__builtin_amdgcn_rcpf(v.x), __builtin_amdgcn_rcpf(v.y)}; }
DI f32x2 fma2(f32x2 a, f32x2 b, f32x2 c) { return __builtin_elementwise_fma(a, b, c); }
DI float sfma(float a, float b, float c) { return __builtin_fmaf(a, b, c); }
DI float smul(float a, float b) { return a * b; }
DI float sadd(float a, float b) { return a + b; }
DI f32x2 fma2s(f32x2 a, f32x2 b, f32x2 c) { return fma2(a, b, c); }
DI f32x2 mul2s(f32x2 a, f32x2 b) { return a * b; }
DI f32x2 add2s(f32x2 a, f32x2 b) { return a + b; }
template <class T> DI T lds_ld(u32 off) { return *(const LAS T*)(off); }
template <class T> DI void lds_st(u32 off, T v) { *(LAS T*)(off) = v; }
DI u32x2 lds_tr(u32 off) { return __builtin_bit_cast(u32x2, __builtin_amdgcn_ds_read_tr16_b64_v4i16((LAS s16x4*)(off))); }
DI u32x4 lds_tr2(u32 o0, u32 o1) { u32x2 a = lds_tr(o0), b = lds_tr(o1); return u32x4{a.x, a.y, b.x, b.y}; }
DI u32x4 lds_2x64(u32 o) { u32x2 a = *(const volatile LAS u32x2*)(o), b = *(const volatile LAS u32x2*)(o + 8); return u32x4{a.x, a.y, b.x, b.y}; }
DI f32x4 mfma(u32x4 a, u32x4 b, f32x4 c) {
  return __builtin_amdgcn_mfma_f32_16x16x32_bf16(__builtin_bit_cast(bf16x8, a), __builtin_bit_cast(bf16x8, b), c, 0, 0, 0);
}
DI void st128(u32x4 v, __amdgpu_buffer_rsrc_t rs, u32 voff, u32 soff) {
  __builtin_amdgcn_raw_buffer_store_b128(v, rs, voff, soff, 16);
  asm volatile("s_nop 1" ::"v"(v) : "memory");
}
DI u32x4 ld128(__amdgpu_buffer_rsrc_t rs, u32 voff, u32 soff) { return __builtin_amdgcn_raw_buffer_load_b128(rs, voff, soff, 16); }
template <int C> DI float dpp_add(float v) {
  return v + __builtin_bit_cast(float, __builtin_amdgcn_update_dpp(0, __builtin_bit_cast(int, v), C, 0xf, 0xf, true));
}
DI float bperm(int src_lane4, float v) { return __builtin_bit_cast(float, __builtin_amdgcn_ds_bpermute(src_lane4, __builtin_bit_cast(int, v))); }
DI float sel(bool c, float a, float b) { return c ? a : b; }
DI float rl(float v, int i) { return __builtin_bit_cast(float, __builtin_amdgcn_readlane(__builtin_bit_cast(int, v), i)); }
DI float wave_sum(float v) {
  v = dpp_add<0xB1>(v); v = dpp_add<0x4E>(v); v = dpp_add<0x141>(v); v = dpp_add<0x140>(v);
  return (rl(v, 0) + rl(v, 16)) + (rl(v, 32) + rl(v, 48));
}
constexpr int SPIN_CAP = 1 << 16;
DI u64 flag_ld(const char* ws, u32 off) { return __hip_atomic_load((const GAS u64*)(ws + off), __ATOMIC_RELAXED, __HIP_MEMORY_SCOPE_AGENT); }
DI void poll_flags(const char* ws, u32 off, u64 tag) {
#pragma unroll 1
  for (int it = 0; it < SPIN_CAP; ++it) {
    if (__all(flag_ld(ws, off) == tag)) break;
    __builtin_amdgcn_s_sleep(PSLEEP);
  }
  CFENCE();
}
DI void flag_chk(u64 v, const char* ws, u32 off, u64 tag) { if (!__all(v == tag)) poll_flags(ws, off, tag); CFENCE(); }
DI void set_flag(char* ws, u32 off, u64 tag) { __hip_atomic_store((GAS u64*)(ws + off), tag, __ATOMIC_RELAXED, __HIP_MEMORY_SCOPE_AGENT); }
DI void arrive_flag(char* ws, u32 cnt, u32 n, u32 off, u64 tag, int l) {
  if (l == 0) {
    u32 old;
    asm volatile("ds_add_rtn_u32 %0, %1, %2\n\ts_waitcnt lgkmcnt(0)" : "=&v"(old) : "v"(cnt), "v"(1u) : "memory");
    if (old == n - 1) set_flag(ws, off, tag);
  }
  CFENCE();
}
DI void cnt_add(u32 cnt) { asm volatile("s_mov_b64 exec, 1\n\tds_add_u32 %0, %1\n\ts_mov_b64 exec, -1" ::"v"(cnt), "v"(1u) : "memory"); }
DI u32 cnt_ld(u32 cnt) { return *(const volatile LAS u32*)cnt; }
DI void cnt_wait(u32 cnt, u32 n) {
#pragma unroll 1
  for (int it = 0; it < (1 << 22); ++it) { if ((u32)__builtin_amdgcn_readfirstlane(cnt_ld(cnt)) >= n) break; __builtin_amdgcn_s_sleep(1); }
  CFENCE(); SB();
}
DI void group_sync(u32 cnt) { SB(); WAITL0(); cnt_add(cnt); cnt_wait(cnt, 4); }
#define AI __attribute__((always_inline))
#define EACH4(stmt) do { _Pragma("unroll") for (int j = 0; j < 4; ++j) { stmt; } SB(); } while (0)
#define HALF(v, j) (((j) & 1) ? (v).hi : (v).lo)

#define KATTR
__global__ __launch_bounds__(512, 2) KATTR void fused_kernel(const u16* __restrict__ q, const u16* __restrict__ kk, const u16* __restrict__ vv,
                                                       const u16* __restrict__ sk, const u16* __restrict__ ssk, const u16* __restrict__ sD,
                                                       const u16* __restrict__ w1, const u16* __restrict__ b1, const u16* __restrict__ w2,
                                                       const u16* __restrict__ b2, u16* __restrict__ out, char* __restrict__ ws) {
  __shared__ __attribute__((aligned(16))) char sm[163840];
  asm volatile("" ::"v"(sm) : "memory");
  const u64 rt0_ = __builtin_amdgcn_s_memrealtime();
  asm volatile("" : "+s"(q), "+s"(kk), "+s"(vv), "+s"(sk), "+s"(ssk), "+s"(sD), "+s"(w1), "+s"(b1), "+s"(w2), "+s"(b2), "+s"(out), "+s"(ws));
  const int tid = threadIdx.x, l = tid & 63;
  const int w = __builtin_amdgcn_readfirstlane(tid >> 6);
  const int b = blockIdx.x, xcd = b & 7, r = b >> 3;
  const bool mainwg = r < 28;
  const int rp = (xcd & 3) * 4 + (r & 3), cc = (xcd >> 2) * 7 + (r >> 2);
  const int rt = (xcd & 3) * 8 + (xcd >> 2) * 4 + (r & 3), ct = r >> 2;
  const int gw = w & 3;
  const int n16 = l & 15, g4 = l >> 4;
  STAMP_INIT
  STAMP(0);
  if (tid < 8) lds_st<u32x4>(L_CNT + tid * 16, u32x4{0, 0, 0, 0});
  const u32x4 srd_q = mk_srd(q, S_ * D_ * 2);
  const u32x4 srd_w1m = mk_srd((const char*)w1 - F_ * 2, D_ * F_ * 2 + F_ * 2);
  u32x2 ev;
  u64 tag;
  auto load_tag = [&]() AI { asm volatile("s_mov_b64 exec, 1\n\tglobal_load_dwordx2 %0, %1, %2 sc1\n\ts_mov_b64 exec, -1" : "=v"(ev) : "v"(0u), "s"(ws) : "memory"); };
  auto get_tag = [&]() AI {
    SB();
    asm volatile("" : "+v"(ev));
    tag = (((u64)__builtin_amdgcn_readfirstlane(ev.y) << 32) | (u32)__builtin_amdgcn_readfirstlane(ev.x)) + 0x9E3779B97F4A7C15ull;
  };
  const __amdgpu_buffer_rsrc_t rs_ws = __builtin_amdgcn_make_buffer_rsrc(ws, 0, WS_BYTES, 0x00020000);
  const u32 vr = (l >> 3) * 1024 + (l & 7) * 16;
  u32x4 w2f[28];
  auto load_w2 = [&](int j) AI { w2f[j] = ld128(rs_ws, l * 16 + (j & 3) * 1024, OFF_W2T + (ct * 56 + 7 * w + (j >> 2)) * 4096); SB(); };
  const u32 fw_off = OFF_FW + (4 * w + (ct >> 1)) * FS;
  const u32 fh_off = OFF_FH + ((rt >> 1) * 16 + min(((7 * w) >> 2) + (l & 3), (7 * w + 6) >> 2)) * FS;

  const u32 sx32 = ((2 * g4 + (n16 >> 3)) & 3) << 5, tr0 = (4 * g4 + (n16 >> 2)) * 128 + (l & 3) * 8, vsw = ((l & 7) ^ ((l >> 3) & 6)) << 4;
  const int sp = (r - 28) * 8 + xcd;
  if (w >= 4) {
    const int t2 = tid & 255;
    u32x4 ha[2]; u32x2 hn[2];
    auto load_taps = [&]() AI {
      asm volatile("buffer_load_dwordx4 %0, %4, %5, %6 offen nt\n\tbuffer_load_dwordx4 %1, %4, %5, %6 offen offset:1024 nt\n\t"
                   "buffer_load_dwordx2 %2, %4, %5, %6 offen offset:16\n\tbuffer_load_dwordx2 %3, %4, %5, %6 offen offset:1040"
                   : "=&v"(ha[0]), "=&v"(ha[1]), "=&v"(hn[0]), "=&v"(hn[1]) : "v"(l * 16), "s"(mk_srd(ssk, S_ * D_ * 2)), "s"(b * 8192 + gw * 2048) : "memory");
    };
    __builtin_amdgcn_s_setprio(LPRIO);
    const u32 fu_off = (l & 8) ? fw_off : OFF_FU + ((b >> 5) * 32 + 8 * gw + (l & 7)) * FS;
    u64 fuv = 0;
    load_tag();
    const u32 vx0 = (l >> 3) * 1024 + (((l & 7) ^ (l >> 3)) << 4), vx1 = vx0 + 8192 - 1024;
    const u32 vwb = (g4 + 1) * (F_ * 2) - 2048, vws = (n16 ^ (g4 << 1)) << 4;
    const u32 vw[4] = {vwb + vws, vwb + vws + 4 * (F_ * 2) - 1024, vwb + (vws ^ 128), vwb + (vws ^ 128) + 4 * (F_ * 2) - 1024};
    auto stage = [&](int s) AI {
      const u32 sx = rp * 131072 + (gw & 1) * 65536 + s * 128, sw = ((gw & 1) * 32 + s * 64) * (F_ * 2) + cc * 256, dst = (s & 3) * SLOT + (gw & 1) * 16384;
#pragma unroll
      for (int i = 0; i < 4; ++i) {
        dma16g<NT_G, 0>(srd_q, vx0, sx + i * 16384, dst + i * 4096); dma16g<NT_G, 1>(srd_q, vx1, sx + i * 16384, 0);
        dma16g<NT_G, 2>(srd_w1m, vw[(i & 1) * 2], sw + i * 8 * (F_ * 2), 0); dma16g<NT_G, 3>(srd_w1m, vw[(i & 1) * 2 + 1], sw + i * 8 * (F_ * 2), 0);
      }
    };
    if (!LFIRST) bar();
    if (mainwg) { stage(gw >> 1); if (LFIRST) bar(); stage((gw >> 1) + 2); waitv<16>(); cnt_add(C_FULL + 4 * (gw >> 1)); }
    else {
#pragma unroll
      for (int p = 0; p < 4; ++p) {
        const int hp = gw * 4 + p;
        sfor<0, 4>([&](auto I) AI { dma16g<NT_P, I.v>(mk_srd(w2, hp < 14 ? D_ * F_ * 2 : 0), (l >> 3) * 1024 + vsw,
                                                     ((sp >> 2) * 7 + (hp >> 1)) * 32768 + (sp & 3) * 256 + (hp & 1) * 128 + I.v * 7168, hp * 4096); });
      }
      if (LFIRST) bar();
    }
    waitv<16>();
    get_tag();
    STAMP2(40);
    STAMP2(43);
    if (mainwg) {
      const int s0 = gw >> 1;
      waitv<0>(); cnt_add(C_FULL + 4 * s0 + 8);
      cnt_wait(C_FREE + 4 * s0, 4);
      stage(s0 + 4);
      waitv<0>(); cnt_add(C_FULL + 4 * s0 + 16);
      u32x4 g;
      asm volatile("buffer_load_dwordx4 %0, %1, %2, %3 offen sc1" : "=v"(g) : "v"(l * 16), "s"(mk_srd(ws, WS_BYTES)), "s"(OFF_ST + rp * 2048 + (gw & 1) * 1024) : "memory");
      dma16m(mk_srd(b1, F_ * 2), l * 16, cc * 256, L_B1, 0xffffull);
      cnt_wait(C_FREE + 4 * s0 + 8, 4);
      stage(s0 + 6);
      load_taps();
      waitv<20>();
      STAMP2(41);
      SB();
      asm volatile("" : "+v"(g));
      if (gw < 2) {
        auto g_ok = [&]() AI { return __all((g.y == (u32)tag) & (g.w == (u32)(tag >> 32))); };
        if (!g_ok()) {
#pragma unroll 1
          for (int it = 0; it < SPIN_CAP; ++it) { g = ld128(rs_ws, l * 16, OFF_ST + rp * 2048 + gw * 1024); if (g_ok()) break; __builtin_amdgcn_s_sleep(4); CFENCE(); }
        }
        const float i2 = __builtin_amdgcn_rcpf(bfhi(g.z));
        float ra = __builtin_amdgcn_rcpf(bfhi(g.x)) * i2;
        asm volatile("" : "+v"(ra));
        lds_st<f32x2>(L_RC + (gw * 64 + l) * 8, f32x2{ra, -bflo(g.x) * ra - bflo(g.z) * i2});
      } else lds_st<u32>(L_CC + ((gw & 1) * 64 + l) * 4, (u32)lds_ld<u16>(L_B1 + ((gw & 1) * 64 + l) * 2) << 16);
      fuv = flag_ld(ws, fu_off);
      waitv<5>();
      cnt_add(C_FULL + 4 * s0 + 24);
      STAMP2(42);
    } else {
      waitv<0>();
      SB();
#pragma unroll
      for (int p = 0; p < 4; ++p) {
        const int hp = gw * 4 + p;
        u32x4 wt[4];
#pragma unroll
        for (int nt = 0; nt < 4; ++nt) { const u32 a = hp * 4096 + tr0 + ((nt * 32) ^ sx32); wt[nt] = lds_tr2(a, a + 2048); }
#pragma unroll
        for (int nt = 0; nt < 4; ++nt)
          st128(wt[nt], rs_ws, l * 16, hp < 14 ? OFF_W2T + ((((sp & 3) * 2 + (hp & 1)) * 56 + (sp >> 2) * 7 + (hp >> 1)) * 4 + nt) * 1024 : 0x7f000000u);
      }
      STAMP2(41);
      waitv<0>();
      arrive_flag(ws, C_W, 4, OFF_FW + sp * FS, tag, l);
      STAMP2(42);
      load_taps();
      cnt_wait(C_W, 4);
      __builtin_amdgcn_s_sleep(SPQ);
    }
    STAMP(2);
    if (!mainwg) { cnt_wait(C_U, 4); }
    __builtin_amdgcn_s_setprio(0);
    waitv<0>();
    flag_chk(fuv, ws, fu_off, tag);
    STAMP(3);
    u32x4 uu[2];
#pragma unroll
    for (int i = 0; i < 2; ++i) uu[i] = ld128(rs_ws, l * 16 + i * 1024, OFF_U + b * 8192 + gw * 2048);
    SB();
#pragma unroll
    for (int j = 0; j < NWB; ++j) load_w2(j);
    STAMP2(30);
    if (mainwg) cnt_wait(C_FREE + 4 * CPS, 4);
    asm volatile("" : "+v"(ha[0]), "+v"(ha[1]), "+v"(hn[0]), "+v"(hn[1]));
#pragma unroll
    for (int i = 0; i < 2; ++i) {
      const u32x4 x = ha[i];
      const u32 o0 = __builtin_amdgcn_alignbit(x.y, x.x, 16), o1 = __builtin_amdgcn_alignbit(x.z, x.y, 16), o2 = __builtin_amdgcn_alignbit(x.w, x.z, 16),
                o3 = __builtin_amdgcn_alignbit(hn[i].x, x.w, 16), o4 = __builtin_amdgcn_alignbit(hn[i].y, hn[i].x, 16);
      const u32 d = ((gw >> 1) ? L_HC1 : L_HC0) + 32 + (gw & 1) * 2048 + i * 1024 + l * 16;
      lds_st<u32x4>(d, x); lds_st<u32x4>(d + HCS, u32x4{o0, o1, o2, o3});
      lds_st<u32x4>(d + 2 * HCS, u32x4{x.y, x.z, x.w, hn[i].x}); lds_st<u32x4>(d + 3 * HCS, u32x4{o1, o2, o3, o4});
    }
    if (l < 19) {
      const int z = gw * 19 + l;
      lds_st<u32x4>(z < 16 ? ((z & 8) ? L_HC1 : L_HC0) + ((z >> 1) & 3) * HCS + (z & 1) * 16 : L_UR + 4096 + (z - 16) * 16 + (z >= 46 ? URS - 480 : 0), u32x4{0, 0, 0, 0});
    }
#pragma unroll
    for (int i = 0; i < 2; ++i) {
      const u32 a = L_UR + (2044 - 4 * (gw * 128 + i * 64 + l)) * 2;
      lds_st<u32x2>(a, u32x2{uu[i].x, uu[i].y}); lds_st<u32x2>(a + URS, u32x2{uu[i].z, uu[i].w});
    }
    cnt_add(C_S2);
    STAMP(4);
    cnt_wait(C_S2, mainwg && !NOPK ? 8 : 4);
    STAMP(5);
    {
      const int ch = gw & 1, kh = gw >> 1;
      f32x4 ca[8];
#pragma unroll
      for (int i = 0; i < 8; ++i) ca[i] = f32x4{0, 0, 0, 0};
      const u32 ab = (ch ? L_HC1 : L_HC0) + ((n16 + 1) & 3) * HCS + (((n16 + 1) & ~3) + 8 * g4) * 2 + kh * 256;
      const u32 bb = L_UR + ch * URS + (2032 - 1792 - 16 * n16 + 8 * g4) * 2 + kh * 256;
      const u32 pa = L_CP + gw * 8192 + (4 * n16 + (g4 ^ ((n16 >> 1) & 3))) * 16;
      u32x4 AL[2][4], BL[2][4], AH[4], BH[4];
      auto rdp = [&](u32x4& a, u32x4& bq, int k0, int i) AI { SB(); a = lds_2x64(ab + k0 * 64 + i * 512); bq = lds_ld<u32x4>(bb + k0 * 64 + (7 - i) * 512); SB(); };
#pragma unroll
      for (int i = 0; i < 4; ++i) rdp(AL[0][i], BL[0][i], 0, i);
      sfor<0, 4>([&](auto K) AI {
        constexpr int k0 = K.v;
        u32x4 (&A)[4] = AL[k0 & 1]; u32x4 (&B)[4] = BL[k0 & 1];
#pragma unroll
        for (int n = 0; n < 16; ++n) {
          constexpr int oi[16] = {0, 0, 1, 0, 1, 2, 0, 1, 2, 3, 1, 2, 3, 2, 3, 3}, ot[16] = {0, 1, 0, 2, 1, 0, 3, 2, 1, 0, 3, 2, 1, 3, 2, 3};
          ca[oi[n] + ot[n]] = mfma(A[oi[n]], B[ot[n]], ca[oi[n] + ot[n]]);
          if (n < 8 && (n & 1)) rdp(AH[n >> 1], BH[n >> 1], k0, 4 + (n >> 1));
          if (k0 == 3 && n >= 12) { SB(); lds_st<f32x4>(pa + (n - 12) * 1024, ca[n - 12]); SB(); }
        }
        int cn = 0;
#pragma unroll
        for (int T = 0; T < 4; ++T)
#pragma unroll
          for (int i = 0; i <= T; ++i) {
            ca[4 + T] = mfma(A[i], BH[T - i], ca[4 + T]); ca[4 + T] = mfma(AH[T - i], B[i], ca[4 + T]);
            if (k0 < 3 && cn >= 2 && cn < 6) rdp(AL[(k0 + 1) & 1][cn - 2], BL[(k0 + 1) & 1][cn - 2], k0 + 1, cn - 2);
            if (k0 == 3 && T > 0 && i == 1) { SB(); lds_st<f32x4>(pa + (3 + T) * 1024, ca[3 + T]); SB(); }
            ++cn;
          }
        SB();
      });
      lds_st<f32x4>(pa + 7 * 1024, ca[7]);
    }
    cnt_add(C_S3);
    STAMP(6);
#pragma unroll
    for (int j = NWB; j < 28; ++j) load_w2(j);
  } else {
    {
      __builtin_amdgcn_s_setprio(3);
      const u32 so = (rt * 64 + gw * 16) * 1024 + ct * 128;
      const bool top = rt == 0 && gw == 0;
      sfor<0, 2>([&](auto I) AI { dma16g<NT_P, I.v>(mk_srd(kk, S_ * D_ * 2), vr, so + I.v * 7168, L_PK + 1024 + gw * 2048); });
      sfor<0, 2>([&](auto I) AI { dma16g<NT_P, I.v>(mk_srd(vv, S_ * D_ * 2), vr, so + I.v * 7168, L_PV + gw * 2048); });
      dma16m(mk_srd(kk, top ? 0 : S_ * D_ * 2), vr, top ? 0 : so - 8192, L_PK + gw * 2048, ~0ull << 40);
      dma16m(mk_srd(sk, D_ * 8), l * 16, ct * 512, L_SK, 0xffffffffull);
    const int srow = rt * 64 + ct * 8 + gw * 2;
    u32x4 x0[2];
      load_tag();
    asm volatile("buffer_load_dwordx4 %0, %2, %3, %4 offen\n\tbuffer_load_dwordx4 %1, %2, %3, %4 offen offset:1024"
                 : "=&v"(x0[0]), "=&v"(x0[1]) : "v"(l * 16), "s"(srd_q), "s"(srow * 1024) : "memory");
      bar();
      waitv<0>();
      get_tag();
      STAMP2(40);
      const int cpl = l & 31, hh = l >> 5;
      const u32x4 sv = lds_ld<u32x4>(L_SK + cpl * 16);
      u32 a = L_PK + (gw * 16 + hh * 8) * 128 + cpl * 4;
      asm volatile("" : "+v"(a));
      u32 kr[11], vq[8];
#pragma unroll
      for (int i = 0; i < 11; ++i) kr[i] = lds_ld<u32>(a + 5 * 128 + i * 128);
#pragma unroll
      for (int i = 0; i < 8; ++i) vq[i] = lds_ld<u32>(a + (L_PV - L_PK) + i * 128);
      SB();
      const f32x2 kT[4] = {f32x2{bflo(sv.x), bflo(sv.z)}, f32x2{bfhi(sv.x), bfhi(sv.z)}, f32x2{bflo(sv.y), bflo(sv.w)}, f32x2{bfhi(sv.y), bfhi(sv.w)}};
      f32x2 kK[11];
#pragma unroll
      for (int i = 0; i < 3; ++i) kK[i] = bf2(kr[i]);
      u32x4 uo[2];
#pragma unroll
      for (int u = 0; u < 2; ++u) {
        f32x2 ka[4];
        EACH4(kK[4 * u + 3 + j] = bf2(kr[4 * u + 3 + j]));
        EACH4(ka[j] = kK[4 * u + j] * kT[3]); EACH4(ka[j] = fma2(kK[4 * u + j + 1], kT[2], ka[j]));
        EACH4(ka[j] = fma2(kK[4 * u + j + 2], kT[1], ka[j])); EACH4(ka[j] = fma2(kK[4 * u + j + 3], kT[0], ka[j]));
        EACH4(ka[j] = rb2(ka[j])); EACH4(ka[j] *= bf2(vq[4 * u + j]));
        uo[u] = u32x4{pk2(ka[3].x, ka[2].x), pk2(ka[1].x, ka[0].x), pk2(ka[3].y, ka[2].y), pk2(ka[1].y, ka[0].y)};
        SB();
      }
#pragma unroll
      for (int u = 0; u < 2; ++u) lds_st<u32x4>(L_PV + gw * 2048 + cpl * 64 + (((hh * 2 + u) ^ ((cpl >> 1) & 3)) << 4), uo[u]);
#pragma unroll
      for (int i = 0; i < 2; ++i) uo[i] = lds_ld<u32x4>(L_PV + gw * 2048 + i * 1024 + (l >> 2) * 64 + (((l & 3) ^ ((l >> 3) & 3)) << 4));
#pragma unroll
      for (int i = 0; i < 2; ++i) st128(uo[i], rs_ws, (l >> 2) * 8192 + (l & 3) * 16, OFF_U + ((ct * 32 + 16 * i) * 512 + rt * 16 + gw * 4) * 16);
    {
      asm volatile("" : "+v"(x0[0]), "+v"(x0[1]));
      float c0[2], sa[2], sq[2];
#pragma unroll
      for (int i = 0; i < 2; ++i) {
        c0[i] = bflo(__builtin_amdgcn_readfirstlane(x0[i].x));
        const f32x2 d[4] = {bf2(x0[i].x) - sp2(c0[i]), bf2(x0[i].y) - sp2(c0[i]), bf2(x0[i].z) - sp2(c0[i]), bf2(x0[i].w) - sp2(c0[i])};
        const f32x2 s2 = (d[0] + d[1]) + (d[2] + d[3]), q2 = fma2(d[0], d[0], d[1] * d[1]) + fma2(d[2], d[2], d[3] * d[3]);
        sa[i] = s2.x + s2.y; sq[i] = q2.x + q2.y;
      }
#define RED4(C) do { sa[0] = dpp_add<C>(sa[0]); sq[0] = dpp_add<C>(sq[0]); sa[1] = dpp_add<C>(sa[1]); sq[1] = dpp_add<C>(sq[1]); SB(); } while (0)
      SB(); RED4(0xB1); RED4(0x4E); RED4(0x141); RED4(0x140); RED4(0x142); RED4(0x143);
      const bool odd = l & 1;
      const float md = sel(odd, sa[1], sa[0]) * (1.f / D_), mean = sel(odd, c0[1], c0[0]) + md, var = fmaxf(sel(odd, sq[1], sq[0]) * (1.f / D_) - md * md, 0.f);
      const float m1 = rb(mean), sd1 = rb(__builtin_amdgcn_sqrtf(rb(rb(var) + 1e-5f)));
      const float i1 = __builtin_amdgcn_rcpf(sd1);
      const float m2 = (mean - m1) * i1, sd2 = __builtin_amdgcn_sqrtf(rb(rb(var * i1 * i1) + 1e-5f));
      if (l >= 62) st128(u32x4{pk2(m1, sd1), (u32)tag, pk2(m2, sd2), (u32)(tag >> 32)}, rs_ws, (l - 62) * 16, OFF_ST + srow * 16);
    }

      __builtin_amdgcn_s_setprio(APRIO);
      if (!mainwg) { waitv<0>(); arrive_flag(ws, C_U, 4, OFF_FU + (ct * 32 + rt) * FS, tag, l); STAMP2(42); cnt_wait(C_W, 4); __builtin_amdgcn_s_sleep(SPQ); }
    }
    if (mainwg) {
      const int th = gw >> 1, fh = gw & 1;
      f32x4 acc[4][4];
#pragma unroll
      for (int i = 0; i < 16; ++i) acc[i >> 2][i & 3] = f32x4{0, 0, 0, 0};
      const u32 xa = th * 16384 + n16 * 128 + ((g4 ^ (n16 & 7)) << 4);
      const u32 wa = g4 * 4096 + 2048 + (n16 >> 2) * 256 + (((4 * fh + 2 * th) ^ ((n16 >> 2) | ((g4 & 1) << 2))) << 5) + (l & 3) * 8;
      u32 ad[2][6];
#pragma unroll
      for (int h = 0; h < 2; ++h) {
#pragma unroll
        for (int e = 0; e < 4; ++e) ad[h][e] = (wa ^ (e * 32)) + h * 65536;
        ad[h][4] = xa + h * 65536; ad[h][5] = (xa ^ 64) + h * 65536;
        asm volatile("" : "+v"(ad[h][0]), "+v"(ad[h][1]), "+v"(ad[h][2]), "+v"(ad[h][3]), "+v"(ad[h][4]), "+v"(ad[h][5]));
      }
      u32x4 xf[2][4], wf[2][4];
      f32x4 cf[4], bf[4]; f32x2 rc[4];
      auto rd = [&](int hs, int p) AI {
        const int s = hs >> 1, ks = hs & 1, hi = (s >> 1) & 1, e = p >> 1;
        const u32 so = (s & 1) * SLOT;
        if (hs > 15) {
          if (p < 4) bf[p] = lds_ld<f32x4>(L_CC + fh * 256 + ((p * 64) ^ (th * 128)) + g4 * 16);
          else rc[p - 4] = lds_ld<f32x2>(L_RC + (th * 64 + (p - 4) * 16 + n16) * 8);
        } else if (!(p & 1)) { const u32 a = ad[hi][e] + so + ks * 16384; wf[ks][e] = lds_tr2(a, a + 1024); }
        else xf[ks][e] = lds_ld<u32x4>(ad[hi][4 + ks] + so + e * 4096);
      };
      u32 fullv;
      u32x4 ones = {0x3f803f80u, 0x3f803f80u, 0x3f803f80u, 0x3f803f80u};
      asm volatile("" : "+v"(ones));
#pragma unroll
      for (int i = 0; i < 4; ++i) cf[i] = f32x4{0, 0, 0, 0};
      auto half = [&](int hs) AI {
        const int ks = hs & 1;
        sfor<0, 16>([&](auto J) AI {
          constexpr int j = J.v, o = j < 4 ? (j & 1) * 4 + (j >> 1) : j < 9 ? (j == 4 ? 8 : j == 5 ? 9 : j == 6 ? 2 : j == 7 ? 6 : 10) : (j < 12 ? 12 + (j - 9) : (j - 12) * 4 + 3);
          acc[o >> 2][o & 3] = mfma(wf[ks][o >> 2], xf[ks][o & 3], acc[o >> 2][o & 3]);
          SB();
          if (j < 12 && j % 3 != 1) rd(hs + 1, (j / 3) * 2 + (j % 3) / 2);
          if (ks == 0 && j == 12) cnt_add(C_FREE + 2 * hs);
          if (ks == 0 && j == 13 && hs < 14) fullv = cnt_ld(C_FULL + 2 * hs + 4);
          SB();
        });
        cf[0] = mfma(wf[ks][0], ones, cf[0]); cf[1] = mfma(wf[ks][1], ones, cf[1]);
        SB();
      };
      STAMP2(34);
      cnt_wait(C_FULL, 2);
      STAMP2(35);
#pragma unroll
      for (int p = 0; p < 8; ++p) rd(0, p);
      SB();
      sfor<0, 8>([&](auto S) AI {
        constexpr int s = S.v;
        half(2 * s);
        if (s < 7 && (u32)__builtin_amdgcn_readfirstlane(fullv) < 2) cnt_wait(C_FULL + 4 * s + 4, 2);
        CFENCE(); SB();
        if (s == UDR) { waitv<0>(); arrive_flag(ws, C_U, 4, OFF_FU + (ct * 32 + rt) * FS, tag, l); }
        STAMP2(16 + s);
        half(2 * s + 1);
      });
      STAMP(2);
      __builtin_amdgcn_s_setprio(2);
      if (n16 == 0) { lds_st<f32x4>(L_CSX + gw * 128 + g4 * 16, cf[0]); lds_st<f32x4>(L_CSX + gw * 128 + 64 + g4 * 16, cf[1]); }
      cnt_add(C_SA + fh * 4);
      const float c1 = -2.f * 0.7978845608028654f * 1.4426950408889634f, c2 = c1 * 0.044715f;
      const u32 hso = OFF_H + ((rp * 8 + th * 4) * 56 + cc * 4 + fh * 2) * 1024;
#pragma unroll
      for (int u = 0; u < 8; ++u) {
        const int tt = u & 3, kp = u >> 2;
        if (u == 3) { cnt_wait(C_SA + fh * 4, 2); cf[2] = lds_ld<f32x4>(L_CSX + (gw ^ 2) * 128 + g4 * 16); cf[3] = lds_ld<f32x4>(L_CSX + (gw ^ 2) * 128 + 64 + g4 * 16); SB(); }
        f32x2 x[4], p[4]; u32 hp[4];
        EACH4(x[j] = fma2(HALF(cf[kp * 2 + (j >> 1)], j), sp2(rc[tt].y), HALF(bf[kp * 2 + (j >> 1)], j)));
        EACH4(x[j] = fma2(HALF(acc[kp * 2 + (j >> 1)][tt], j), sp2(rc[tt].x), x[j]));
        EACH4(p[j] = x[j] * x[j]);
        EACH4(p[j] = fma2(p[j], sp2(c2), sp2(c1)));
        EACH4(p[j] = p[j] * x[j]);
        EACH4(p[j] = exp2x2(p[j]));
        EACH4(p[j] = p[j] + sp2(1.f));
        EACH4(p[j] = rcpx2(p[j]));
        EACH4(p[j] = p[j] * x[j]);
        EACH4(hp[j] = pk2(p[j].x, p[j].y));
        st128(u32x4{hp[0], hp[1], hp[2], hp[3]}, rs_ws, l * 16, hso + tt * 56 * 1024 + ((kp * 1024) ^ (th * 1024)));
        SB();
      }
      if (!NOPK) cnt_add(C_S2);
      STAMP(4);
    }
    if (!mainwg) __builtin_amdgcn_s_sleep(SPQ);
    poll_flags(ws, fw_off, tag);
    waitv<0>();
    if (mainwg) arrive_flag(ws, C_H, 4, OFF_FH + (rp * 16 + cc) * FS, tag, l);
    __builtin_amdgcn_s_setprio(0);
    STAMP(5);
#pragma unroll
    for (int j = 0; j < 28; ++j) load_w2(j);
    STAMP(6);
    if (!mainwg) cnt_wait(C_S3, 4);
  }
  u32 qv[4];
  const u32 b2v = *(const GAS u32*)(b2 + ct * 64 + (tid & 31) * 2), dd = *(const volatile GAS u32*)(sD + 2 * b);
#pragma unroll
  for (int e = 0; e < 4; ++e) qv[e] = *(const GAS u32*)(q + (rt * 64 + e * 16 + (tid >> 5)) * D_ + ct * 64 + (tid & 31) * 2);
  SB();
  poll_flags(ws, fh_off, tag);
  STAMP(7);
  u32x4 hf[28];
  auto load_h = [&](int n) AI { if (n < 28) { hf[n] = ld128(rs_ws, l * 16, OFF_H + ((rt * 4 + n / 7) * 56 + 7 * w + n % 7) * 1024); SB(); } };
  const u32 stf = __builtin_amdgcn_raw_buffer_load_b32(rs_ws, l * 16, OFF_ST + rt * 1024, 16);
#pragma unroll
  for (int n = 0; n < HL0; ++n) load_h(n);
  cnt_wait(C_S3, 4);
  STAMP2(29);
  {
    const u32x2 hc = lds_ld<u32x2>(L_HC0 + 32), hb = lds_ld<u32x2>(L_HC1 + 32);
    const u32x2 u0 = lds_ld<u32x2>(L_UR + (2044 - 4 * tid) * 2), u1 = lds_ld<u32x2>(L_UR + URS + (2044 - 4 * tid) * 2);
    const u32 pa = L_CP + ((tid & ~3) | ((tid & 3) ^ ((tid >> 3) & 3))) * 16;
    f32x4 pr[4];
#pragma unroll
    for (int p = 0; p < 4; ++p) pr[p] = lds_ld<f32x4>(pa + p * 8192);
    SB();
    const f32x2 d2 = bf2(dd), h0 = {bflo(hc.x), bflo(hb.x)}, h1 = {bfhi(hc.x), bfhi(hb.x)}, h2 = {bflo(hc.y), bflo(hb.y)};
    const f32x2 a[4] = {f32x2{bfhi(u0.y), bfhi(u1.y)}, f32x2{bflo(u0.y), bflo(u1.y)}, f32x2{bfhi(u0.x), bfhi(u1.x)}, f32x2{bflo(u0.x), bflo(u1.x)}};
    const f32x4 y0 = pr[0] + pr[2], y1 = pr[1] + pr[3];
    f32x2 y[4]; u32 o[4];
    EACH4((y[j] = f32x2{y0[j], y1[j]}));
    y[0] = fma2(a[0], h0, y[0]); y[1] = fma2(a[1], h0, y[1]); y[2] = fma2(a[2], h0, y[2]);
    y[1] = fma2(a[0], h1, y[1]); y[2] = fma2(a[1], h1, y[2]); y[2] = fma2(a[0], h2, y[2]);
    EACH4(y[j] = rb2(y[j]) + rb2(a[j] * d2));
    EACH4(o[j] = pk2(y[j].x, y[j].y));
    st128(u32x4{o[0], o[1], o[2], o[3]}, rs_ws, (tid >> 4) * 65536 + (tid & 15) * 16, OFF_Y + b * 256);
  }
  cnt_add(C_S1);
#pragma unroll
  for (int n = HL0; n < HLA; ++n) load_h(n);
  STAMP(8);
  const int tk = tid >> 5, cq = tid & 31;
  const u32 fy_off = OFF_FY + (ct * 32 + 4 * w + (l & 3)) * FS;
  u64 fyv; u32x4 yl;
  f32x2 xn[4];
#pragma unroll
  for (int e = 0; e < 4; ++e) {
    const u32 sb = __builtin_bit_cast(u32, bperm((e * 16 + tk) * 4, __builtin_bit_cast(float, stf)));
    xn[e] = rb2(rb2(bf2(qv[e]) - sp2(bflo(sb))) * sp2(__builtin_amdgcn_rcpf(bfhi(sb))));
    asm volatile("" : "+v"(xn[e]));
  }
  SB();
  u32 ra0 = tk * 256 + (((cq >> 1) ^ (tk & 7)) << 4) + (cq & 1) * 8, ra1 = ra0 + 65536;
  asm volatile("" : "+v"(ra1));
  f32x2 g[4], pr[8], zz;
  auto gate = [&](int e, int part) AI {
    if (part == 0) {
      cnt_wait(C_T + 4 * e, 8);
#pragma unroll
      for (int i = 0; i < 8; ++i) pr[i] = *(const volatile LAS f32x2*)((i < 4 ? ra0 : ra1) + e * 16384 + (i & 3) * 4096);
    } else if (part == 1) {
      zz = (((pr[0] + pr[1]) + (pr[2] + pr[3])) + ((pr[4] + pr[5]) + (pr[6] + pr[7]))) + bf2(b2v);
      asm volatile("" : "+v"(zz));
    } else { g[e] = zz * rcpx2(exp2x2(zz * sp2(-1.4426950408889634f)) + sp2(1.f)); asm volatile("" : "+v"(g[e])); }
    SB();
  };
  f32x4 a2[2][4];
  const u32 wa2 = (w < 4 ? 65536 : 0) + gw * 4096 + n16 * 256;
  auto pwrite = [&](int t, int nt) AI { lds_st<f32x4>(t * 16384 + wa2 + (((4 * nt + g4) ^ (n16 & 7)) << 4), a2[t & 1][nt]); SB(); };
#pragma unroll
  for (int t = 0; t < 4; ++t) {
#pragma unroll
    for (int i = 0; i < 7; ++i) {
      if (t == 0 && i == YDR) { waitv<0>(); arrive_flag(ws, C_Y, 8, OFF_FY + b * FS, tag, l); SB(); }
      if (t == 1 && i == 0) cnt_wait(C_S1, 8);
#pragma unroll
      for (int nt = 0; nt < 4; ++nt) a2[t & 1][nt] = mfma(w2f[i * 4 + nt], hf[t * 7 + i], i ? a2[t & 1][nt] : f32x4{0, 0, 0, 0});
      SB();
      if (t > 0 && i < 4) pwrite(t - 1, i);
      if (t > 0 && i == 4) cnt_add(C_T + 4 * (t - 1));
      load_h(t * 7 + i + HLA);
      if (t >= 2 && i >= 4) gate(t - 2, i - 4);
      if (t * 7 + i + HLA == 28) { fyv = flag_ld(ws, fy_off); SB(); }
      if (t == 3 && i == 0) {
        STAMP2(32);
        flag_chk(fyv, ws, fy_off, tag);
        STAMP2(33);
        yl = ld128(rs_ws, tid * 16, OFF_Y + rt * 65536 + ct * 8192);
        SB();
      }
    }
    STAMP(9 + t);
  }
#pragma unroll
  for (int nt = 0; nt < 4; ++nt) pwrite(3, nt);
#pragma unroll
  for (int e = 0; e < 4; ++e) lds_st<u32>(L_YS + ((tid >> 4) * 65 + (tid & 15) * 4 + e) * 4, yl[e]);
  cnt_add(C_T + 12);
  gate(2, 0); gate(2, 1); gate(2, 2);
  STAMP2(36);
  gate(3, 0);
  STAMP(13);
  {
    u32 yv[4];
#pragma unroll
    for (int e = 0; e < 4; ++e) yv[e] = lds_ld<u32>(L_YS + (cq * 65 + e * 16 + tk) * 4);
    SB();
    gate(3, 1); gate(3, 2);
    EACH4(xn[j] = rb2(xn[j] * bf2(yv[j])));
    EACH4(g[j] = fma2(xn[j], g[j], bf2(qv[j])));
    u16* op = out + (rt * 64 + tk) * D_ + ct * 64 + cq * 2;
    EACH4(*(GAS u32*)(op + j * 16 * D_) = pk2(g[j].x, g[j].y));
  }
  if (b == 0 && w == 0) {
#pragma unroll 1
    for (int i = 0; i < 4; ++i) poll_flags(ws, OFF_FY + (i * 64 + l) * FS, tag);
    if (l == 0) set_flag(ws, OFF_E, tag);
  }
  while (__builtin_amdgcn_s_memrealtime() - rt0_ < 4000) __builtin_amdgcn_s_sleep(1);
}

extern "C" void kernel_launch(void* const* d_in, const int* in_sizes, int n_in, void* d_out, int out_size, void* d_ws, size_t ws_size,
                              hipStream_t stream) {
  char* ws = (char*)d_ws + (WS_TAIL ? ((ws_size - WS_BYTES) & ~(size_t)4095) : 0);
  fused_kernel<<<256, 512, 0, stream>>>((const u16*)d_in[0], (const u16*)d_in[1], (const u16*)d_in[2], (const u16*)d_in[3], (const u16*)d_in[4],
                                        (const u16*)d_in[5], (const u16*)d_in[7], (const u16*)d_in[8], (const u16*)d_in[9], (const u16*)d_in[10],
                                        (u16*)d_out, ws);
}
```

```cpp
#include <hip/hip_runtime.h>
typedef unsigned short u16;
typedef unsigned u32;
typedef unsigned long long u64;
typedef __bf16 bf16x8 __attribute__((ext_vector_type(8)));
typedef __bf16 bf16x2 __attribute__((ext_vector_type(2)));
typedef short s16x4 __attribute__((ext_vector_type(4)));
typedef float f32x4 __attribute__((ext_vector_type(4)));
typedef float f32x2 __attribute__((ext_vector_type(2)));
typedef unsigned u32x4 __attribute__((ext_vector_type(4)));
typedef unsigned u32x2 __attribute__((ext_vector_type(2)));
#define LAS __attribute__((address_space(3)))
#define GAS __attribute__((address_space(1)))
#define DI __device__ __forceinline__
#pragma clang diagnostic ignored "-Wint-to-pointer-cast"

#define FSTR 256
constexpr int S_ = 2048, D_ = 512, F_ = 1792;
constexpr u32 OFF_E = 0, OFF_CS = 16384, OFF_ST = 32768, FS = FSTR  , OFF_FU = 65536, OFF_FW = OFF_FU + 256 * FS, OFF_FH = OFF_FW + 256 * FS, OFF_FY = OFF_FH + 256 * FS;
constexpr u32 OFF_U = 1u << 20, OFF_Y = 3u << 20, OFF_W2T = 5u << 20, OFF_H = 7u << 20, WS_BYTES = 14u << 20;
constexpr u32 SLOT = 32768, L_PK = 131072, L_PV = 140288, L_PW = 148480, L_SK = 156928, L_CNT = 157696,
              L_RC = 157824  , L_CC = 158848  , L_CSX = 159872,
              L_HC0 = 131072, L_HC1 = 0, HCS = 4160, L_UR = 147712, URS = 4576, L_CP = 32768, L_YS = 131072, L_QS = 147712;
constexpr u32 C_FULL = L_CNT  , C_FREE = L_CNT + 48  , C_H = L_CNT + 80, C_W = L_CNT + 84, C_U = L_CNT + 88, C_Y = L_CNT + 92,
              C_S1 = L_CNT + 96, C_S2 = L_CNT + 100, C_S3 = L_CNT + 104, C_SA = L_CNT + 36  , C_T = L_CNT + 112  , C_YS = L_CNT + 108, C_KV = L_CNT + 44, C_TG = L_CNT + 32, C_EP = L_CNT + 80  , L_B1 = 157440;
#define PSLEEP 8
#define NT_G 0
#define NT_P 0
#define WS_TAIL 0
#define BPRIO 1
#define NWB 28
#define WDR 1
#define SPQ 32
#define KVD -1
#define KROT 0
#define DEEP 0
#define PREPS 6
#define LPRIO 2
#define WGO 5
#define PFIRST 0
#define UDR 2
#define LFIRST 0
#define APRIO 1
#define CPS 4
#define HL0 8
#define YDR 0
#define NOPK 0
#define CPF 1
#define XNF 0
#define KVP 1
#define KVC 0
#define NWK 12
#define UQ 16
#define NWL 20
#define HLA 8

#define STAMP(i)
#define STAMP_INIT
#define STAMP_END
#define STAMP2(i)

template <int V> struct IC { static constexpr int v = V; };
template <int I, int N, class F> DI void sfor(F&& f) { if constexpr (I < N) { f(IC<I>{}); sfor<I + 1, N>(f); } }
DI u32x4 mk_srd(const void* p, u32 bytes) { u64 a = (u64)p; return u32x4{(u32)a, (u32)(a >> 32) & 0xffffu, bytes, 0x00020000u}; }
template <int NT> DI void dma16(u32x4 srd, u32 voff, u32 soff, u32 lds) {
  if (NT) asm volatile("s_mov_b32 m0, %3\n\ts_nop 0\n\tbuffer_load_dwordx4 %0, %1, %2 offen nt lds" ::"v"(voff), "s"(srd), "s"(soff), "s"(lds) : "memory");
  else asm volatile("s_mov_b32 m0, %3\n\ts_nop 0\n\tbuffer_load_dwordx4 %0, %1, %2 offen lds" ::"v"(voff), "s"(srd), "s"(soff), "s"(lds) : "memory");
}
template <int NT, int I> DI void dma16g(u32x4 srd, u32 voff, u32 soff, u32 lds) {
  if (I == 0) dma16<NT>(srd, voff, soff, lds);
  else if (NT) asm volatile("buffer_load_dwordx4 %0, %1, %2 offen offset:%3 nt lds" ::"v"(voff), "s"(srd), "s"(soff), "i"(I * 1024) : "memory");
  else asm volatile("buffer_load_dwordx4 %0, %1, %2 offen offset:%3 lds" ::"v"(voff), "s"(srd), "s"(soff), "i"(I * 1024) : "memory");
}
DI void dma16m(u32x4 srd, u32 voff, u32 soff, u32 lds, u64 mask) {
  asm volatile("s_mov_b32 m0, %3\n\ts_mov_b64 exec, %4\n\tbuffer_load_dwordx4 %0, %1, %2 offen lds\n\ts_mov_b64 exec, -1"
               ::"v"(voff), "s"(srd), "s"(soff), "s"(lds), "s"(mask) : "memory");
}
template <int N> DI void waitv() { asm volatile("s_waitcnt vmcnt(%0)" ::"i"(N) : "memory"); }
#define WAITL0() asm volatile("s_waitcnt lgkmcnt(0)" ::: "memory")
#define CFENCE() asm volatile("" ::: "memory")
#define SB() __builtin_amdgcn_sched_barrier(0)
DI void bar() { SB(); CFENCE(); __builtin_amdgcn_s_waitcnt(0xC07F); __builtin_amdgcn_s_barrier(); CFENCE(); SB(); }
DI float bflo(u32 x) { return __uint_as_float(x << 16); }
DI float bfhi(u32 x) { return __uint_as_float(x & 0xffff0000u); }
DI u32 pk2(float a, float b) { f32x2 p = {a, b}; return __builtin_bit_cast(u32, __builtin_convertvector(p, bf16x2)); }
DI float rb(float a) { return bflo(pk2(a, a)); }
DI f32x2 bf2(u32 x) { return f32x2{bflo(x), bfhi(x)}; }
DI f32x2 rb2(f32x2 v) { return bf2(pk2(v.x, v.y)); }
DI f32x2 sp2(float v) { return f32x2{v, v}; }
DI f32x2 exp2x2(f32x2 v) { return f32x2{__builtin_amdgcn_exp2f(v.x), __builtin_amdgcn_exp2f(v.y)}; }
DI f32x2 rcpx2(f32x2 v) { return f32x2{__builtin_amdgcn_rcpf(v.x), __builtin_amdgcn_rcpf(v.y)}; }
DI f32x2 fma2(f32x2 a, f32x2 b, f32x2 c) { return __builtin_elementwise_fma(a, b, c); }
DI float sfma(float a, float b, float c) { float d; asm("v_fma_f32 %0, %1, %2, %3" : "=v"(d) : "v"(a), "v"(b), "v"(c)); return d; }
DI float smul(float a, float b) { float d; asm("v_mul_f32 %0, %1, %2" : "=v"(d) : "v"(a), "v"(b)); return d; }
DI f32x2 fma2s(f32x2 a, f32x2 b, f32x2 c) { return f32x2{sfma(a.x, b.x, c.x), sfma(a.y, b.y, c.y)}; }
DI f32x2 mul2s(f32x2 a, f32x2 b) { return f32x2{smul(a.x, b.x), smul(a.y, b.y)}; }
template <class T> DI T lds_ld(u32 off) { return *(const LAS T*)(off); }
template <class T> DI void lds_st(u32 off, T v) { *(LAS T*)(off) = v; }
DI u32x2 lds_tr(u32 off) { return __builtin_bit_cast(u32x2, __builtin_amdgcn_ds_read_tr16_b64_v4i16((LAS s16x4*)(off))); }
DI u32x4 lds_tr2(u32 o0, u32 o1) { u32x2 a = lds_tr(o0), b = lds_tr(o1); return u32x4{a.x, a.y, b.x, b.y}; }
DI u32x4 lds_2x64(u32 o) { u32x2 a = *(const volatile LAS u32x2*)(o), b = *(const volatile LAS u32x2*)(o + 8); return u32x4{a.x, a.y, b.x, b.y}; }
DI f32x4 mfma(u32x4 a, u32x4 b, f32x4 c) {
  return __builtin_amdgcn_mfma_f32_16x16x32_bf16(__builtin_bit_cast(bf16x8, a), __builtin_bit_cast(bf16x8, b), c, 0, 0, 0);
}
DI void st128(u32x4 v, __amdgpu_buffer_rsrc_t rs, u32 voff, u32 soff) {
  __builtin_amdgcn_raw_buffer_store_b128(v, rs, voff, soff, 16);
  asm volatile("s_nop 1" ::"v"(v) : "memory");
}
DI u32x4 ld128(__amdgpu_buffer_rsrc_t rs, u32 voff, u32 soff) { return __builtin_amdgcn_raw_buffer_load_b128(rs, voff, soff, 16); }
template <int C> DI float dpp_add(float v) {
  return v + __builtin_bit_cast(float, __builtin_amdgcn_update_dpp(0, __builtin_bit_cast(int, v), C, 0xf, 0xf, true));
}
DI float bperm(int src_lane4, float v) { return __builtin_bit_cast(float, __builtin_amdgcn_ds_bpermute(src_lane4, __builtin_bit_cast(int, v))); }
DI float sel(bool c, float a, float b) { return c ? a : b; }
DI float rl(float v, int i) { return __builtin_bit_cast(float, __builtin_amdgcn_readlane(__builtin_bit_cast(int, v), i)); }
DI float wave_sum(float v) {
  v = dpp_add<0xB1>(v); v = dpp_add<0x4E>(v); v = dpp_add<0x141>(v); v = dpp_add<0x140>(v);
  return (rl(v, 0) + rl(v, 16)) + (rl(v, 32) + rl(v, 48));
}
constexpr int SPIN_CAP = 1 << 16;
DI u64 flag_ld(const char* ws, u32 off) { return __hip_atomic_load((const GAS u64*)(ws + off), __ATOMIC_RELAXED, __HIP_MEMORY_SCOPE_AGENT); }
DI void poll_flags(const char* ws, u32 off, u64 tag) {
#pragma unroll 1
  for (int it = 0; it < SPIN_CAP; ++it) {
    if (__all(flag_ld(ws, off) == tag)) break;
    __builtin_amdgcn_s_sleep(PSLEEP);
  }
  CFENCE();
}
DI void flag_chk(u64 v, const char* ws, u32 off, u64 tag) { if (!__all(v == tag)) poll_flags(ws, off, tag); CFENCE(); }
DI void set_flag(char* ws, u32 off, u64 tag) { __hip_atomic_store((GAS u64*)(ws + off), tag, __ATOMIC_RELAXED, __HIP_MEMORY_SCOPE_AGENT); }
DI void arrive_flag(char* ws, u32 cnt, u32 n, u32 off, u64 tag, int l) {
  if (l == 0) {
    u32 old;
    asm volatile("ds_add_rtn_u32 %0, %1, %2\n\ts_waitcnt lgkmcnt(0)" : "=&v"(old) : "v"(cnt), "v"(1u) : "memory");
    if (old == n - 1) set_flag(ws, off, tag);
  }
  CFENCE();
}
DI void cnt_add(u32 cnt) { asm volatile("s_mov_b64 exec, 1\n\tds_add_u32 %0, %1\n\ts_mov_b64 exec, -1" ::"v"(cnt), "v"(1u) : "memory"); }
DI u32 cnt_ld(u32 cnt) { return *(const volatile LAS u32*)cnt; }
DI void cnt_wait(u32 cnt, u32 n) {
#pragma unroll 1
  for (int it = 0; it < (1 << 22); ++it) { if ((u32)__builtin_amdgcn_readfirstlane(cnt_ld(cnt)) >= n) break; __builtin_amdgcn_s_sleep(1); }
  CFENCE(); SB();
}
DI void group_sync(u32 cnt) { SB(); WAITL0(); cnt_add(cnt); cnt_wait(cnt, 4); }
#define AI __attribute__((always_inline))
#define EACH4(stmt) do { _Pragma("unroll") for (int j = 0; j < 4; ++j) { stmt; } SB(); } while (0)
#define HALF(v, j) (((j) & 1) ? (v).hi : (v).lo)

#define KATTR
__global__ __launch_bounds__(512, 2) KATTR void fused_kernel(const u16* __restrict__ q, const u16* __restrict__ kk, const u16* __restrict__ vv,
                                                       const u16* __restrict__ sk, const u16* __restrict__ ssk, const u16* __restrict__ sD,
                                                       const u16* __restrict__ w1, const u16* __restrict__ b1, const u16* __restrict__ w2,
                                                       const u16* __restrict__ b2, u16* __restrict__ out, char* __restrict__ ws) {
  __shared__ __attribute__((aligned(16))) char sm[163840];
  asm volatile("" ::"v"(sm) : "memory");
  const int tid = threadIdx.x, l = tid & 63;
  const int w = __builtin_amdgcn_readfirstlane(tid >> 6);
  const int b = blockIdx.x, xcd = b & 7, r = b >> 3;
  const bool mainwg = r < 28;
  const int rp = (xcd & 3) * 4 + (r & 3), cc = (xcd >> 2) * 7 + (r >> 2);
  const int rt = (xcd & 3) * 8 + (xcd >> 2) * 4 + (r & 3), ct = r >> 2;
  const int gw = w & 3;
  const int n16 = l & 15, g4 = l >> 4;
  STAMP_INIT
  STAMP(0);
  if (CPF && w == 7) {
    u64 pc; u32 dummy;
    asm volatile("s_getpc_b64 %0" : "=s"(pc));
    asm volatile("global_load_dword %0, %1, %2" : "=v"(dummy) : "v"((u32)((r & 3) * 64 + l) * 128u - 128u * ((r & 3) == 3 && l >= 24 ? l - 24 : 0)), "s"(pc) : "memory");
  }
  if (tid < 8) lds_st<u32x4>(L_CNT + tid * 16, u32x4{0, 0, 0, 0});
  int rp_ = rp, cc_ = cc, rt_ = rt;
  u32 vr = (l >> 3) * 1024 + (l & 7) * 16;
  asm volatile("" : "+s"(rp_), "+s"(cc_), "+s"(rt_), "+v"(vr));
  SB();
  asm volatile("" : "+s"(q), "+s"(kk), "+s"(vv), "+s"(sk), "+s"(ssk), "+s"(sD), "+s"(w1), "+s"(b1), "+s"(w2), "+s"(b2), "+s"(out), "+s"(ws));
  const u32x4 srd_q = mk_srd(q, S_ * D_ * 2);
  const u32x4 srd_w1m = mk_srd((const char*)w1 - F_ * 2, D_ * F_ * 2 + F_ * 2);
  u32x2 ev;
  u64 tag;
  auto load_tag = [&]() AI { asm volatile("s_mov_b64 exec, 1\n\tglobal_load_dwordx2 %0, %1, %2 sc1\n\ts_mov_b64 exec, -1" : "=v"(ev) : "v"(0u), "s"(ws) : "memory"); };
  auto get_tag = [&]() AI {
    SB();
    asm volatile("" : "+v"(ev));
    tag = (((u64)__builtin_amdgcn_readfirstlane(ev.y) << 32) | (u32)__builtin_amdgcn_readfirstlane(ev.x)) + 0x9E3779B97F4A7C15ull;
  };
  const __amdgpu_buffer_rsrc_t rs_ws = __builtin_amdgcn_make_buffer_rsrc(ws, 0, WS_BYTES, 0x00020000);
  u32x4 w2f[28];
  auto load_w2 = [&](int j) AI { w2f[j] = ld128(rs_ws, l * 16 + (j & 3) * 1024, OFF_W2T + (ct * 56 + 7 * w + (j >> 2)) * 4096); SB(); };
  const u32 fw_off = OFF_FW + (4 * w + (ct >> 1)) * FS;
  const u32 fh_off = OFF_FH + ((rt >> 1) * 16 + min(((7 * w) >> 2) + (l & 3), (7 * w + 6) >> 2)) * FS;

  const u32 sx32 = ((2 * g4 + (n16 >> 3)) & 3) << 5, tr0 = (4 * g4 + (n16 >> 2)) * 128 + (l & 3) * 8, vsw = ((l & 7) ^ ((l >> 3) & 6)) << 4;
  const int sp = (r - 28) * 8 + xcd;
  if (w >= 4) {
    u32x4 ha[2]; u32 hn[2], dd;
    auto load_taps = [&]() AI {
      asm volatile("buffer_load_dwordx4 %0, %4, %5, %6 offen nt\n\tbuffer_load_dwordx4 %1, %4, %5, %6 offen offset:1024 nt\n\t"
                   "buffer_load_dword %2, %4, %5, %6 offen offset:16\n\tbuffer_load_dword %3, %4, %5, %6 offen offset:1040"
                   : "=&v"(ha[0]), "=&v"(ha[1]), "=&v"(hn[0]), "=&v"(hn[1]) : "v"(l * 16), "s"(mk_srd(ssk, S_ * D_ * 2)), "s"(b * 8192 + gw * 2048) : "memory");
      asm volatile("buffer_load_ushort %0, off, %1, %2" : "=v"(dd) : "s"(mk_srd(sD, D_ * 2)), "s"(b * 4 + (gw >> 1) * 2) : "memory");
    };
    __builtin_amdgcn_s_setprio(LPRIO);
    const u32 vx0 = (l >> 3) * 1024 + (((l & 7) ^ (l >> 3)) << 4), vx1 = vx0 + 8192 - 1024;
    const u32 vwb = (g4 + 1) * (F_ * 2) - 2048, vws = (n16 ^ (g4 << 1)) << 4;
    const u32 vw[4] = {vwb + vws, vwb + vws + 4 * (F_ * 2) - 1024, vwb + (vws ^ 128), vwb + (vws ^ 128) + 4 * (F_ * 2) - 1024};
    const int kc0 = KROT == 1 ? ((r >> 2) + 2 * (r & 3)) & 7 : KROT == 2 ? (((r >> 2) + r) & 1) * 4 : 0;
    auto stage = [&](int s) AI {
      const int kc = (s + kc0) & 7;
      const u32 sx = rp * 131072 + (gw & 1) * 65536 + kc * 128, sw = ((gw & 1) * 32 + kc * 64) * (F_ * 2) + cc * 256, dst = (s & 3) * SLOT + (gw & 1) * 16384;
#pragma unroll
      for (int i = 0; i < 4; ++i) {
        dma16g<NT_G, 0>(srd_q, vx0, sx + i * 16384, dst + i * 4096); dma16g<NT_G, 1>(srd_q, vx1, sx + i * 16384, 0);
        dma16g<NT_G, 2>(srd_w1m, vw[(i & 1) * 2], sw + i * 8 * (F_ * 2), 0); dma16g<NT_G, 3>(srd_w1m, vw[(i & 1) * 2 + 1], sw + i * 8 * (F_ * 2), 0);
      }
    };
    if (mainwg) stage(gw >> 1);
    bar();
    if (mainwg) { waitv<0>(); cnt_add(C_FULL + 4 * (gw >> 1)); }
    load_tag();
    if (mainwg) stage((gw >> 1) + 2);
    if (gw < 2) {
      if (mainwg) {
        u32x4 g;
#pragma unroll 1
        for (int s = 4; s < 8; ++s) {
          if (s == 4) { waitv<0>(); cnt_add(C_FULL + 8); }
          cnt_wait(C_FREE + 4 * (s - 4), 4);
          if (s == 7) {
            asm volatile("buffer_load_dwordx4 %0, %1, %2, %3 offen sc1" : "=v"(g) : "v"(l * 16), "s"(mk_srd(ws, WS_BYTES)), "s"(OFF_ST + rp * 2048 + gw * 1024) : "memory");
            dma16m(mk_srd(b1, F_ * 2), l * 16, cc * 256, L_B1, 0xffffull);
          }
          stage(s);
          if (s > 4) { waitv<16>(); cnt_add(C_FULL + 4 * (s - 1)); }
        }
        STAMP2(41);
        get_tag();
        asm volatile("" : "+v"(g));
        load_taps();
        auto g_ok = [&]() AI { return __all((g.y == (u32)tag) & (g.w == (u32)(tag >> 32))); };
        if (!g_ok()) {
#pragma unroll 1
          for (int it = 0; it < SPIN_CAP; ++it) { g = ld128(rs_ws, l * 16, OFF_ST + rp * 2048 + gw * 1024); if (g_ok()) break; __builtin_amdgcn_s_sleep(4); CFENCE(); }
        }
        const float i2 = __builtin_amdgcn_rcpf(bfhi(g.z));
        float ra = __builtin_amdgcn_rcpf(bfhi(g.x)) * i2;
        asm volatile("" : "+v"(ra));
        lds_st<f32x2>(L_RC + (gw * 64 + l) * 8, f32x2{ra, -bflo(g.x) * ra - bflo(g.z) * i2});
        lds_st<u32>(L_CC + (gw * 64 + l) * 4, (u32)lds_ld<u16>(L_B1 + (gw * 64 + l) * 2) << 16);
        waitv<5>();
        cnt_add(C_FULL + 28);
        STAMP2(42);
      } else { load_taps(); waitv<0>(); get_tag(); }
      cnt_add(C_KV);
    } else {
      const int pw = gw & 1;
      const u32 so = (rt * 64 + pw * 32) * 1024 + ct * 128;
      const bool top = rt == 0 && pw == 0;
      sfor<0, 4>([&](auto I) AI { dma16g<NT_P, I.v>(mk_srd(kk, S_ * D_ * 2), vr, so + I.v * 7168, L_PK + 1024 + pw * 4096); });
      dma16m(mk_srd(kk, top ? 0 : S_ * D_ * 2), vr, top ? 0 : so - 8192, L_PK + pw * 4096, ~0ull << 40);
      dma16m(mk_srd(sk, D_ * 8), l * 16, ct * 512, L_SK, 0xffffffffull);
      sfor<0, 4>([&](auto I) AI { dma16g<NT_P, I.v>(mk_srd(vv, S_ * D_ * 2), vr, so + I.v * 7168, L_PV + pw * 4096); });
      waitv<10>(); cnt_add(C_FULL + 12);
      waitv<0>();
      get_tag();
      load_taps();
      STAMP2(41);
      const int cpl = l & 31, hh = l >> 5;
      const u32x4 sv = lds_ld<u32x4>(L_SK + cpl * 16);
      const f32x2 kT[4] = {f32x2{bflo(sv.x), bflo(sv.z)}, f32x2{bfhi(sv.x), bfhi(sv.z)}, f32x2{bflo(sv.y), bflo(sv.w)}, f32x2{bfhi(sv.y), bfhi(sv.w)}};
      u32 a = L_PK + (pw * 32 + hh * 16) * 128 + cpl * 4;
      asm volatile("" : "+v"(a));
      u32 kr[19], vq[16];
#pragma unroll
      for (int i = 0; i < 19; ++i) kr[i] = lds_ld<u32>(a + 5 * 128 + i * 128);
#pragma unroll
      for (int i = 0; i < 16; ++i) vq[i] = lds_ld<u32>(a + (L_PV - L_PK) + i * 128);
      SB();
      f32x2 kK[19];
#pragma unroll
      for (int i = 0; i < 3; ++i) kK[i] = bf2(kr[i]);
      u32x4 uo[4];
#pragma unroll
      for (int u = 0; u < 4; ++u) {
        f32x2 ka[4];
        EACH4(kK[4 * u + 3 + j] = bf2(kr[4 * u + 3 + j]));
        EACH4(ka[j] = mul2s(kK[4 * u + j], kT[3])); EACH4(ka[j] = fma2s(kK[4 * u + j + 1], kT[2], ka[j]));
        EACH4(ka[j] = fma2s(kK[4 * u + j + 2], kT[1], ka[j])); EACH4(ka[j] = fma2s(kK[4 * u + j + 3], kT[0], ka[j]));
        EACH4(ka[j] = rb2(ka[j])); EACH4(ka[j] = mul2s(ka[j], bf2(vq[4 * u + j])));
        uo[u] = u32x4{pk2(ka[3].x, ka[2].x), pk2(ka[1].x, ka[0].x), pk2(ka[3].y, ka[2].y), pk2(ka[1].y, ka[0].y)};
        SB();
      }
#pragma unroll
      for (int u = 0; u < 4; ++u) lds_st<u32x4>(L_PV + pw * 4096 + cpl * 128 + (((hh * 4 + u) ^ (cpl & 7)) << 4), uo[u]);
#pragma unroll
      for (int i = 0; i < 4; ++i) uo[i] = lds_ld<u32x4>(L_PV + pw * 4096 + i * 1024 + (l >> 3) * 128 + (((l & 7) ^ (l >> 3)) << 4));
#pragma unroll
      for (int i = 0; i < 4; ++i) st128(uo[i], rs_ws, (l >> 3) * 8192 + (l & 7) * 16, OFF_U + ((ct * 32 + 8 * i) * 512 + rt * 16 + pw * 8) * 16);
      cnt_add(C_KV);
      STAMP2(42);
      waitv<0>();
      cnt_wait(C_TG, 4);
      arrive_flag(ws, C_U, 2, OFF_FU + (ct * 32 + rt) * FS, tag, l);
    }
    STAMP(2);
    if (!mainwg) cnt_wait(C_W, 4);
    poll_flags(ws, fw_off, tag);
#pragma unroll
    for (int j = 0; j < NWK; ++j) load_w2(j);
    STAMP2(43);
    __builtin_amdgcn_s_setprio(3);
    cnt_wait(C_KV, 4);
    if (mainwg) cnt_wait(C_FREE + 4 * CPS, 4);
    asm volatile("" : "+v"(ha[0]), "+v"(ha[1]), "+v"(hn[0]), "+v"(hn[1]), "+v"(dd));
    {
      const u32 hb = (gw >> 1) ? L_HC1 : L_HC0;
      const u32 h0 = pk2(0.f, bflo(ha[0].x) + bflo(dd));
      if ((tid & 127) == 0) ha[0].x = __builtin_amdgcn_perm(ha[0].x, h0, 0x07060302u);
      const u32 h0u = __builtin_amdgcn_readfirstlane(h0);
      if (gw & 1) { if (l < 60) lds_st<u32x2>(L_UR + (gw >> 1) * URS + 4096 + l * 8, u32x2{0, 0}); }
      else if (l < 31 && l != 23) lds_st<u32>(hb + (l >> 3) * HCS + (l & 7) * 4, (l == 15 || l == 30) ? h0u : 0u);
#pragma unroll
      for (int i = 0; i < 2; ++i) {
        const u32x4 x = ha[i];
        const u32x4 o = {__builtin_amdgcn_alignbit(x.y, x.x, 16), __builtin_amdgcn_alignbit(x.z, x.y, 16), __builtin_amdgcn_alignbit(x.w, x.z, 16), __builtin_amdgcn_alignbit(hn[i], x.w, 16)};
        const u32 d = hb + 32 + (gw & 1) * 2048 + i * 1024 + l * 16, d2 = d + 2 * HCS - 4, d3 = d + 3 * HCS - 4;
        lds_st<u32x4>(d, x); lds_st<u32x4>(d + HCS, o);
        asm volatile("ds_write2_b32 %0, %2, %3 offset1:1\n\tds_write2_b32 %0, %4, %5 offset0:2 offset1:3\n\tds_write2_b32 %1, %6, %7 offset1:1\n\tds_write2_b32 %1, %8, %9 offset0:2 offset1:3"
                     ::"v"(d2), "v"(d3), "v"(x.x), "v"(x.y), "v"(x.z), "v"(x.w), "v"(o.x), "v"(o.y), "v"(o.z), "v"(o.w) : "memory");
      }
    }
    STAMP2(31);
    if (!mainwg) __builtin_amdgcn_s_sleep(SPQ);
    const u32 fu_off = OFF_FU + ((b >> 5) * 32 + 8 * gw + (l & 7)) * FS;
#pragma unroll
    for (int j = NWK; j < NWL; ++j) load_w2(j);
    poll_flags(ws, fu_off, tag);
    STAMP2(37);
    u32x4 uu[2];
#pragma unroll
    for (int i = 0; i < 2; ++i) uu[i] = ld128(rs_ws, l * 16 + i * 1024, OFF_U + b * 8192 + gw * 2048);
    SB();
#pragma unroll
    for (int j = NWL; j < 28; ++j) load_w2(j);
#pragma unroll
    for (int i = 0; i < 2; ++i) {
      const u32 a = L_UR + (2044 - 4 * (gw * 128 + i * 64 + l)) * 2;
      lds_st<u32x2>(a, u32x2{uu[i].x, uu[i].y}); lds_st<u32x2>(a + URS, u32x2{uu[i].z, uu[i].w});
    }
    cnt_add(C_S2);
    STAMP(4);
    cnt_wait(C_S2, mainwg && !NOPK ? 8 : 4);
    STAMP(5);
    {
      const int ch = gw & 1, kh = gw >> 1;
      f32x4 ca[8];
#pragma unroll
      for (int i = 0; i < 8; ++i) ca[i] = f32x4{0, 0, 0, 0};
      const u32 ab = (ch ? L_HC1 : L_HC0) + ((n16 + 1) & 3) * HCS + (((n16 + 1) & ~3) + 8 * g4) * 2 + kh * 256;
      const u32 bb = L_UR + ch * URS + (2032 - 1792 - 16 * n16 + 8 * g4) * 2 + kh * 256;
      const u32 pa = L_CP + gw * 8192 + (4 * n16 + (g4 ^ ((n16 >> 1) & 3))) * 16;
      u32x4 AL[2][4], BL[2][4], AH[4], BH[4];
      auto rdp = [&](u32x4& a, u32x4& bq, int k0, int i) AI { SB(); a = lds_2x64(ab + k0 * 64 + i * 512); bq = lds_ld<u32x4>(bb + k0 * 64 + (7 - i) * 512); SB(); };
#pragma unroll
      for (int i = 0; i < 4; ++i) rdp(AL[0][i], BL[0][i], 0, i);
      sfor<0, 4>([&](auto K) AI {
        constexpr int k0 = K.v;
        u32x4 (&A)[4] = AL[k0 & 1]; u32x4 (&B)[4] = BL[k0 & 1];
#pragma unroll
        for (int n = 0; n < 16; ++n) {
          constexpr int oi[16] = {0, 0, 1, 0, 1, 2, 0, 1, 2, 3, 1, 2, 3, 2, 3, 3}, ot[16] = {0, 1, 0, 2, 1, 0, 3, 2, 1, 0, 3, 2, 1, 3, 2, 3};
          ca[oi[n] + ot[n]] = mfma(A[oi[n]], B[ot[n]], ca[oi[n] + ot[n]]);
          if (n < 8 && (n & 1)) rdp(AH[n >> 1], BH[n >> 1], k0, 4 + (n >> 1));
          if (k0 == 3 && n >= 12) { SB(); lds_st<f32x4>(pa + (n - 12) * 1024, ca[n - 12]); SB(); }
        }
        int cn = 0;
#pragma unroll
        for (int T = 0; T < 4; ++T)
#pragma unroll
          for (int i = 0; i <= T; ++i) {
            ca[4 + T] = mfma(A[i], BH[T - i], ca[4 + T]); ca[4 + T] = mfma(AH[T - i], B[i], ca[4 + T]);
            if (k0 < 3 && cn >= 2 && cn < 6) rdp(AL[(k0 + 1) & 1][cn - 2], BL[(k0 + 1) & 1][cn - 2], k0 + 1, cn - 2);
            if (k0 == 3 && T > 0 && i == 1) { SB(); lds_st<f32x4>(pa + (3 + T) * 1024, ca[3 + T]); SB(); }
            ++cn;
          }
        SB();
      });
      lds_st<f32x4>(pa + 7 * 1024, ca[7]);
    }
    cnt_add(C_S3);
    __builtin_amdgcn_s_setprio(0);
    STAMP(6);
    const u64 fhv = flag_ld(ws, fh_off);
    cnt_wait(C_S3, 4);
    {
      const int t2 = tid & 255;
      f32x4 pr[2][4];
#pragma unroll
      for (int i = 0; i < 2; ++i)
#pragma unroll
        for (int p = 0; p < 4; ++p) pr[i][p] = lds_ld<f32x4>(L_CP + ((t2 & ~3) | ((t2 & 3) ^ ((t2 >> 3) & 3))) * 16 + i * 4096 + p * 8192);
      cnt_add(C_S1);
      SB();
#pragma unroll
      for (int i = 0; i < 2; ++i) {
        const f32x4 y0 = pr[i][0] + pr[i][2], y1 = pr[i][1] + pr[i][3];
        st128(u32x4{pk2(y0[0], y1[0]), pk2(y0[1], y1[1]), pk2(y0[2], y1[2]), pk2(y0[3], y1[3])}, rs_ws, (t2 >> 4) * 65536 + (t2 & 15) * 16, OFF_Y + b * 256 + i * 16 * 65536);
      }
    }
    STAMP2(29);
    flag_chk(fhv, ws, fh_off, tag);
  } else {
    {
      __builtin_amdgcn_s_setprio(3);
    const int srow = rt * 64 + ct * 8 + gw * 2;
    u32x4 x0[2];
      load_tag();
    asm volatile("buffer_load_dwordx4 %0, %2, %3, %4 offen\n\tbuffer_load_dwordx4 %1, %2, %3, %4 offen offset:1024"
                 : "=&v"(x0[0]), "=&v"(x0[1]) : "v"(l * 16), "s"(srd_q), "s"(srow * 1024) : "memory");
      if (!mainwg) {
#pragma unroll
        for (int p = 0; p < 4; ++p) {
          const int hp = gw * 4 + p;
          sfor<0, 4>([&](auto I) AI { dma16g<NT_P, I.v>(mk_srd(w2, hp < 14 ? D_ * F_ * 2 : 0), (l >> 3) * 1024 + vsw,
                                                       ((sp >> 2) * 7 + (hp >> 1)) * 32768 + (sp & 3) * 256 + (hp & 1) * 128 + I.v * 7168, hp * 4096); });
        }
      }
      bar();
      if (mainwg) waitv<0>(); else waitv<16>();
      get_tag();
      cnt_add(C_TG);
      STAMP2(40);
    {
      asm volatile("" : "+v"(x0[0]), "+v"(x0[1]));
      float c0[2], sa[2], sq[2];
#pragma unroll
      for (int i = 0; i < 2; ++i) {
        c0[i] = bflo(__builtin_amdgcn_readfirstlane(x0[i].x));
        const f32x2 d[4] = {bf2(x0[i].x) - sp2(c0[i]), bf2(x0[i].y) - sp2(c0[i]), bf2(x0[i].z) - sp2(c0[i]), bf2(x0[i].w) - sp2(c0[i])};
        const f32x2 s2 = (d[0] + d[1]) + (d[2] + d[3]), q2 = fma2(d[0], d[0], d[1] * d[1]) + fma2(d[2], d[2], d[3] * d[3]);
        sa[i] = s2.x + s2.y; sq[i] = q2.x + q2.y;
      }
#define RED4(C) do { sa[0] = dpp_add<C>(sa[0]); sq[0] = dpp_add<C>(sq[0]); sa[1] = dpp_add<C>(sa[1]); sq[1] = dpp_add<C>(sq[1]); SB(); } while (0)
      SB(); RED4(0xB1); RED4(0x4E); RED4(0x141); RED4(0x140); RED4(0x142); RED4(0x143);
      const bool odd = l & 1;
      const float md = sel(odd, sa[1], sa[0]) * (1.f / D_), mean = sel(odd, c0[1], c0[0]) + md, var = fmaxf(sel(odd, sq[1], sq[0]) * (1.f / D_) - md * md, 0.f);
      const float m1 = rb(mean), sd1 = rb(__builtin_amdgcn_sqrtf(rb(rb(var) + 1e-5f)));
      const float i1 = __builtin_amdgcn_rcpf(sd1);
      const float m2 = (mean - m1) * i1, sd2 = __builtin_amdgcn_sqrtf(rb(rb(var * i1 * i1) + 1e-5f));
      if (l >= 62) st128(u32x4{pk2(m1, sd1), (u32)tag, pk2(m2, sd2), (u32)(tag >> 32)}, rs_ws, (l - 62) * 16, OFF_ST + srow * 16);
    }

      __builtin_amdgcn_s_setprio(APRIO);
      if (!mainwg) {
        waitv<1>();
        SB();
#pragma unroll
        for (int p = 0; p < 4; ++p) {
          const int hp = gw * 4 + p;
          u32x4 wt[4];
#pragma unroll
          for (int nt = 0; nt < 4; ++nt) { const u32 a = hp * 4096 + tr0 + ((nt * 32) ^ sx32); wt[nt] = lds_tr2(a, a + 2048); }
#pragma unroll
          for (int nt = 0; nt < 4; ++nt)
            st128(wt[nt], rs_ws, l * 16, hp < 14 ? OFF_W2T + ((((sp & 3) * 2 + (hp & 1)) * 56 + (sp >> 2) * 7 + (hp >> 1)) * 4 + nt) * 1024 : 0x7f000000u);
        }
        waitv<0>();
        arrive_flag(ws, C_W, 4, OFF_FW + sp * FS, tag, l);
        STAMP2(42);
      }
    }
    if (mainwg) {
      const int th = gw >> 1, fh = gw & 1;
      f32x4 acc[4][4];
#pragma unroll
      for (int i = 0; i < 16; ++i) acc[i >> 2][i & 3] = f32x4{0, 0, 0, 0};
      const u32 xa = th * 16384 + n16 * 128 + ((g4 ^ (n16 & 7)) << 4);
      const u32 wa = g4 * 4096 + 2048 + (n16 >> 2) * 256 + (((4 * fh + 2 * th) ^ ((n16 >> 2) | ((g4 & 1) << 2))) << 5) + (l & 3) * 8;
      u32 ad[2][6];
#pragma unroll
      for (int h = 0; h < 2; ++h) {
#pragma unroll
        for (int e = 0; e < 4; ++e) ad[h][e] = (wa ^ (e * 32)) + h * 65536;
        ad[h][4] = xa + h * 65536; ad[h][5] = (xa ^ 64) + h * 65536;
        asm volatile("" : "+v"(ad[h][0]), "+v"(ad[h][1]), "+v"(ad[h][2]), "+v"(ad[h][3]), "+v"(ad[h][4]), "+v"(ad[h][5]));
      }
      u32x4 xf[2][4], wf[2][4];
      f32x4 cf[4], bf[4]; f32x2 rc[4];
      auto rd = [&](int hs, int p) AI {
        const int s = hs >> 1, ks = hs & 1, hi = (s >> 1) & 1, e = p >> 1;
        const u32 so = (s & 1) * SLOT;
        if (hs > 15) {
          if (p < 4) bf[p] = lds_ld<f32x4>(L_CC + fh * 256 + ((p * 64) ^ (th * 128)) + g4 * 16);
          else rc[p - 4] = lds_ld<f32x2>(L_RC + (th * 64 + (p - 4) * 16 + n16) * 8);
        } else if (!(p & 1)) { const u32 a = ad[hi][e] + so + ks * 16384; wf[ks][e] = lds_tr2(a, a + 1024); }
        else xf[ks][e] = lds_ld<u32x4>(ad[hi][4 + ks] + so + e * 4096);
      };
      u32 fullv;
      u32x4 ones = {0x3f803f80u, 0x3f803f80u, 0x3f803f80u, 0x3f803f80u};
      asm volatile("" : "+v"(ones));
#pragma unroll
      for (int i = 0; i < 4; ++i) cf[i] = f32x4{0, 0, 0, 0};
      auto half = [&](int hs) AI {
        const int ks = hs & 1;
        sfor<0, 16>([&](auto J) AI {
          constexpr int j = J.v, o = j < 4 ? (j & 1) * 4 + (j >> 1) : j < 9 ? (j == 4 ? 8 : j == 5 ? 9 : j == 6 ? 2 : j == 7 ? 6 : 10) : (j < 12 ? 12 + (j - 9) : (j - 12) * 4 + 3);
          acc[o >> 2][o & 3] = mfma(wf[ks][o >> 2], xf[ks][o & 3], acc[o >> 2][o & 3]);
          SB();
          if (j < 12 && j % 3 != 1) rd(hs + 1, (j / 3) * 2 + (j % 3) / 2);
          if (ks == 0 && j == 12) cnt_add(C_FREE + 2 * hs);
          if (ks == 0 && j == 13 && hs < 14) fullv = cnt_ld(C_FULL + 2 * hs + 4);
          SB();
        });
        cf[0] = mfma(wf[ks][0], ones, cf[0]); cf[1] = mfma(wf[ks][1], ones, cf[1]);
        SB();
      };
      STAMP2(34);
      cnt_wait(C_FULL, 2);
      STAMP2(35);
#pragma unroll
      for (int p = 0; p < 8; ++p) rd(0, p);
      SB();
      sfor<0, 8>([&](auto S) AI {
        constexpr int s = S.v;
        half(2 * s);
        if (s < 7 && (u32)__builtin_amdgcn_readfirstlane(fullv) < 2) cnt_wait(C_FULL + 4 * s + 4, 2);
        CFENCE(); SB();
        STAMP2(16 + s);
        half(2 * s + 1);
      });
      STAMP(2);
      __builtin_amdgcn_s_setprio(2);
      if (n16 == 0) { lds_st<f32x4>(L_CSX + gw * 128 + g4 * 16, cf[0]); lds_st<f32x4>(L_CSX + gw * 128 + 64 + g4 * 16, cf[1]); }
      cnt_add(C_SA + fh * 4);
      const float c1 = -2.f * 0.7978845608028654f * 1.4426950408889634f, c2 = c1 * 0.044715f;
      const u32 hso = OFF_H + ((rp * 8 + th * 4) * 56 + cc * 4 + fh * 2) * 1024;
#pragma unroll
      for (int u = 0; u < 8; ++u) {
        const int tt = u & 3, kp = u >> 2;
        if (u == 3) { cnt_wait(C_SA + fh * 4, 2); cf[2] = lds_ld<f32x4>(L_CSX + (gw ^ 2) * 128 + g4 * 16); cf[3] = lds_ld<f32x4>(L_CSX + (gw ^ 2) * 128 + 64 + g4 * 16); SB(); }
        f32x2 x[4], p[4]; u32 hp[4];
        EACH4(x[j] = fma2(HALF(cf[kp * 2 + (j >> 1)], j), sp2(rc[tt].y), HALF(bf[kp * 2 + (j >> 1)], j)));
        EACH4(x[j] = fma2(HALF(acc[kp * 2 + (j >> 1)][tt], j), sp2(rc[tt].x), x[j]));
        EACH4(p[j] = x[j] * x[j]);
        EACH4(p[j] = fma2(p[j], sp2(c2), sp2(c1)));
        EACH4(p[j] = p[j] * x[j]);
        EACH4(p[j] = exp2x2(p[j]));
        EACH4(p[j] = p[j] + sp2(1.f));
        EACH4(p[j] = rcpx2(p[j]));
        EACH4(p[j] = p[j] * x[j]);
        EACH4(hp[j] = pk2(p[j].x, p[j].y));
        st128(u32x4{hp[0], hp[1], hp[2], hp[3]}, rs_ws, l * 16, hso + tt * 56 * 1024 + ((kp * 1024) ^ (th * 1024)));
        SB();
      }
      if (!NOPK) cnt_add(C_S2);
      STAMP(4);
    }
    if (!mainwg) __builtin_amdgcn_s_sleep(SPQ);
    poll_flags(ws, fw_off, tag);
    waitv<0>();
    if (mainwg) arrive_flag(ws, C_H, 4, OFF_FH + (rp * 16 + cc) * FS, tag, l);
    __builtin_amdgcn_s_setprio(0);
    STAMP(5);
#pragma unroll
    for (int j = 0; j < 28; ++j) load_w2(j);
    STAMP(6);
    if (!mainwg) {
      cnt_wait(C_S3, 4);
    }
    poll_flags(ws, fh_off, tag);
  }
  STAMP(7);
  u32x4 hf[28];
  auto load_h = [&](int n) AI { if (n < 28) { hf[n] = ld128(rs_ws, l * 16, OFF_H + ((rt * 4 + n / 7) * 56 + 7 * w + n % 7) * 1024); SB(); } };
  const int t2 = tid & 255;
#pragma unroll
  for (int n = 0; n < HLA; ++n) load_h(n);
  STAMP(8);
  const int tk = t2 >> 4;
  const u32 fy_off = OFF_FY + (ct * 32 + 8 * gw + (l & 7)) * FS;
  u64 fyv; u32x2 qv[4], b2v; u32 stf;
  f32x4 a2[2][4];
  const u32 wa2 = (w < 4 ? 65536 : 0) + gw * 4096 + n16 * 256;
  auto pwrite = [&](int t, int nt) AI { lds_st<f32x4>(t * 16384 + wa2 + (((4 * nt + g4) ^ (n16 & 7)) << 4), a2[t & 1][nt]); SB(); };
#pragma unroll
  for (int t = 0; t < 4; ++t) {
#pragma unroll
    for (int i = 0; i < 7; ++i) {
      if (t * 7 + i == YDR && w >= 4) { waitv<0>(); arrive_flag(ws, C_Y, 4, OFF_FY + b * FS, tag, l); SB(); }
      if (t == 1 && i == 0 && (w >= 4 || mainwg)) cnt_wait(w < 4 ? C_FREE + 28 : C_S1, 4);
#pragma unroll
      for (int nt = 0; nt < 4; ++nt) a2[t & 1][nt] = mfma(w2f[i * 4 + nt], hf[t * 7 + i], i ? a2[t & 1][nt] : f32x4{0, 0, 0, 0});
      SB();
      if (t > 0 && i < 4) pwrite(t - 1, i);
      if (t > 0 && i == 4) cnt_add(C_T + 4 * (t - 1));
      load_h(t * 7 + i + HLA);
      if (t == 3 && i == 0) {
        const __amdgpu_buffer_rsrc_t rq = __builtin_amdgcn_make_buffer_rsrc((void*)q, 0, w < 4 ? S_ * D_ * 2 : 0, 0x00020000);
#pragma unroll
        for (int e = 0; e < 4; ++e) qv[e] = __builtin_amdgcn_raw_buffer_load_b64(rq, tk * 1024 + n16 * 8, (rt * 64 + e * 16) * 1024 + ct * 128, 0);
        stf = __builtin_amdgcn_raw_buffer_load_b32(rs_ws, l * 16, OFF_ST + rt * 1024, 16);
        b2v = *(const GAS u32x2*)(b2 + ct * 64 + n16 * 4);
        CFENCE(); SB();
      }
      if (t == 3 && i == 4) { fyv = flag_ld(ws, fy_off); SB(); }
    }
    STAMP(9 + t);
  }
#pragma unroll
  for (int nt = 0; nt < 4; ++nt) pwrite(3, nt);
  cnt_add(C_T + 12);
  if (w < 4) {
    u32 ra0 = tk * 256 + ((n16 ^ (tk & 7)) << 4), ra1 = ra0 + 65536;
    asm volatile("" : "+v"(ra1));
    f32x4 pr[8];
    auto rdp = [&](int e) AI {
      cnt_wait(C_T + 4 * e, 8);
#pragma unroll
      for (int i = 0; i < 8; ++i) pr[i] = lds_ld<f32x4>((i < 4 ? ra0 : ra1) + e * 16384 + (i & 3) * 4096);
      SB();
    };
    STAMP2(32);
    flag_chk(fyv, ws, fy_off, tag);
    STAMP2(33);
    u32x4 yl[2];
#pragma unroll
    for (int i = 0; i < 2; ++i) yl[i] = ld128(rs_ws, l * 16 + i * 1024, OFF_Y + rt * 65536 + ct * 8192 + gw * 2048);
    SB();
    f32x2 xn[4][2];
    {
      u32 sb[4];
      EACH4(sb[j] = __builtin_bit_cast(u32, bperm((j * 16 + tk) * 4, __builtin_bit_cast(float, stf))));
      float iv[4];
      EACH4(iv[j] = __builtin_amdgcn_rcpf(bfhi(sb[j])));
      EACH4((xn[j][0] = bf2(qv[j].x) - sp2(bflo(sb[j])), xn[j][1] = bf2(qv[j].y) - sp2(bflo(sb[j]))));
      EACH4((xn[j][0] *= sp2(iv[j]), xn[j][1] *= sp2(iv[j])));
    }
    const f32x2 bb[2] = {bf2(b2v.x), bf2(b2v.y)};
    cnt_wait(C_S3, 4);
#pragma unroll
    for (int i = 0; i < 2; ++i)
#pragma unroll
      for (int e = 0; e < 2; ++e) lds_st<u32x2>(L_YS + ((gw * 8 + i * 4 + g4) * 66 + n16 * 4 + e * 2) * 4, e ? yl[i].hi : yl[i].lo);
    cnt_add(C_YS);
    cnt_wait(C_YS, 4);
    u32 yv[4][2];
    EACH4((yv[j][0] = lds_ld<u32>(L_YS + (n16 * 132 + j * 16 + tk) * 4), yv[j][1] = lds_ld<u32>(L_YS + (n16 * 132 + 66 + j * 16 + tk) * 4)));
    rdp(0);
    EACH4((xn[j][0] *= bf2(yv[j][0]), xn[j][1] *= bf2(yv[j][1])));
    STAMP2(36);
    u16* op = out + (rt * 64 + tk) * D_ + ct * 64 + n16 * 4;
#pragma unroll
    for (int e = 0; e < 4; ++e) {
      const f32x4 z4 = ((pr[0] + pr[1]) + (pr[2] + pr[3])) + ((pr[4] + pr[5]) + (pr[6] + pr[7]));
      f32x2 z[2] = {z4.lo + bb[0], z4.hi + bb[1]}, p[2];
      asm volatile("" : "+v"(z[0]), "+v"(z[1]));
      SB();
      if (e < 3) rdp(e + 1);
      if (e == 2) STAMP(13);
#pragma unroll
      for (int j = 0; j < 2; ++j) p[j] = exp2x2(z[j] * sp2(-1.4426950408889634f));
#pragma unroll
      for (int j = 0; j < 2; ++j) { z[j] *= xn[e][j]; p[j] = rcpx2(p[j] + sp2(1.f)); }
#pragma unroll
      for (int j = 0; j < 2; ++j) p[j] = fma2(z[j], p[j], bf2(qv[e][j]));
      *(GAS u32x2*)(op + e * 16 * D_) = u32x2{pk2(p[0].x, p[0].y), pk2(p[1].x, p[1].y)};
      SB();
    }
  }
  if (b == 255 && w >= 4) {
    poll_flags(ws, OFF_FY + (gw * 64 + l) * FS, tag);
    arrive_flag(ws, C_EP, 4, OFF_E, tag, l);
  }
  STAMP(14);
  STAMP_END;
}

extern "C" void kernel_launch(void* const* d_in, const int* in_sizes, int n_in, void* d_out, int out_size, void* d_ws, size_t ws_size,
                              hipStream_t stream) {
  char* ws = (char*)d_ws + (WS_TAIL ? ((ws_size - WS_BYTES) & ~(size_t)4095) : 0);
  fused_kernel<<<256, 512, 0, stream>>>((const u16*)d_in[0], (const u16*)d_in[1], (const u16*)d_in[2], (const u16*)d_in[3], (const u16*)d_in[4],
                                        (const u16*)d_in[5], (const u16*)d_in[7], (const u16*)d_in[8], (const u16*)d_in[9], (const u16*)d_in[10],
                                        (u16*)d_out, ws);
}
```

```cpp
#include <hip/hip_runtime.h>
typedef unsigned short u16;
typedef unsigned u32;
typedef unsigned long long u64;
typedef __bf16 bf16x8 __attribute__((ext_vector_type(8)));
typedef __bf16 bf16x2 __attribute__((ext_vector_type(2)));
typedef short s16x4 __attribute__((ext_vector_type(4)));
typedef float f32x4 __attribute__((ext_vector_type(4)));
typedef float f32x2 __attribute__((ext_vector_type(2)));
typedef unsigned u32x4 __attribute__((ext_vector_type(4)));
typedef unsigned u32x2 __attribute__((ext_vector_type(2)));
#define LAS __attribute__((address_space(3)))
#define GAS __attribute__((address_space(1)))
#define DI __device__ __forceinline__
#pragma clang diagnostic ignored "-Wint-to-pointer-cast"

#define FSTR 256
constexpr int S_ = 2048, D_ = 512, F_ = 1792;
constexpr u32 OFF_E = 0, OFF_CS = 16384, OFF_ST = 32768, FS = FSTR  , OFF_FU = 65536, OFF_FW = OFF_FU + 256 * FS, OFF_FH = OFF_FW + 256 * FS, OFF_FY = OFF_FH + 256 * FS;
constexpr u32 OFF_U = 1u << 20, OFF_Y = 3u << 20, OFF_W2T = 5u << 20, OFF_H = 7u << 20, WS_BYTES = 14u << 20;
constexpr u32 SLOT = 32768, L_PK = 131072, L_PV = 140288, L_PW = 148480, L_SK = 156928, L_CNT = 157696,
              L_RC = 157824  , L_CC = 158848  , L_CSX = 159872,
              L_HC0 = 131072, L_HC1 = 0, HCS = 4160, L_UR = 147712, URS = 4576, L_CP = 32768, L_YS = 131072, L_QS = 147712;
constexpr u32 C_FULL = L_CNT  , C_FREE = L_CNT + 48  , C_H = L_CNT + 80, C_W = L_CNT + 84, C_U = L_CNT + 88, C_Y = L_CNT + 92,
              C_S1 = L_CNT + 96, C_S2 = L_CNT + 100, C_S3 = L_CNT + 104, C_SA = L_CNT + 36  , C_T = L_CNT + 112  , C_YS = L_CNT + 108, C_KV = L_CNT + 44, C_TG = L_CNT + 32, C_EP = L_CNT + 80  , L_B1 = 157440, L_EP = 163072;
#define PSLEEP 8
#define NT_G 0
#define NT_P 0
#define WS_TAIL 0
#define BPRIO 1
#define NWB 28
#define WDR 1
#define SPQ 32
#define KVD -1
#define KROT 0
#define DEEP 0
#define PREPS 6
#define LPRIO 2
#define WGO 5
#define PFIRST 0
#define UDR 2
#define LFIRST 0
#define APRIO 1
#define CPS 4
#define HL0 8
#define YDR 0
#define NOPK 0
#define CPF 1
#define XNF 0
#define KVP 1
#define KVC 0
#define NWK 12
#define UQ 16
#define NWL 20
#define TCH 0
#define KW 1
#define CODE_BYTES 30720
#define G2P 1
#define YFL 24
#define YCK 23
#define HLA 7

#define STAMP(i)
#define STAMP_INIT
#define STAMP_END
#define STAMP2(i)

template <int V> struct IC { static constexpr int v = V; };
template <int I, int N, class F> DI void sfor(F&& f) { if constexpr (I < N) { f(IC<I>{}); sfor<I + 1, N>(f); } }
DI u32x4 mk_srd(const void* p, u32 bytes) { u64 a = (u64)p; return u32x4{(u32)a, (u32)(a >> 32) & 0xffffu, bytes, 0x00020000u}; }
template <int NT> DI void dma16(u32x4 srd, u32 voff, u32 soff, u32 lds) {
  if (NT) asm volatile("s_mov_b32 m0, %3\n\ts_nop 0\n\tbuffer_load_dwordx4 %0, %1, %2 offen nt lds" ::"v"(voff), "s"(srd), "s"(soff), "s"(lds) : "memory");
  else asm volatile("s_mov_b32 m0, %3\n\ts_nop 0\n\tbuffer_load_dwordx4 %0, %1, %2 offen lds" ::"v"(voff), "s"(srd), "s"(soff), "s"(lds) : "memory");
}
template <int NT, int I> DI void dma16g(u32x4 srd, u32 voff, u32 soff, u32 lds) {
  if (I == 0) dma16<NT>(srd, voff, soff, lds);
  else if (NT) asm volatile("buffer_load_dwordx4 %0, %1, %2 offen offset:%3 nt lds" ::"v"(voff), "s"(srd), "s"(soff), "i"(I * 1024) : "memory");
  else asm volatile("buffer_load_dwordx4 %0, %1, %2 offen offset:%3 lds" ::"v"(voff), "s"(srd), "s"(soff), "i"(I * 1024) : "memory");
}
DI void dma16m(u32x4 srd, u32 voff, u32 soff, u32 lds, u64 mask) {
  asm volatile("s_mov_b32 m0, %3\n\ts_mov_b64 exec, %4\n\tbuffer_load_dwordx4 %0, %1, %2 offen lds\n\ts_mov_b64 exec, -1"
               ::"v"(voff), "s"(srd), "s"(soff), "s"(lds), "s"(mask) : "memory");
}
template <int N> DI void waitv() { asm volatile("s_waitcnt vmcnt(%0)" ::"i"(N) : "memory"); }
#define WAITL0() asm volatile("s_waitcnt lgkmcnt(0)" ::: "memory")
#define CFENCE() asm volatile("" ::: "memory")
#define SB() __builtin_amdgcn_sched_barrier(0)
DI void bar() { SB(); CFENCE(); __builtin_amdgcn_s_waitcnt(0xC07F); __builtin_amdgcn_s_barrier(); CFENCE(); SB(); }
DI float bflo(u32 x) { return __uint_as_float(x << 16); }
DI float bfhi(u32 x) { return __uint_as_float(x & 0xffff0000u); }
DI u32 pk2(float a, float b) { f32x2 p = {a, b}; return __builtin_bit_cast(u32, __builtin_convertvector(p, bf16x2)); }
DI float rb(float a) { return bflo(pk2(a, a)); }
DI f32x2 bf2(u32 x) { return f32x2{bflo(x), bfhi(x)}; }
DI f32x2 rb2(f32x2 v) { return bf2(pk2(v.x, v.y)); }
DI f32x2 sp2(float v) { return f32x2{v, v}; }
DI f32x2 exp2x2(f32x2 v) { return f32x2{__builtin_amdgcn_exp2f(v.x), __builtin_amdgcn_exp2f(v.y)}; }
DI f32x2 rcpx2(f32x2 v) { return f32x2{__builtin_amdgcn_rcpf(v.x), __builtin_amdgcn_rcpf(v.y)}; }
DI f32x2 fma2(f32x2 a, f32x2 b, f32x2 c) { return __builtin_elementwise_fma(a, b, c); }
DI float sfma(float a, float b, float c) { float d; asm("v_fma_f32 %0, %1, %2, %3" : "=v"(d) : "v"(a), "v"(b), "v"(c)); return d; }
DI float smul(float a, float b) { float d; asm("v_mul_f32 %0, %1, %2" : "=v"(d) : "v"(a), "v"(b)); return d; }
DI float sadd(float a, float b) { float d; asm("v_add_f32 %0, %1, %2" : "=v"(d) : "v"(a), "v"(b)); return d; }
DI float ssub(float a, float b) { float d; asm("v_sub_f32 %0, %1, %2" : "=v"(d) : "v"(a), "v"(b)); return d; }
DI f32x2 fma2s(f32x2 a, f32x2 b, f32x2 c) { return f32x2{sfma(a.x, b.x, c.x), sfma(a.y, b.y, c.y)}; }
DI f32x2 mul2s(f32x2 a, f32x2 b) { return f32x2{smul(a.x, b.x), smul(a.y, b.y)}; }
template <class T> DI T lds_ld(u32 off) { return *(const LAS T*)(off); }
template <class T> DI void lds_st(u32 off, T v) { *(LAS T*)(off) = v; }
DI u32x2 lds_tr(u32 off) { return __builtin_bit_cast(u32x2, __builtin_amdgcn_ds_read_tr16_b64_v4i16((LAS s16x4*)(off))); }
DI u32x4 lds_tr2(u32 o0, u32 o1) { u32x2 a = lds_tr(o0), b = lds_tr(o1); return u32x4{a.x, a.y, b.x, b.y}; }
DI u32x4 lds_2x64(u32 o) { u32x2 a = *(const volatile LAS u32x2*)(o), b = *(const volatile LAS u32x2*)(o + 8); return u32x4{a.x, a.y, b.x, b.y}; }
DI f32x4 mfma(u32x4 a, u32x4 b, f32x4 c) {
  return __builtin_amdgcn_mfma_f32_16x16x32_bf16(__builtin_bit_cast(bf16x8, a), __builtin_bit_cast(bf16x8, b), c, 0, 0, 0);
}
DI void st128(u32x4 v, __amdgpu_buffer_rsrc_t rs, u32 voff, u32 soff) {
  __builtin_amdgcn_raw_buffer_store_b128(v, rs, voff, soff, 16);
  asm volatile("s_nop 1" ::"v"(v) : "memory");
}
DI u32x4 ld128(__amdgpu_buffer_rsrc_t rs, u32 voff, u32 soff) { return __builtin_amdgcn_raw_buffer_load_b128(rs, voff, soff, 16); }
template <int C> DI float dpp_add(float v) {
  return v + __builtin_bit_cast(float, __builtin_amdgcn_update_dpp(0, __builtin_bit_cast(int, v), C, 0xf, 0xf, true));
}
DI float bperm(int src_lane4, float v) { return __builtin_bit_cast(float, __builtin_amdgcn_ds_bpermute(src_lane4, __builtin_bit_cast(int, v))); }
DI float sel(bool c, float a, float b) { return c ? a : b; }
DI float rl(float v, int i) { return __builtin_bit_cast(float, __builtin_amdgcn_readlane(__builtin_bit_cast(int, v), i)); }
DI float wave_sum(float v) {
  v = dpp_add<0xB1>(v); v = dpp_add<0x4E>(v); v = dpp_add<0x141>(v); v = dpp_add<0x140>(v);
  return (rl(v, 0) + rl(v, 16)) + (rl(v, 32) + rl(v, 48));
}
constexpr int SPIN_CAP = 1 << 16;
DI u64 flag_ld(const char* ws, u32 off) { return __hip_atomic_load((const GAS u64*)(ws + off), __ATOMIC_RELAXED, __HIP_MEMORY_SCOPE_AGENT); }
DI void poll_flags(const char* ws, u32 off, u64 tag) {
#pragma unroll 1
  for (int it = 0; it < SPIN_CAP; ++it) {
    if (__all(flag_ld(ws, off) == tag)) break;
    __builtin_amdgcn_s_sleep(PSLEEP);
  }
  CFENCE();
}
DI void flag_chk(u64 v, const char* ws, u32 off, u64 tag) { if (!__all(v == tag)) poll_flags(ws, off, tag); CFENCE(); }
DI void set_flag(char* ws, u32 off, u64 tag) { __hip_atomic_store((GAS u64*)(ws + off), tag, __ATOMIC_RELAXED, __HIP_MEMORY_SCOPE_AGENT); }
DI void arrive_flag(char* ws, u32 cnt, u32 n, u32 off, u64 tag, int l) {
  if (l == 0) {
    u32 old;
    asm volatile("ds_add_rtn_u32 %0, %1, %2\n\ts_waitcnt lgkmcnt(0)" : "=&v"(old) : "v"(cnt), "v"(1u) : "memory");
    if (old == n - 1) set_flag(ws, off, tag);
  }
  CFENCE();
}
DI void cnt_add(u32 cnt) { asm volatile("s_mov_b64 exec, 1\n\tds_add_u32 %0, %1\n\ts_mov_b64 exec, -1" ::"v"(cnt), "v"(1u) : "memory"); }
DI u32 cnt_ld(u32 cnt) { return *(const volatile LAS u32*)cnt; }
DI void cnt_wait(u32 cnt, u32 n) {
#pragma unroll 1
  for (int it = 0; it < (1 << 22); ++it) { if ((u32)__builtin_amdgcn_readfirstlane(cnt_ld(cnt)) >= n) break; __builtin_amdgcn_s_sleep(1); }
  CFENCE(); SB();
}
DI void group_sync(u32 cnt) { SB(); WAITL0(); cnt_add(cnt); cnt_wait(cnt, 4); }
#define AI __attribute__((always_inline))
#define EACH4(stmt) do { _Pragma("unroll") for (int j = 0; j < 4; ++j) { stmt; } SB(); } while (0)
#define HALF(v, j) (((j) & 1) ? (v).hi : (v).lo)

#define KATTR
__global__ __launch_bounds__(512, 2) KATTR void fused_kernel(const u16* __restrict__ q, const u16* __restrict__ kk, const u16* __restrict__ vv,
                                                       const u16* __restrict__ sk, const u16* __restrict__ ssk, const u16* __restrict__ sD,
                                                       const u16* __restrict__ w1, const u16* __restrict__ b1, const u16* __restrict__ w2,
                                                       const u16* __restrict__ b2, u16* __restrict__ out, char* __restrict__ ws) {
  __shared__ __attribute__((aligned(16))) char sm[163840];
  asm volatile("" ::"v"(sm) : "memory");
  const int tid = threadIdx.x, l = tid & 63;
  const int w = __builtin_amdgcn_readfirstlane(tid >> 6);
  const int b = blockIdx.x, xcd = b & 7, r = b >> 3;
  const bool mainwg = r < 28;
  const int rp = (xcd & 3) * 4 + (r & 3), cc = (xcd >> 2) * 7 + (r >> 2);
  const int rt = (xcd & 3) * 8 + (xcd >> 2) * 4 + (r & 3), ct = r >> 2;
  const int gw = w & 3;
  const int n16 = l & 15, g4 = l >> 4;
  STAMP_INIT
  STAMP(0);
  {
    u64 pc;
    asm volatile("s_getpc_b64 %0" : "=s"(pc));
    asm volatile("s_mov_b32 m0, %3\n\ts_mov_b64 exec, 0xff\n\tbuffer_load_dword %0, %1, %2 offen lds\n\tds_write_b128 %4, %5\n\ts_mov_b64 exec, -1"
                 ::"v"(l * 128), "s"(mk_srd((const void*)pc, CODE_BYTES)), "s"((r & 3) * 8192 + w * 1024), "s"(162816u), "v"(L_CNT + l * 16), "v"(u32x4{0, 0, 0, 0}) : "memory");
  }
  u32 vx0 = (l >> 3) * 1024 + (((l & 7) ^ (l >> 3)) << 4), vx1 = vx0 + 8192 - 1024;
  const u32 vwb = (g4 + 1) * (F_ * 2) - 2048, vws = (n16 ^ (g4 << 1)) << 4;
  u32 vo0 = vwb + (vws ^ ((w & 1) << 7)), vo1 = vo0 + 4 * (F_ * 2) - 1024;
  u32 sxw = rp * 131072 + w * 16384, sww = w * 8 * (F_ * 2) + cc * 256;
  asm volatile("" ::"s"(sxw), "s"(sww), "v"(vx0), "v"(vx1), "v"(vo0), "v"(vo1));
  SB();
  asm volatile("" ::"s"(q), "s"(kk), "s"(vv), "s"(sk), "s"(ssk), "s"(sD), "s"(w1), "s"(b1), "s"(w2), "s"(b2), "s"(out), "s"(ws));
  asm volatile("s_mov_b32 m0, %2\n\ts_mov_b64 exec, 3\n\tbuffer_load_dword %0, %1, 0 offen sc1 lds\n\ts_mov_b64 exec, -1" ::"v"(l * 4), "s"(mk_srd(ws, 8)), "s"(L_EP) : "memory");
  const int srow = rt * 64 + ct * 8 + gw * 2;
  asm volatile("s_mov_b32 m0, %3\n\ts_mov_b64 exec, %4\n\tbuffer_load_dwordx4 %0, %1, %2 offen lds\n\tbuffer_load_dwordx4 %0, %1, %2 offen offset:1024 lds\n\ts_mov_b64 exec, -1"
               ::"v"(l * 16), "s"(mk_srd(q, S_ * D_ * 2)), "s"(srow * 1024), "s"(L_PW + gw * 2048), "s"(w < 4 ? ~0ull : 1ull) : "memory");
  if (mainwg) {
    const u32x4 sq_ = mk_srd(q, S_ * D_ * 2), sw_ = mk_srd((const char*)w1 - F_ * 2, D_ * F_ * 2 + F_ * 2);
#pragma unroll
    for (int s = 0; s < 2; ++s) {
      dma16g<NT_G, 0>(sq_, vx0, sxw + s * 128, s * SLOT + w * 4096); dma16g<NT_G, 1>(sq_, vx1, sxw + s * 128, 0);
      dma16g<NT_G, 2>(sw_, vo0, sww + s * 64 * (F_ * 2), 0); dma16g<NT_G, 3>(sw_, vo1, sww + s * 64 * (F_ * 2), 0);
      if (s == 0) bar();
    }
  } else bar();
  SB();
  STAMP2(46);
  const u32 vr = (l >> 3) * 1024 + (l & 7) * 16;
  const u32x4 srd_q = mk_srd(q, S_ * D_ * 2);
  const u32x4 srd_w1m = mk_srd((const char*)w1 - F_ * 2, D_ * F_ * 2 + F_ * 2);
  u64 tag;
  auto get_tag = [&]() AI {
    const u32x2 ev = *(const volatile LAS u32x2*)L_EP;
    tag = (((u64)__builtin_amdgcn_readfirstlane(ev.y) << 32) | (u32)__builtin_amdgcn_readfirstlane(ev.x)) + 0x9E3779B97F4A7C15ull;
  };
  const __amdgpu_buffer_rsrc_t rs_ws = __builtin_amdgcn_make_buffer_rsrc(ws, 0, WS_BYTES, 0x00020000);
  u32x4 w2f[28];
  auto load_w2 = [&](int j) AI { w2f[j] = ld128(rs_ws, l * 16 + (j & 3) * 1024, OFF_W2T + (ct * 56 + 7 * w + (j >> 2)) * 4096); SB(); };
  const u32 fw_off = OFF_FW + (4 * w + (ct >> 1)) * FS;
  const u32 fh_off = OFF_FH + ((rt >> 1) * 16 + min(((7 * w) >> 2) + (l & 3), (7 * w + 6) >> 2)) * FS;

  const u32 sx32 = ((2 * g4 + (n16 >> 3)) & 3) << 5, tr0 = (4 * g4 + (n16 >> 2)) * 128 + (l & 3) * 8, vsw = ((l & 7) ^ ((l >> 3) & 6)) << 4;
  const int sp = (r - 28) * 8 + xcd;
  if (w >= 4) {
    u32x4 ha[2]; u32 hn[2], dd;
    auto load_taps = [&]() AI {
      asm volatile("buffer_load_dwordx4 %0, %4, %5, %6 offen nt\n\tbuffer_load_dwordx4 %1, %4, %5, %6 offen offset:1024 nt\n\t"
                   "buffer_load_dword %2, %4, %5, %6 offen offset:16\n\tbuffer_load_dword %3, %4, %5, %6 offen offset:1040"
                   : "=&v"(ha[0]), "=&v"(ha[1]), "=&v"(hn[0]), "=&v"(hn[1]) : "v"(l * 16), "s"(mk_srd(ssk, S_ * D_ * 2)), "s"(b * 8192 + gw * 2048) : "memory");
      asm volatile("buffer_load_ushort %0, off, %1, %2" : "=v"(dd) : "s"(mk_srd(sD, D_ * 2)), "s"(b * 4 + (gw >> 1) * 2) : "memory");
    };
    __builtin_amdgcn_s_setprio(LPRIO);
    const u32 vw[4] = {vwb + vws, vwb + vws + 4 * (F_ * 2) - 1024, vwb + (vws ^ 128), vwb + (vws ^ 128) + 4 * (F_ * 2) - 1024};
    auto regions = [&](int s, int r0, auto N) AI {
      const u32 sx = rp * 131072 + r0 * 16384 + s * 128, sw = (r0 * 8 + s * 64) * (F_ * 2) + cc * 256, dst = (s & 3) * SLOT + r0 * 4096;
#pragma unroll
      for (int i = 0; i < N.v; ++i) {
        dma16g<NT_G, 0>(srd_q, vx0, sx + i * 16384, dst + i * 4096); dma16g<NT_G, 1>(srd_q, vx1, sx + i * 16384, 0);
        dma16g<NT_G, 2>(srd_w1m, vw[(i & 1) * 2], sw + i * 8 * (F_ * 2), 0); dma16g<NT_G, 3>(srd_w1m, vw[(i & 1) * 2 + 1], sw + i * 8 * (F_ * 2), 0);
      }
    };
    auto stage = [&](int s) AI { regions(s, gw * 4, IC<4>{}); };
    if (mainwg) {
      regions(2, gw * 2, IC<2>{}); waitv<12>(); cnt_add(C_FULL);
      regions(3, gw * 2, IC<2>{}); waitv<16>(); cnt_add(C_FULL + 4);
    }
    if (gw < 2) {
      if (mainwg) { waitv<8>(); cnt_add(C_FULL + 8); } else waitv<0>();
      cnt_add(C_TG);
      if (mainwg) {
        u32x4 g;
#pragma unroll 1
        for (int s = 4; s < 8; ++s) {
          cnt_wait(C_FREE + 4 * (s - 4), 4);
          if (s == 7) {
            asm volatile("buffer_load_dwordx4 %0, %1, %2, %3 offen sc1" : "=v"(g) : "v"(l * 16), "s"(mk_srd(ws, WS_BYTES)), "s"(OFF_ST + rp * 2048 + gw * 1024) : "memory");
            dma16m(mk_srd(b1, F_ * 2), l * 16, cc * 256, L_B1, 0xffffull);
          }
          stage(s);
          waitv<16>(); cnt_add(C_FULL + 4 * (s - 1));
        }
        STAMP2(41);
        get_tag();
        SB();
        asm volatile("" : "+v"(g));
        load_taps();
        auto g_ok = [&]() AI { return __all((g.y == (u32)tag) & (g.w == (u32)(tag >> 32))); };
        if (!g_ok()) {
#pragma unroll 1
          for (int it = 0; it < SPIN_CAP; ++it) { g = ld128(rs_ws, l * 16, OFF_ST + rp * 2048 + gw * 1024); if (g_ok()) break; __builtin_amdgcn_s_sleep(4); CFENCE(); }
        }
        const float i2 = __builtin_amdgcn_rcpf(bfhi(g.z));
        float ra = __builtin_amdgcn_rcpf(bfhi(g.x)) * i2;
        asm volatile("" : "+v"(ra));
        lds_st<f32x2>(L_RC + (gw * 64 + l) * 8, f32x2{ra, -bflo(g.x) * ra - bflo(g.z) * i2});
        lds_st<u32>(L_CC + (gw * 64 + l) * 4, (u32)lds_ld<u16>(L_B1 + (gw * 64 + l) * 2) << 16);
        waitv<5>();
        cnt_add(C_FULL + 28);
        STAMP2(42);
      } else { load_taps(); waitv<0>(); get_tag(); }
      cnt_add(C_KV);
    } else {
      waitv<8>(); cnt_add(C_FULL + 8);
      const int pw = gw & 1;
      const u32 so = (rt * 64 + pw * 32) * 1024 + ct * 128;
      const bool top = rt == 0 && pw == 0;
      sfor<0, 4>([&](auto I) AI { dma16g<NT_P, I.v>(mk_srd(kk, S_ * D_ * 2), vr, so + I.v * 7168, L_PK + 1024 + pw * 4096); });
      dma16m(mk_srd(kk, top ? 0 : S_ * D_ * 2), vr, top ? 0 : so - 8192, L_PK + pw * 4096, ~0ull << 40);
      dma16m(mk_srd(sk, D_ * 8), l * 16, ct * 512, L_SK, 0xffffffffull);
      sfor<0, 4>([&](auto I) AI { dma16g<NT_P, I.v>(mk_srd(vv, S_ * D_ * 2), vr, so + I.v * 7168, L_PV + pw * 4096); });
      waitv<10>(); cnt_add(C_FULL + 12);
      waitv<0>();
      get_tag();
      load_taps();
      STAMP2(41);
      const int cpl = l & 31, hh = l >> 5;
      const u32x4 sv = lds_ld<u32x4>(L_SK + cpl * 16);
      const f32x2 kT[4] = {f32x2{bflo(sv.x), bflo(sv.z)}, f32x2{bfhi(sv.x), bfhi(sv.z)}, f32x2{bflo(sv.y), bflo(sv.w)}, f32x2{bfhi(sv.y), bfhi(sv.w)}};
      u32 a = L_PK + (pw * 32 + hh * 16) * 128 + cpl * 4;
      asm volatile("" : "+v"(a));
      u32 kr[19], vq[16];
#pragma unroll
      for (int i = 0; i < 19; ++i) kr[i] = lds_ld<u32>(a + 5 * 128 + i * 128);
#pragma unroll
      for (int i = 0; i < 16; ++i) vq[i] = lds_ld<u32>(a + (L_PV - L_PK) + i * 128);
      SB();
      f32x2 kK[19];
#pragma unroll
      for (int i = 0; i < 3; ++i) kK[i] = bf2(kr[i]);
      u32x4 uo[4];
#pragma unroll
      for (int u = 0; u < 4; ++u) {
        f32x2 ka[4];
        EACH4(kK[4 * u + 3 + j] = bf2(kr[4 * u + 3 + j]));
        EACH4(ka[j] = mul2s(kK[4 * u + j], kT[3])); EACH4(ka[j] = fma2s(kK[4 * u + j + 1], kT[2], ka[j]));
        EACH4(ka[j] = fma2s(kK[4 * u + j + 2], kT[1], ka[j])); EACH4(ka[j] = fma2s(kK[4 * u + j + 3], kT[0], ka[j]));
        EACH4(ka[j] = rb2(ka[j])); EACH4(ka[j] = mul2s(ka[j], bf2(vq[4 * u + j])));
        uo[u] = u32x4{pk2(ka[3].x, ka[2].x), pk2(ka[1].x, ka[0].x), pk2(ka[3].y, ka[2].y), pk2(ka[1].y, ka[0].y)};
        SB();
      }
#pragma unroll
      for (int u = 0; u < 4; ++u) lds_st<u32x4>(L_PV + pw * 4096 + cpl * 128 + (((hh * 4 + u) ^ (cpl & 7)) << 4), uo[u]);
#pragma unroll
      for (int i = 0; i < 4; ++i) uo[i] = lds_ld<u32x4>(L_PV + pw * 4096 + i * 1024 + (l >> 3) * 128 + (((l & 7) ^ (l >> 3)) << 4));
#pragma unroll
      for (int i = 0; i < 4; ++i) st128(uo[i], rs_ws, (l >> 3) * 8192 + (l & 7) * 16, OFF_U + ((ct * 32 + 8 * i) * 512 + rt * 16 + pw * 8) * 16);
      cnt_add(C_KV);
      STAMP2(42);
      waitv<0>();
      cnt_wait(C_TG, 6);
      arrive_flag(ws, C_U, 2, OFF_FU + (ct * 32 + rt) * FS, tag, l);
    }
    STAMP(2);
    if (!mainwg) cnt_wait(C_W, 4);
    poll_flags(ws, fw_off, tag);
#pragma unroll
    for (int j = 0; j < NWK; ++j) load_w2(j);
    STAMP2(43);
    __builtin_amdgcn_s_setprio(3);
    cnt_wait(C_KV, 4);
    if (mainwg) cnt_wait(C_FREE + 4 * CPS, 4);
    asm volatile("" : "+v"(ha[0]), "+v"(ha[1]), "+v"(hn[0]), "+v"(hn[1]), "+v"(dd));
    {
      const u32 hb = (gw >> 1) ? L_HC1 : L_HC0;
      const u32 h0 = pk2(0.f, bflo(ha[0].x) + bflo(dd));
      if ((tid & 127) == 0) ha[0].x = __builtin_amdgcn_perm(ha[0].x, h0, 0x07060302u);
      const u32 h0u = __builtin_amdgcn_readfirstlane(h0);
      if (gw & 1) { if (l < 60) lds_st<u32x2>(L_UR + (gw >> 1) * URS + 4096 + l * 8, u32x2{0, 0}); }
      else if (l < 31 && l != 23) lds_st<u32>(hb + (l >> 3) * HCS + (l & 7) * 4, (l == 15 || l == 30) ? h0u : 0u);
#pragma unroll
      for (int i = 0; i < 2; ++i) {
        const u32x4 x = ha[i];
        const u32x4 o = {__builtin_amdgcn_alignbit(x.y, x.x, 16), __builtin_amdgcn_alignbit(x.z, x.y, 16), __builtin_amdgcn_alignbit(x.w, x.z, 16), __builtin_amdgcn_alignbit(hn[i], x.w, 16)};
        const u32 d = hb + 32 + (gw & 1) * 2048 + i * 1024 + l * 16, d2 = d + 2 * HCS - 4, d3 = d + 3 * HCS - 4;
        lds_st<u32x4>(d, x); lds_st<u32x4>(d + HCS, o);
        asm volatile("ds_write2_b32 %0, %2, %3 offset1:1\n\tds_write2_b32 %0, %4, %5 offset0:2 offset1:3\n\tds_write2_b32 %1, %6, %7 offset1:1\n\tds_write2_b32 %1, %8, %9 offset0:2 offset1:3"
                     ::"v"(d2), "v"(d3), "v"(x.x), "v"(x.y), "v"(x.z), "v"(x.w), "v"(o.x), "v"(o.y), "v"(o.z), "v"(o.w) : "memory");
      }
    }
    STAMP2(31);
    if (!mainwg) __builtin_amdgcn_s_sleep(SPQ);
    const u32 fu_off = OFF_FU + ((b >> 5) * 32 + 8 * gw + (l & 7)) * FS;
#pragma unroll
    for (int j = NWK; j < NWL; ++j) load_w2(j);
    poll_flags(ws, fu_off, tag);
    STAMP2(37);
    u32x4 uu[2];
#pragma unroll
    for (int i = 0; i < 2; ++i) uu[i] = ld128(rs_ws, l * 16 + i * 1024, OFF_U + b * 8192 + gw * 2048);
    SB();
#pragma unroll
    for (int j = NWL; j < 28; ++j) load_w2(j);
#pragma unroll
    for (int i = 0; i < 2; ++i) {
      const u32 a = L_UR + (2044 - 4 * (gw * 128 + i * 64 + l)) * 2;
      lds_st<u32x2>(a, u32x2{uu[i].x, uu[i].y}); lds_st<u32x2>(a + URS, u32x2{uu[i].z, uu[i].w});
    }
    cnt_add(C_S2);
    STAMP(4);
    cnt_wait(C_S2, mainwg && !NOPK ? 8 : 4);
    STAMP(5);
    {
      const int ch = gw & 1, kh = gw >> 1;
      f32x4 ca[8];
#pragma unroll
      for (int i = 0; i < 8; ++i) ca[i] = f32x4{0, 0, 0, 0};
      const u32 ab = (ch ? L_HC1 : L_HC0) + ((n16 + 1) & 3) * HCS + (((n16 + 1) & ~3) + 8 * g4) * 2 + kh * 256;
      const u32 bb = L_UR + ch * URS + (2032 - 1792 - 16 * n16 + 8 * g4) * 2 + kh * 256;
      const u32 pa = L_CP + gw * 8192 + (4 * n16 + (g4 ^ ((n16 >> 1) & 3))) * 16;
      u32x4 AL[2][4], BL[2][4], AH[4], BH[4];
      auto rdp = [&](u32x4& a, u32x4& bq, int k0, int i) AI { SB(); a = lds_2x64(ab + k0 * 64 + i * 512); bq = lds_ld<u32x4>(bb + k0 * 64 + (7 - i) * 512); SB(); };
#pragma unroll
      for (int i = 0; i < 4; ++i) rdp(AL[0][i], BL[0][i], 0, i);
      sfor<0, 4>([&](auto K) AI {
        constexpr int k0 = K.v;
        u32x4 (&A)[4] = AL[k0 & 1]; u32x4 (&B)[4] = BL[k0 & 1];
#pragma unroll
        for (int n = 0; n < 16; ++n) {
          constexpr int oi[16] = {0, 0, 1, 0, 1, 2, 0, 1, 2, 3, 1, 2, 3, 2, 3, 3}, ot[16] = {0, 1, 0, 2, 1, 0, 3, 2, 1, 0, 3, 2, 1, 3, 2, 3};
          ca[oi[n] + ot[n]] = mfma(A[oi[n]], B[ot[n]], ca[oi[n] + ot[n]]);
          if (n < 8 && (n & 1)) rdp(AH[n >> 1], BH[n >> 1], k0, 4 + (n >> 1));
          if (k0 == 3 && n >= 12) { SB(); lds_st<f32x4>(pa + (n - 12) * 1024, ca[n - 12]); SB(); }
        }
        int cn = 0;
#pragma unroll
        for (int T = 0; T < 4; ++T)
#pragma unroll
          for (int i = 0; i <= T; ++i) {
            ca[4 + T] = mfma(A[i], BH[T - i], ca[4 + T]); ca[4 + T] = mfma(AH[T - i], B[i], ca[4 + T]);
            if (k0 < 3 && cn >= 2 && cn < 6) rdp(AL[(k0 + 1) & 1][cn - 2], BL[(k0 + 1) & 1][cn - 2], k0 + 1, cn - 2);
            if (k0 == 3 && T > 0 && i == 1) { SB(); lds_st<f32x4>(pa + (3 + T) * 1024, ca[3 + T]); SB(); }
            ++cn;
          }
        SB();
      });
      lds_st<f32x4>(pa + 7 * 1024, ca[7]);
    }
    cnt_add(C_S3);
    __builtin_amdgcn_s_setprio(0);
    STAMP(6);
    const u64 fhv = flag_ld(ws, fh_off);
    cnt_wait(C_S3, 4);
    {
      const int t2 = tid & 255;
      f32x4 pr[2][4];
#pragma unroll
      for (int i = 0; i < 2; ++i)
#pragma unroll
        for (int p = 0; p < 4; ++p) pr[i][p] = lds_ld<f32x4>(L_CP + ((t2 & ~3) | ((t2 & 3) ^ ((t2 >> 3) & 3))) * 16 + i * 4096 + p * 8192);
      cnt_add(C_S1);
      SB();
#pragma unroll
      for (int i = 0; i < 2; ++i) {
        const f32x4 y0 = pr[i][0] + pr[i][2], y1 = pr[i][1] + pr[i][3];
        st128(u32x4{pk2(y0[0], y1[0]), pk2(y0[1], y1[1]), pk2(y0[2], y1[2]), pk2(y0[3], y1[3])}, rs_ws, (t2 >> 4) * 65536 + (t2 & 15) * 16, OFF_Y + b * 256 + i * 16 * 65536);
      }
    }
    STAMP2(29);
    if (YDR < 0) { waitv<0>(); arrive_flag(ws, C_Y, 4, OFF_FY + b * FS, tag, l); }
    flag_chk(fhv, ws, fh_off, tag);
  } else {
    {
      __builtin_amdgcn_s_setprio(3);
      if (!mainwg) {
#pragma unroll
        for (int p = 0; p < 4; ++p) {
          const int hp = gw * 4 + p;
          sfor<0, 4>([&](auto I) AI { dma16g<NT_P, I.v>(mk_srd(w2, hp < 14 ? D_ * F_ * 2 : 0), (l >> 3) * 1024 + vsw,
                                                       ((sp >> 2) * 7 + (hp >> 1)) * 32768 + (sp & 3) * 256 + (hp & 1) * 128 + I.v * 7168, hp * 4096); });
        }
      }
      if (mainwg) waitv<8>(); else waitv<16>();
      get_tag(); cnt_add(C_TG);
      STAMP2(40);
    {
      const u32x4 x0[2] = {lds_ld<u32x4>(L_PW + gw * 2048 + l * 16), lds_ld<u32x4>(L_PW + gw * 2048 + 1024 + l * 16)};
      float c0[2], sa[2], sq[2];
#pragma unroll
      for (int i = 0; i < 2; ++i) {
        c0[i] = bflo(__builtin_amdgcn_readfirstlane(x0[i].x));
        const f32x2 d[4] = {bf2(x0[i].x) - sp2(c0[i]), bf2(x0[i].y) - sp2(c0[i]), bf2(x0[i].z) - sp2(c0[i]), bf2(x0[i].w) - sp2(c0[i])};
        const f32x2 s2 = (d[0] + d[1]) + (d[2] + d[3]), q2 = fma2(d[0], d[0], d[1] * d[1]) + fma2(d[2], d[2], d[3] * d[3]);
        sa[i] = s2.x + s2.y; sq[i] = q2.x + q2.y;
      }
#define RED4(C) do { sa[0] = dpp_add<C>(sa[0]); sq[0] = dpp_add<C>(sq[0]); sa[1] = dpp_add<C>(sa[1]); sq[1] = dpp_add<C>(sq[1]); SB(); } while (0)
      SB(); RED4(0xB1); RED4(0x4E); RED4(0x141); RED4(0x140); RED4(0x142); RED4(0x143);
      const bool odd = l & 1;
      const float md = sel(odd, sa[1], sa[0]) * (1.f / D_), mean = sel(odd, c0[1], c0[0]) + md, var = fmaxf(sel(odd, sq[1], sq[0]) * (1.f / D_) - md * md, 0.f);
      const float m1 = rb(mean), sd1 = rb(__builtin_amdgcn_sqrtf(rb(rb(var) + 1e-5f)));
      const float i1 = __builtin_amdgcn_rcpf(sd1);
      const float m2 = (mean - m1) * i1, sd2 = __builtin_amdgcn_sqrtf(rb(rb(var * i1 * i1) + 1e-5f));
      if (l >= 62) st128(u32x4{pk2(m1, sd1), (u32)tag, pk2(m2, sd2), (u32)(tag >> 32)}, rs_ws, (l - 62) * 16, OFF_ST + srow * 16);
    }

      __builtin_amdgcn_s_setprio(APRIO);
      if (!mainwg) {
        waitv<1>();
        SB();
#pragma unroll
        for (int p = 0; p < 4; ++p) {
          const int hp = gw * 4 + p;
          u32x4 wt[4];
#pragma unroll
          for (int nt = 0; nt < 4; ++nt) { const u32 a = hp * 4096 + tr0 + ((nt * 32) ^ sx32); wt[nt] = lds_tr2(a, a + 2048); }
#pragma unroll
          for (int nt = 0; nt < 4; ++nt)
            st128(wt[nt], rs_ws, l * 16, hp < 14 ? OFF_W2T + ((((sp & 3) * 2 + (hp & 1)) * 56 + (sp >> 2) * 7 + (hp >> 1)) * 4 + nt) * 1024 : 0x7f000000u);
        }
        waitv<0>();
        arrive_flag(ws, C_W, 4, OFF_FW + sp * FS, tag, l);
        STAMP2(42);
      }
    }
    if (mainwg) {
      const int th = gw >> 1, fh = gw & 1;
      f32x4 acc[4][4];
#pragma unroll
      for (int i = 0; i < 16; ++i) acc[i >> 2][i & 3] = f32x4{0, 0, 0, 0};
      const u32 xa = th * 16384 + n16 * 128 + ((g4 ^ (n16 & 7)) << 4);
      const u32 wa = g4 * 4096 + 2048 + (n16 >> 2) * 256 + (((4 * fh + 2 * th) ^ ((n16 >> 2) | ((g4 & 1) << 2))) << 5) + (l & 3) * 8;
      u32 ad[2][6];
#pragma unroll
      for (int h = 0; h < 2; ++h) {
#pragma unroll
        for (int e = 0; e < 4; ++e) ad[h][e] = (wa ^ (e * 32)) + h * 65536;
        ad[h][4] = xa + h * 65536; ad[h][5] = (xa ^ 64) + h * 65536;
        asm volatile("" : "+v"(ad[h][0]), "+v"(ad[h][1]), "+v"(ad[h][2]), "+v"(ad[h][3]), "+v"(ad[h][4]), "+v"(ad[h][5]));
      }
      u32x4 xf[2][4], wf[2][4];
      f32x4 cf[4], bf[4]; f32x2 rc[4];
      auto rd = [&](int hs, int p) AI {
        const int s = hs >> 1, ks = hs & 1, hi = (s >> 1) & 1, e = p >> 1;
        const u32 so = (s & 1) * SLOT;
        if (hs > 15) {
          if (p < 4) bf[p] = lds_ld<f32x4>(L_CC + fh * 256 + ((p * 64) ^ (th * 128)) + g4 * 16);
          else rc[p - 4] = lds_ld<f32x2>(L_RC + (th * 64 + (p - 4) * 16 + n16) * 8);
        } else if (!(p & 1)) { const u32 a = ad[hi][e] + so + ks * 16384; wf[ks][e] = lds_tr2(a, a + 1024); }
        else xf[ks][e] = lds_ld<u32x4>(ad[hi][4 + ks] + so + e * 4096);
      };
      u32 fullv;
      u32x4 ones = {0x3f803f80u, 0x3f803f80u, 0x3f803f80u, 0x3f803f80u};
      asm volatile("" : "+v"(ones));
#pragma unroll
      for (int i = 0; i < 4; ++i) cf[i] = f32x4{0, 0, 0, 0};
      auto half = [&](int hs) AI {
        const int ks = hs & 1;
        sfor<0, 16>([&](auto J) AI {
          constexpr int j = J.v, o = j < 4 ? (j & 1) * 4 + (j >> 1) : j < 9 ? (j == 4 ? 8 : j == 5 ? 9 : j == 6 ? 2 : j == 7 ? 6 : 10) : (j < 12 ? 12 + (j - 9) : (j - 12) * 4 + 3);
          acc[o >> 2][o & 3] = mfma(wf[ks][o >> 2], xf[ks][o & 3], acc[o >> 2][o & 3]);
          SB();
          if (j < 12 && j % 3 != 1) rd(hs + 1, (j / 3) * 2 + (j % 3) / 2);
          if (ks == 0 && j == 12) cnt_add(C_FREE + 2 * hs);
          if (ks == 0 && j == 13 && hs < 14) fullv = cnt_ld(C_FULL + 2 * hs + 4);
          SB();
        });
        cf[0] = mfma(wf[ks][0], ones, cf[0]); cf[1] = mfma(wf[ks][1], ones, cf[1]);
        SB();
      };
      STAMP2(34);
      waitv<5>(); cnt_add(C_FULL);
      cnt_wait(C_FULL, 8);
      STAMP2(35);
#pragma unroll
      for (int p = 0; p < 8; ++p) rd(0, p);
      SB();
      sfor<0, 8>([&](auto S) AI {
        constexpr int s = S.v;
        half(2 * s);
        constexpr u32 need = s == 0 ? 8 : s < 3 ? 4 : 2;
        if (s == 0) { waitv<1>(); cnt_add(C_FULL + 4); }
        if (s < 7 && (u32)__builtin_amdgcn_readfirstlane(fullv) < need) cnt_wait(C_FULL + 4 * s + 4, need);
        CFENCE(); SB();
        STAMP2(16 + s);
        half(2 * s + 1);
      });
      STAMP(2);
      __builtin_amdgcn_s_setprio(2);
      if (n16 == 0) { lds_st<f32x4>(L_CSX + gw * 128 + g4 * 16, cf[0]); lds_st<f32x4>(L_CSX + gw * 128 + 64 + g4 * 16, cf[1]); }
      cnt_add(C_SA + fh * 4);
      const float c1 = -2.f * 0.7978845608028654f * 1.4426950408889634f, c2 = c1 * 0.044715f;
      const u32 hso = OFF_H + ((rp * 8 + th * 4) * 56 + cc * 4 + fh * 2) * 1024;
#pragma unroll
      for (int u = 0; u < 8; ++u) {
        const int tt = u & 3, kp = u >> 2;
        if (u == 3) { cnt_wait(C_SA + fh * 4, 2); cf[2] = lds_ld<f32x4>(L_CSX + (gw ^ 2) * 128 + g4 * 16); cf[3] = lds_ld<f32x4>(L_CSX + (gw ^ 2) * 128 + 64 + g4 * 16); SB(); }
        f32x2 x[4], p[4]; u32 hp[4];
        EACH4(x[j] = fma2(HALF(cf[kp * 2 + (j >> 1)], j), sp2(rc[tt].y), HALF(bf[kp * 2 + (j >> 1)], j)));
        EACH4(x[j] = fma2(HALF(acc[kp * 2 + (j >> 1)][tt], j), sp2(rc[tt].x), x[j]));
        EACH4(p[j] = x[j] * x[j]);
        EACH4(p[j] = fma2(p[j], sp2(c2), sp2(c1)));
        EACH4(p[j] = p[j] * x[j]);
        EACH4(p[j] = exp2x2(p[j]));
        EACH4(p[j] = p[j] + sp2(1.f));
        EACH4(p[j] = rcpx2(p[j]));
        EACH4(p[j] = p[j] * x[j]);
        EACH4(hp[j] = pk2(p[j].x, p[j].y));
        st128(u32x4{hp[0], hp[1], hp[2], hp[3]}, rs_ws, l * 16, hso + tt * 56 * 1024 + ((kp * 1024) ^ (th * 1024)));
        SB();
      }
      if (!NOPK) cnt_add(C_S2);
      STAMP(4);
    }
    if (!mainwg) __builtin_amdgcn_s_sleep(SPQ);
    poll_flags(ws, fw_off, tag);
    waitv<0>();
    if (mainwg) arrive_flag(ws, C_H, 4, OFF_FH + (rp * 16 + cc) * FS, tag, l);
    __builtin_amdgcn_s_setprio(0);
    STAMP(5);
#pragma unroll
    for (int j = 0; j < 28; ++j) load_w2(j);
    STAMP(6);
    if (!mainwg) {
      cnt_wait(C_S2, 4);
      if (b == 255) {
        poll_flags(ws, OFF_FU + (gw * 64 + l) * FS, tag);
        arrive_flag(ws, C_EP, 4, OFF_E, tag, l);
      }
      cnt_wait(C_S3, 4);
    }
    poll_flags(ws, fh_off, tag);
  }
  STAMP(7);
  u32x4 hf[28];
  auto load_h = [&](int n) AI { if (n < 28) { hf[n] = ld128(rs_ws, l * 16, OFF_H + ((rt * 4 + n / 7) * 56 + 7 * w + n % 7) * 1024); SB(); } };
  if (w >= 4) __builtin_amdgcn_s_setprio(G2P);
  const int tok = tid >> 3, cg = tid & 7;
  const u32x4 qv = *(const GAS u32x4*)(q + (rt * 64 + tok) * D_ + ct * 64 + cg * 8), b2v = *(const GAS u32x4*)(b2 + ct * 64 + cg * 8);
  const u32 stf = __builtin_amdgcn_raw_buffer_load_b32(rs_ws, tok * 16, OFF_ST + rt * 1024, 16);
  CFENCE(); SB();
#pragma unroll
  for (int n = 0; n < HLA; ++n) load_h(n);
  STAMP(8);
  f32x2 xn[4], xq[4], bq[4];
  {
    const f32x2 m = sp2(bflo(stf)), iv = sp2(__builtin_amdgcn_rcpf(bfhi(stf)));
    EACH4((xq[j] = bf2(qv[j]), bq[j] = bf2(b2v[j])));
    EACH4(xn[j] = rb2(rb2(xq[j] - m) * iv));
    asm volatile("" : "+v"(xn[0]), "+v"(xn[1]), "+v"(xn[2]), "+v"(xn[3]), "+v"(xq[0]), "+v"(xq[1]), "+v"(xq[2]), "+v"(xq[3]));
    asm volatile("" : "+v"(bq[0]), "+v"(bq[1]), "+v"(bq[2]), "+v"(bq[3]));
    SB();
  }
  const u32 fy_off = OFF_FY + (ct * 32 + (l & 31)) * FS;
  u64 fyv; u32 yv[4];
  f32x4 a2[2][4];
  const u32 wa2 = (w < 4 ? 65536 : 0) + gw * 4096 + n16 * 256;
  auto pwrite = [&](int t, int nt) AI { lds_st<f32x4>(t * 16384 + wa2 + (((4 * nt + g4) ^ (n16 & 7)) << 4), a2[t & 1][nt]); SB(); };
#pragma unroll
  for (int t = 0; t < 4; ++t) {
#pragma unroll
    for (int i = 0; i < 7; ++i) {
      if (t * 7 + i == YDR && w >= 4) { waitv<0>(); arrive_flag(ws, C_Y, 4, OFF_FY + b * FS, tag, l); SB(); }
      if (t == 1 && i == 0 && (w >= 4 || mainwg)) cnt_wait(w < 4 ? C_FREE + 28 : C_S1, 4);
#pragma unroll
      for (int nt = 0; nt < 4; ++nt) a2[t & 1][nt] = mfma(w2f[i * 4 + nt], hf[t * 7 + i], i ? a2[t & 1][nt] : f32x4{0, 0, 0, 0});
      SB();
      if (t > 0 && i < 4) pwrite(t - 1, i);
      if (t > 0 && i == 4) cnt_add(C_T + 4 * (t - 1));
      load_h(t * 7 + i + HLA);
      if (t * 7 + i == YFL) { fyv = flag_ld(ws, fy_off); SB(); }
    }
    STAMP(9 + t);
  }
#pragma unroll
  for (int nt = 0; nt < 4; ++nt) pwrite(3, nt);
  cnt_add(C_T + 12);
  u32 ra0 = (tok >> 4) * 16384 + (tok & 15) * 256 + (((2 * cg) ^ (tok & 7)) << 4), ra1 = ra0 ^ 16, ra2 = ra0 + 65536, ra3 = ra1 + 65536;
  asm volatile("" : "+v"(ra0), "+v"(ra1), "+v"(ra2), "+v"(ra3));
  auto load_y = [&](bool go) AI { EACH4(yv[j] = __builtin_amdgcn_raw_buffer_load_b32(rs_ws, (cg * 4 + j) * 256 + tok * 4, go ? OFF_Y + rt * 65536 + ct * 8192 : 0x7f000000u, 16)); };
  const bool yok = __all(fyv == tag);
  CFENCE();
  load_y(yok);
  STAMP2(36);
  cnt_wait(C_T + 4 * (w >> 1), 8);
  STAMP(13);
  {
    f32x4 pr[16];
#pragma unroll
    for (int i = 0; i < 4; ++i) { pr[i] = lds_ld<f32x4>(ra0 + i * 4096); pr[4 + i] = lds_ld<f32x4>(ra2 + i * 4096); }
#pragma unroll
    for (int i = 0; i < 4; ++i) { pr[8 + i] = lds_ld<f32x4>(ra1 + i * 4096); pr[12 + i] = lds_ld<f32x4>(ra3 + i * 4096); }
    SB();
    const f32x4 s0 = ((pr[0] + pr[4]) + (pr[1] + pr[5])) + ((pr[2] + pr[6]) + (pr[3] + pr[7]));
    const f32x4 s1 = ((pr[8] + pr[12]) + (pr[9] + pr[13])) + ((pr[10] + pr[14]) + (pr[11] + pr[15]));
    f32x2 z[4] = {s0.lo, s0.hi, s1.lo, s1.hi}, p[4];
    EACH4(z[j] += bq[j]);
    EACH4(p[j] = exp2x2(z[j] * sp2(-1.4426950408889634f)));
    EACH4(p[j] += sp2(1.f));
    EACH4(p[j] = rcpx2(p[j]));
    EACH4(z[j] *= p[j]);
    asm volatile("" : "+v"(z[0]), "+v"(z[1]), "+v"(z[2]), "+v"(z[3]));
    STAMP2(32);
    if (!yok) { poll_flags(ws, fy_off, tag); load_y(true); }
    STAMP2(33);
    EACH4(p[j] = fma2(rb2(xn[j] * bf2(yv[j])), z[j], xq[j]));
    *(GAS u32x4*)(out + (rt * 64 + tok) * D_ + ct * 64 + cg * 8) = u32x4{pk2(p[0].x, p[0].y), pk2(p[1].x, p[1].y), pk2(p[2].x, p[2].y), pk2(p[3].x, p[3].y)};
  }
  STAMP(14);
  STAMP_END;
}

extern "C" void kernel_launch(void* const* d_in, const int* in_sizes, int n_in, void* d_out, int out_size, void* d_ws, size_t ws_size,
                              hipStream_t stream) {
  char* ws = (char*)d_ws + (WS_TAIL ? ((ws_size - WS_BYTES) & ~(size_t)4095) : 0);
  fused_kernel<<<256, 512, 0, stream>>>((const u16*)d_in[0], (const u16*)d_in[1], (const u16*)d_in[2], (const u16*)d_in[3], (const u16*)d_in[4],
                                        (const u16*)d_in[5], (const u16*)d_in[7], (const u16*)d_in[8], (const u16*)d_in[9], (const u16*)d_in[10],
                                        (u16*)d_out, ws);
}
```

```cpp
#include <hip/hip_runtime.h>
typedef unsigned short u16;
typedef unsigned u32;
typedef unsigned long long u64;
typedef __bf16 bf16x8 __attribute__((ext_vector_type(8)));
typedef __bf16 bf16x2 __attribute__((ext_vector_type(2)));
typedef short s16x4 __attribute__((ext_vector_type(4)));
typedef float f32x4 __attribute__((ext_vector_type(4)));
typedef float f32x2 __attribute__((ext_vector_type(2)));
typedef unsigned u32x4 __attribute__((ext_vector_type(4)));
typedef unsigned u32x2 __attribute__((ext_vector_type(2)));
#define LAS __attribute__((address_space(3)))
#define GAS __attribute__((address_space(1)))
#define DI __device__ __forceinline__
#pragma clang diagnostic ignored "-Wint-to-pointer-cast"

constexpr int S_ = 2048, D_ = 512, F_ = 1792;
constexpr u32 OFF_E = 0, OFF_ST = 32768, FS = 256, OFF_FU = 65536, OFF_FW = OFF_FU + 256 * FS, OFF_FH = OFF_FW + 256 * FS, OFF_FY = OFF_FH + 256 * FS;
constexpr u32 OFF_U = 1u << 20, OFF_Y = 3u << 20, OFF_W2T = 5u << 20, OFF_H = 7u << 20, WS_BYTES = 14u << 20;
constexpr u32 OOR = 0x7f000000u;
constexpr u32 SLOT = 32768, L_PK = 131072, L_PV = 140288, L_PW = 148480, L_SK = 156928, L_B1 = 157440, L_CNT = 157696,
              L_RC = 157824  , L_CC = 158848  , L_CSX = 159872, L_DUM = 162816, L_EP = 163072,
              L_HC0 = 131072, L_HC1 = 0, HCS = 4160, L_UR = 147712, URS = 4576, L_CP = 32768;
constexpr u32 C_FULL = L_CNT  , C_TG = L_CNT + 32, C_SA = L_CNT + 36  , C_KV = L_CNT + 44, C_FREE = L_CNT + 48  ,
              C_H = L_CNT + 80, C_W = L_CNT + 84, C_U = L_CNT + 88, C_Y = L_CNT + 92, C_S1 = L_CNT + 96, C_S2 = L_CNT + 100, C_S3 = L_CNT + 104,
              C_EP = L_CNT + 108, C_T = L_CNT + 112  ;
constexpr u32 CODE_BYTES = 30720;
constexpr int HLA = 7;

template <int V> struct IC { static constexpr int v = V; };
template <int I, int N, class F> DI void sfor(F&& f) { if constexpr (I < N) { f(IC<I>{}); sfor<I + 1, N>(f); } }
DI u32x4 mk_srd(const void* p, u32 bytes) { u64 a = (u64)p; return u32x4{(u32)a, (u32)(a >> 32) & 0xffffu, bytes, 0x00020000u}; }
template <int I> DI void dma16g(u32x4 srd, u32 voff, u32 soff, u32 lds) {
  if (I == 0) asm volatile("s_mov_b32 m0, %3\n\ts_nop 0\n\tbuffer_load_dwordx4 %0, %1, %2 offen lds" ::"v"(voff), "s"(srd), "s"(soff), "s"(lds) : "memory");
  else asm volatile("buffer_load_dwordx4 %0, %1, %2 offen offset:%3 lds" ::"v"(voff), "s"(srd), "s"(soff), "i"(I * 1024) : "memory");
}
DI void dma16m(u32x4 srd, u32 voff, u32 soff, u32 lds, u64 mask) {
  asm volatile("s_mov_b32 m0, %3\n\ts_mov_b64 exec, %4\n\tbuffer_load_dwordx4 %0, %1, %2 offen lds\n\ts_mov_b64 exec, -1"
               ::"v"(voff), "s"(srd), "s"(soff), "s"(lds), "s"(mask) : "memory");
}
template <int N> DI void waitv() { asm volatile("s_waitcnt vmcnt(%0)" ::"i"(N) : "memory"); }
#define CFENCE() asm volatile("" ::: "memory")
#define SB() __builtin_amdgcn_sched_barrier(0)
DI void bar() { SB(); CFENCE(); __builtin_amdgcn_s_waitcnt(0xC07F); __builtin_amdgcn_s_barrier(); CFENCE(); SB(); }
DI float bflo(u32 x) { return __uint_as_float(x << 16); }
DI float bfhi(u32 x) { return __uint_as_float(x & 0xffff0000u); }
DI u32 pk2(float a, float b) { f32x2 p = {a, b}; return __builtin_bit_cast(u32, __builtin_convertvector(p, bf16x2)); }
DI float rb(float a) { return bflo(pk2(a, a)); }
DI f32x2 bf2(u32 x) { return f32x2{bflo(x), bfhi(x)}; }
DI f32x2 rb2(f32x2 v) { return bf2(pk2(v.x, v.y)); }
DI f32x2 sp2(float v) { return f32x2{v, v}; }
DI f32x2 exp2x2(f32x2 v) { return f32x2{__builtin_amdgcn_exp2f(v.x), __builtin_amdgcn_exp2f(v.y)}; }
DI f32x2 rcpx2(f32x2 v) { return f32x2{__builtin_amdgcn_rcpf(v.x), __builtin_amdgcn_rcpf(v.y)}; }
DI f32x2 fma2(f32x2 a, f32x2 b, f32x2 c) { return __builtin_elementwise_fma(a, b, c); }
DI float sfma(float a, float b, float c) { float d; asm("v_fma_f32 %0, %1, %2, %3" : "=v"(d) : "v"(a), "v"(b), "v"(c)); return d; }
DI float smul(float a, float b) { float d; asm("v_mul_f32 %0, %1, %2" : "=v"(d) : "v"(a), "v"(b)); return d; }
DI f32x2 fma2s(f32x2 a, f32x2 b, f32x2 c) { return f32x2{sfma(a.x, b.x, c.x), sfma(a.y, b.y, c.y)}; }
DI f32x2 mul2s(f32x2 a, f32x2 b) { return f32x2{smul(a.x, b.x), smul(a.y, b.y)}; }
template <class T> DI T lds_ld(u32 off) { return *(const LAS T*)(off); }
template <class T> DI void lds_st(u32 off, T v) { *(LAS T*)(off) = v; }
DI u32x2 lds_tr(u32 off) { return __builtin_bit_cast(u32x2, __builtin_amdgcn_ds_read_tr16_b64_v4i16((LAS s16x4*)(off))); }
DI u32x4 lds_tr2(u32 o0, u32 o1) { u32x2 a = lds_tr(o0), b = lds_tr(o1); return u32x4{a.x, a.y, b.x, b.y}; }
DI u32x4 lds_2x64(u32 o) { u32x2 a = *(const volatile LAS u32x2*)(o), b = *(const volatile LAS u32x2*)(o + 8); return u32x4{a.x, a.y, b.x, b.y}; }
DI f32x4 mfma(u32x4 a, u32x4 b, f32x4 c) {
  return __builtin_amdgcn_mfma_f32_16x16x32_bf16(__builtin_bit_cast(bf16x8, a), __builtin_bit_cast(bf16x8, b), c, 0, 0, 0);
}
DI void st128(u32x4 v, __amdgpu_buffer_rsrc_t rs, u32 voff, u32 soff) {
  __builtin_amdgcn_raw_buffer_store_b128(v, rs, voff, soff, 16);
  asm volatile("s_nop 1" ::"v"(v) : "memory");
}
DI u32x4 ld128(__amdgpu_buffer_rsrc_t rs, u32 voff, u32 soff) { return __builtin_amdgcn_raw_buffer_load_b128(rs, voff, soff, 16); }
template <int C> DI float dpp_add(float v) {
  return v + __builtin_bit_cast(float, __builtin_amdgcn_update_dpp(0, __builtin_bit_cast(int, v), C, 0xf, 0xf, true));
}
DI float sel(bool c, float a, float b) { return c ? a : b; }
constexpr int SPIN_CAP = 1 << 16;
DI u64 flag_ld(const char* ws, u32 off) { return __hip_atomic_load((const GAS u64*)(ws + off), __ATOMIC_RELAXED, __HIP_MEMORY_SCOPE_AGENT); }
DI void poll_flags(const char* ws, u32 off, u64 tag) {
#pragma unroll 1
  for (int it = 0; it < SPIN_CAP; ++it) {
    if (__all(flag_ld(ws, off) == tag)) break;
    __builtin_amdgcn_s_sleep(8);
  }
  CFENCE();
}
DI void flag_chk(u64 v, const char* ws, u32 off, u64 tag) { if (!__all(v == tag)) poll_flags(ws, off, tag); CFENCE(); }
DI void set_flag(char* ws, u32 off, u64 tag) { __hip_atomic_store((GAS u64*)(ws + off), tag, __ATOMIC_RELAXED, __HIP_MEMORY_SCOPE_AGENT); }
DI void arrive_flag(char* ws, u32 cnt, u32 n, u32 off, u64 tag, int l) {
  if (l == 0) {
    u32 old;
    asm volatile("ds_add_rtn_u32 %0, %1, %2\n\ts_waitcnt lgkmcnt(0)" : "=&v"(old) : "v"(cnt), "v"(1u) : "memory");
    if (old == n - 1) set_flag(ws, off, tag);
  }
  CFENCE();
}
DI void cnt_add(u32 cnt) { asm volatile("s_mov_b64 exec, 1\n\tds_add_u32 %0, %1\n\ts_mov_b64 exec, -1" ::"v"(cnt), "v"(1u) : "memory"); }
DI u32 cnt_ld(u32 cnt) { return *(const volatile LAS u32*)cnt; }
DI void cnt_wait(u32 cnt, u32 n) {
#pragma unroll 1
  for (int it = 0; it < (1 << 22); ++it) { if ((u32)__builtin_amdgcn_readfirstlane(cnt_ld(cnt)) >= n) break; __builtin_amdgcn_s_sleep(1); }
  CFENCE(); SB();
}
#define AI __attribute__((always_inline))
#define EACH4(stmt) do { _Pragma("unroll") for (int j = 0; j < 4; ++j) { stmt; } SB(); } while (0)
#define HALF(v, j) (((j) & 1) ? (v).hi : (v).lo)

__global__ __launch_bounds__(512, 2) void fused_kernel(const u16* __restrict__ q, const u16* __restrict__ kk, const u16* __restrict__ vv,
                                                       const u16* __restrict__ sk, const u16* __restrict__ ssk, const u16* __restrict__ sD,
                                                       const u16* __restrict__ w1, const u16* __restrict__ b1, const u16* __restrict__ w2,
                                                       const u16* __restrict__ b2, u16* __restrict__ out, char* __restrict__ ws) {
  __shared__ __attribute__((aligned(16))) char sm[163840];
  asm volatile("" ::"v"(sm) : "memory");
  const int tid = threadIdx.x, l = tid & 63;
  const int w = __builtin_amdgcn_readfirstlane(tid >> 6);
  const int b = blockIdx.x, xcd = b & 7, r = b >> 3;
  const bool mainwg = r < 28;
  const int rp = (xcd & 3) * 4 + (r & 3), cc = (xcd >> 2) * 7 + (r >> 2);
  const int rt = (xcd & 3) * 8 + (xcd >> 2) * 4 + (r & 3), ct = r >> 2;
  const int gw = w & 3;
  const int n16 = l & 15, g4 = l >> 4;
  {
    u64 pc;
    asm volatile("s_getpc_b64 %0" : "=s"(pc));
    asm volatile("s_mov_b32 m0, %3\n\ts_mov_b64 exec, 0xff\n\tbuffer_load_dword %0, %1, %2 offen lds\n\tds_write_b128 %4, %5\n\ts_mov_b64 exec, -1"
                 ::"v"(l * 128), "s"(mk_srd((const void*)pc, CODE_BYTES)), "s"((r & 3) * 8192 + w * 1024), "s"(L_DUM), "v"(L_CNT + l * 16), "v"(u32x4{0, 0, 0, 0}) : "memory");
  }
  u32 vx0 = (l >> 3) * 1024 + (((l & 7) ^ (l >> 3)) << 4), vx1 = vx0 + 8192 - 1024;
  const u32 vwb = (g4 + 1) * (F_ * 2) - 2048, vws = (n16 ^ (g4 << 1)) << 4;
  u32 vo0 = vwb + (vws ^ ((w & 1) << 7)), vo1 = vo0 + 4 * (F_ * 2) - 1024;
  u32 sxw = rp * 131072 + w * 16384, sww = w * 8 * (F_ * 2) + cc * 256;
  asm volatile("" ::"s"(sxw), "s"(sww), "v"(vx0), "v"(vx1), "v"(vo0), "v"(vo1));
  SB();
  asm volatile("" ::"s"(q), "s"(kk), "s"(vv), "s"(sk), "s"(ssk), "s"(sD), "s"(w1), "s"(b1), "s"(w2), "s"(b2), "s"(out), "s"(ws));
  asm volatile("s_mov_b32 m0, %2\n\ts_mov_b64 exec, 3\n\tbuffer_load_dword %0, %1, 0 offen sc1 lds\n\ts_mov_b64 exec, -1" ::"v"(l * 4), "s"(mk_srd(ws, 8)), "s"(L_EP) : "memory");
  const int srow = rt * 64 + ct * 8 + gw * 2;
  asm volatile("s_mov_b32 m0, %3\n\ts_mov_b64 exec, %4\n\tbuffer_load_dwordx4 %0, %1, %2 offen lds\n\tbuffer_load_dwordx4 %0, %1, %2 offen offset:1024 lds\n\ts_mov_b64 exec, -1"
               ::"v"(l * 16), "s"(mk_srd(q, S_ * D_ * 2)), "s"(srow * 1024), "s"(L_PW + gw * 2048), "s"(w < 4 ? ~0ull : 1ull) : "memory");
  if (mainwg) {
    const u32x4 sq_ = mk_srd(q, S_ * D_ * 2), sw_ = mk_srd((const char*)w1 - F_ * 2, D_ * F_ * 2 + F_ * 2);
#pragma unroll
    for (int s = 0; s < 2; ++s) {
      dma16g<0>(sq_, vx0, sxw + s * 128, s * SLOT + w * 4096); dma16g<1>(sq_, vx1, sxw + s * 128, 0);
      dma16g<2>(sw_, vo0, sww + s * 64 * (F_ * 2), 0); dma16g<3>(sw_, vo1, sww + s * 64 * (F_ * 2), 0);
      if (s == 0) bar();
    }
  } else bar();
  SB();
  const u32 vr = (l >> 3) * 1024 + (l & 7) * 16;
  const u32x4 srd_q = mk_srd(q, S_ * D_ * 2);
  const u32x4 srd_w1m = mk_srd((const char*)w1 - F_ * 2, D_ * F_ * 2 + F_ * 2);
  u64 tag;
  auto get_tag = [&]() AI {
    const u32x2 ev = *(const volatile LAS u32x2*)L_EP;
    tag = (((u64)__builtin_amdgcn_readfirstlane(ev.y) << 32) | (u32)__builtin_amdgcn_readfirstlane(ev.x)) + 0x9E3779B97F4A7C15ull;
  };
  const __amdgpu_buffer_rsrc_t rs_ws = __builtin_amdgcn_make_buffer_rsrc(ws, 0, WS_BYTES, 0x00020000);
  u32x4 w2f[28];
  auto load_w2 = [&](int j) AI { w2f[j] = ld128(rs_ws, l * 16 + (j & 3) * 1024, OFF_W2T + (ct * 56 + 7 * w + (j >> 2)) * 4096); SB(); };
  const u32 fw_off = OFF_FW + (4 * w + (ct >> 1)) * FS;
  const u32 fh_off = OFF_FH + ((rt >> 1) * 16 + min(((7 * w) >> 2) + (l & 3), (7 * w + 6) >> 2)) * FS;

  const u32 sx32 = ((2 * g4 + (n16 >> 3)) & 3) << 5, tr0 = (4 * g4 + (n16 >> 2)) * 128 + (l & 3) * 8, vsw = ((l & 7) ^ ((l >> 3) & 6)) << 4;
  const int sp = (r - 28) * 8 + xcd;
  if (w >= 4) {
    u32x4 ha[2]; u32 hn[2], dd;
    auto load_taps = [&]() AI {
      asm volatile("buffer_load_dwordx4 %0, %4, %5, %6 offen nt\n\tbuffer_load_dwordx4 %1, %4, %5, %6 offen offset:1024 nt\n\t"
                   "buffer_load_dword %2, %4, %5, %6 offen offset:16\n\tbuffer_load_dword %3, %4, %5, %6 offen offset:1040"
                   : "=&v"(ha[0]), "=&v"(ha[1]), "=&v"(hn[0]), "=&v"(hn[1]) : "v"(l * 16), "s"(mk_srd(ssk, S_ * D_ * 2)), "s"(b * 8192 + gw * 2048) : "memory");
      asm volatile("buffer_load_ushort %0, off, %1, %2" : "=v"(dd) : "s"(mk_srd(sD, D_ * 2)), "s"(b * 4 + (gw >> 1) * 2) : "memory");
    };
    __builtin_amdgcn_s_setprio(2);
    const u32 vw[4] = {vwb + vws, vwb + vws + 4 * (F_ * 2) - 1024, vwb + (vws ^ 128), vwb + (vws ^ 128) + 4 * (F_ * 2) - 1024};
    auto regions = [&](int s, int r0, auto N) AI {
      const u32 sx = rp * 131072 + r0 * 16384 + s * 128, sw = (r0 * 8 + s * 64) * (F_ * 2) + cc * 256, dst = (s & 3) * SLOT + r0 * 4096;
#pragma unroll
      for (int i = 0; i < N.v; ++i) {
        dma16g<0>(srd_q, vx0, sx + i * 16384, dst + i * 4096); dma16g<1>(srd_q, vx1, sx + i * 16384, 0);
        dma16g<2>(srd_w1m, vw[(i & 1) * 2], sw + i * 8 * (F_ * 2), 0); dma16g<3>(srd_w1m, vw[(i & 1) * 2 + 1], sw + i * 8 * (F_ * 2), 0);
      }
    };
    auto stage = [&](int s) AI { regions(s, gw * 4, IC<4>{}); };
    if (mainwg) {
      regions(2, gw * 2, IC<2>{}); waitv<12>(); cnt_add(C_FULL);
      regions(3, gw * 2, IC<2>{}); waitv<16>(); cnt_add(C_FULL + 4);
    }
    if (gw < 2) {
      if (mainwg) { waitv<8>(); cnt_add(C_FULL + 8); } else waitv<0>();
      cnt_add(C_TG);
      if (mainwg) {
        u32x4 g;
#pragma unroll 1
        for (int s = 4; s < 8; ++s) {
          cnt_wait(C_FREE + 4 * (s - 4), 4);
          if (s == 7) {
            asm volatile("buffer_load_dwordx4 %0, %1, %2, %3 offen sc1" : "=v"(g) : "v"(l * 16), "s"(mk_srd(ws, WS_BYTES)), "s"(OFF_ST + rp * 2048 + gw * 1024) : "memory");
            dma16m(mk_srd(b1, F_ * 2), l * 16, cc * 256, L_B1, 0xffffull);
          }
          stage(s);
          waitv<16>(); cnt_add(C_FULL + 4 * (s - 1));
        }
        get_tag();
        SB();
        asm volatile("" : "+v"(g));
        load_taps();
        auto g_ok = [&]() AI { return __all((g.y == (u32)tag) & (g.w == (u32)(tag >> 32))); };
        if (!g_ok()) {
#pragma unroll 1
          for (int it = 0; it < SPIN_CAP; ++it) { g = ld128(rs_ws, l * 16, OFF_ST + rp * 2048 + gw * 1024); if (g_ok()) break; __builtin_amdgcn_s_sleep(4); CFENCE(); }
        }
        const float i2 = __builtin_amdgcn_rcpf(bfhi(g.z));
        float ra = __builtin_amdgcn_rcpf(bfhi(g.x)) * i2;
        asm volatile("" : "+v"(ra));
        lds_st<f32x2>(L_RC + (gw * 64 + l) * 8, f32x2{ra, -bflo(g.x) * ra - bflo(g.z) * i2});
        lds_st<u32>(L_CC + (gw * 64 + l) * 4, (u32)lds_ld<u16>(L_B1 + (gw * 64 + l) * 2) << 16);
        waitv<5>();
        cnt_add(C_FULL + 28);
      } else { load_taps(); waitv<0>(); get_tag(); }
      cnt_add(C_KV);
    } else {
      waitv<8>(); cnt_add(C_FULL + 8);
      const int pw = gw & 1;
      const u32 so = (rt * 64 + pw * 32) * 1024 + ct * 128;
      const bool top = rt == 0 && pw == 0;
      sfor<0, 4>([&](auto I) AI { dma16g<I.v>(mk_srd(kk, S_ * D_ * 2), vr, so + I.v * 7168, L_PK + 1024 + pw * 4096); });
      dma16m(mk_srd(kk, top ? 0 : S_ * D_ * 2), vr, top ? 0 : so - 8192, L_PK + pw * 4096, ~0ull << 40);
      dma16m(mk_srd(sk, D_ * 8), l * 16, ct * 512, L_SK, 0xffffffffull);
      sfor<0, 4>([&](auto I) AI { dma16g<I.v>(mk_srd(vv, S_ * D_ * 2), vr, so + I.v * 7168, L_PV + pw * 4096); });
      waitv<10>(); cnt_add(C_FULL + 12);
      waitv<0>();
      get_tag();
      load_taps();
      const int cpl = l & 31, hh = l >> 5;
      const u32x4 sv = lds_ld<u32x4>(L_SK + cpl * 16);
      const f32x2 kT[4] = {f32x2{bflo(sv.x), bflo(sv.z)}, f32x2{bfhi(sv.x), bfhi(sv.z)}, f32x2{bflo(sv.y), bflo(sv.w)}, f32x2{bfhi(sv.y), bfhi(sv.w)}};
      u32 a = L_PK + (pw * 32 + hh * 16) * 128 + cpl * 4;
      asm volatile("" : "+v"(a));
      u32 kr[19], vq[16];
#pragma unroll
      for (int i = 0; i < 19; ++i) kr[i] = lds_ld<u32>(a + 5 * 128 + i * 128);
#pragma unroll
      for (int i = 0; i < 16; ++i) vq[i] = lds_ld<u32>(a + (L_PV - L_PK) + i * 128);
      SB();
      f32x2 kK[19];
#pragma unroll
      for (int i = 0; i < 3; ++i) kK[i] = bf2(kr[i]);
      u32x4 uo[4];
#pragma unroll
      for (int u = 0; u < 4; ++u) {
        f32x2 ka[4];
        EACH4(kK[4 * u + 3 + j] = bf2(kr[4 * u + 3 + j]));
        EACH4(ka[j] = mul2s(kK[4 * u + j], kT[3])); EACH4(ka[j] = fma2s(kK[4 * u + j + 1], kT[2], ka[j]));
        EACH4(ka[j] = fma2s(kK[4 * u + j + 2], kT[1], ka[j])); EACH4(ka[j] = fma2s(kK[4 * u + j + 3], kT[0], ka[j]));
        EACH4(ka[j] = rb2(ka[j])); EACH4(ka[j] = mul2s(ka[j], bf2(vq[4 * u + j])));
        uo[u] = u32x4{pk2(ka[3].x, ka[2].x), pk2(ka[1].x, ka[0].x), pk2(ka[3].y, ka[2].y), pk2(ka[1].y, ka[0].y)};
        SB();
      }
#pragma unroll
      for (int u = 0; u < 4; ++u) lds_st<u32x4>(L_PV + pw * 4096 + cpl * 128 + (((hh * 4 + u) ^ (cpl & 7)) << 4), uo[u]);
#pragma unroll
      for (int i = 0; i < 4; ++i) uo[i] = lds_ld<u32x4>(L_PV + pw * 4096 + i * 1024 + (l >> 3) * 128 + (((l & 7) ^ (l >> 3)) << 4));
#pragma unroll
      for (int i = 0; i < 4; ++i) st128(uo[i], rs_ws, (l >> 3) * 8192 + (l & 7) * 16, OFF_U + ((ct * 32 + 8 * i) * 512 + rt * 16 + pw * 8) * 16);
      cnt_add(C_KV);
      waitv<0>();
      cnt_wait(C_TG, 6);
      arrive_flag(ws, C_U, 2, OFF_FU + (ct * 32 + rt) * FS, tag, l);
    }
    if (!mainwg) cnt_wait(C_W, 4);
    poll_flags(ws, fw_off, tag);
#pragma unroll
    for (int j = 0; j < 12; ++j) load_w2(j);
    __builtin_amdgcn_s_setprio(3);
    cnt_wait(C_KV, 4);
    if (mainwg) cnt_wait(C_FREE + 16, 4);
    asm volatile("" : "+v"(ha[0]), "+v"(ha[1]), "+v"(hn[0]), "+v"(hn[1]), "+v"(dd));
    {
      const u32 hb = (gw >> 1) ? L_HC1 : L_HC0;
      const u32 h0 = pk2(0.f, bflo(ha[0].x) + bflo(dd));
      if ((tid & 127) == 0) ha[0].x = __builtin_amdgcn_perm(ha[0].x, h0, 0x07060302u);
      const u32 h0u = __builtin_amdgcn_readfirstlane(h0);
      if (gw & 1) { if (l < 60) lds_st<u32x2>(L_UR + (gw >> 1) * URS + 4096 + l * 8, u32x2{0, 0}); }
      else if (l < 31 && l != 23) lds_st<u32>(hb + (l >> 3) * HCS + (l & 7) * 4, (l == 15 || l == 30) ? h0u : 0u);
#pragma unroll
      for (int i = 0; i < 2; ++i) {
        const u32x4 x = ha[i];
        const u32x4 o = {__builtin_amdgcn_alignbit(x.y, x.x, 16), __builtin_amdgcn_alignbit(x.z, x.y, 16), __builtin_amdgcn_alignbit(x.w, x.z, 16), __builtin_amdgcn_alignbit(hn[i], x.w, 16)};
        const u32 d = hb + 32 + (gw & 1) * 2048 + i * 1024 + l * 16, d2 = d + 2 * HCS - 4, d3 = d + 3 * HCS - 4;
        lds_st<u32x4>(d, x); lds_st<u32x4>(d + HCS, o);
        asm volatile("ds_write2_b32 %0, %2, %3 offset1:1\n\tds_write2_b32 %0, %4, %5 offset0:2 offset1:3\n\tds_write2_b32 %1, %6, %7 offset1:1\n\tds_write2_b32 %1, %8, %9 offset0:2 offset1:3"
                     ::"v"(d2), "v"(d3), "v"(x.x), "v"(x.y), "v"(x.z), "v"(x.w), "v"(o.x), "v"(o.y), "v"(o.z), "v"(o.w) : "memory");
      }
    }
    if (!mainwg) __builtin_amdgcn_s_sleep(32);
    const u32 fu_off = OFF_FU + ((b >> 5) * 32 + 8 * gw + (l & 7)) * FS;
#pragma unroll
    for (int j = 12; j < 20; ++j) load_w2(j);
    poll_flags(ws, fu_off, tag);
    u32x4 uu[2];
#pragma unroll
    for (int i = 0; i < 2; ++i) uu[i] = ld128(rs_ws, l * 16 + i * 1024, OFF_U + b * 8192 + gw * 2048);
    SB();
#pragma unroll
    for (int j = 20; j < 28; ++j) load_w2(j);
#pragma unroll
    for (int i = 0; i < 2; ++i) {
      const u32 a = L_UR + (2044 - 4 * (gw * 128 + i * 64 + l)) * 2;
      lds_st<u32x2>(a, u32x2{uu[i].x, uu[i].y}); lds_st<u32x2>(a + URS, u32x2{uu[i].z, uu[i].w});
    }
    cnt_add(C_S2);
    cnt_wait(C_S2, mainwg ? 8 : 4);
    {
      const int ch = gw & 1, kh = gw >> 1;
      f32x4 ca[8];
#pragma unroll
      for (int i = 0; i < 8; ++i) ca[i] = f32x4{0, 0, 0, 0};
      const u32 ab = (ch ? L_HC1 : L_HC0) + ((n16 + 1) & 3) * HCS + (((n16 + 1) & ~3) + 8 * g4) * 2 + kh * 256;
      const u32 bb = L_UR + ch * URS + (2032 - 1792 - 16 * n16 + 8 * g4) * 2 + kh * 256;
      const u32 pa = L_CP + gw * 8192 + (4 * n16 + (g4 ^ ((n16 >> 1) & 3))) * 16;
      u32x4 AL[2][4], BL[2][4], AH[4], BH[4];
      auto rdp = [&](u32x4& a, u32x4& bq, int k0, int i) AI { SB(); a = lds_2x64(ab + k0 * 64 + i * 512); bq = lds_ld<u32x4>(bb + k0 * 64 + (7 - i) * 512); SB(); };
#pragma unroll
      for (int i = 0; i < 4; ++i) rdp(AL[0][i], BL[0][i], 0, i);
      sfor<0, 4>([&](auto K) AI {
        constexpr int k0 = K.v;
        u32x4 (&A)[4] = AL[k0 & 1]; u32x4 (&B)[4] = BL[k0 & 1];
#pragma unroll
        for (int n = 0; n < 16; ++n) {
          constexpr int oi[16] = {0, 0, 1, 0, 1, 2, 0, 1, 2, 3, 1, 2, 3, 2, 3, 3}, ot[16] = {0, 1, 0, 2, 1, 0, 3, 2, 1, 0, 3, 2, 1, 3, 2, 3};
          ca[oi[n] + ot[n]] = mfma(A[oi[n]], B[ot[n]], ca[oi[n] + ot[n]]);
          if (n < 8 && (n & 1)) rdp(AH[n >> 1], BH[n >> 1], k0, 4 + (n >> 1));
          if (k0 == 3 && n >= 12) { SB(); lds_st<f32x4>(pa + (n - 12) * 1024, ca[n - 12]); SB(); }
        }
        int cn = 0;
#pragma unroll
        for (int T = 0; T < 4; ++T)
#pragma unroll
          for (int i = 0; i <= T; ++i) {
            ca[4 + T] = mfma(A[i], BH[T - i], ca[4 + T]); ca[4 + T] = mfma(AH[T - i], B[i], ca[4 + T]);
            if (k0 < 3 && cn >= 2 && cn < 6) rdp(AL[(k0 + 1) & 1][cn - 2], BL[(k0 + 1) & 1][cn - 2], k0 + 1, cn - 2);
            if (k0 == 3 && T > 0 && i == 1) { SB(); lds_st<f32x4>(pa + (3 + T) * 1024, ca[3 + T]); SB(); }
            ++cn;
          }
        SB();
      });
      lds_st<f32x4>(pa + 7 * 1024, ca[7]);
    }
    cnt_add(C_S3);
    __builtin_amdgcn_s_setprio(0);
    const u64 fhv = flag_ld(ws, fh_off);
    cnt_wait(C_S3, 4);
    {
      const int t2 = tid & 255;
      f32x4 pr[2][4];
#pragma unroll
      for (int i = 0; i < 2; ++i)
#pragma unroll
        for (int p = 0; p < 4; ++p) pr[i][p] = lds_ld<f32x4>(L_CP + ((t2 & ~3) | ((t2 & 3) ^ ((t2 >> 3) & 3))) * 16 + i * 4096 + p * 8192);
      cnt_add(C_S1);
      SB();
#pragma unroll
      for (int i = 0; i < 2; ++i) {
        const f32x4 y0 = pr[i][0] + pr[i][2], y1 = pr[i][1] + pr[i][3];
        st128(u32x4{pk2(y0[0], y1[0]), pk2(y0[1], y1[1]), pk2(y0[2], y1[2]), pk2(y0[3], y1[3])}, rs_ws, (t2 >> 4) * 65536 + (t2 & 15) * 16, OFF_Y + b * 256 + i * 16 * 65536);
      }
    }
    flag_chk(fhv, ws, fh_off, tag);
  } else {
    {
      __builtin_amdgcn_s_setprio(3);
      if (!mainwg) {
#pragma unroll
        for (int p = 0; p < 4; ++p) {
          const int hp = gw * 4 + p;
          sfor<0, 4>([&](auto I) AI { dma16g<I.v>(mk_srd(w2, hp < 14 ? D_ * F_ * 2 : 0), (l >> 3) * 1024 + vsw,
                                                       ((sp >> 2) * 7 + (hp >> 1)) * 32768 + (sp & 3) * 256 + (hp & 1) * 128 + I.v * 7168, hp * 4096); });
        }
      }
      if (mainwg) waitv<8>(); else waitv<16>();
      get_tag(); cnt_add(C_TG);
    {
      const u32x4 x0[2] = {lds_ld<u32x4>(L_PW + gw * 2048 + l * 16), lds_ld<u32x4>(L_PW + gw * 2048 + 1024 + l * 16)};
      float c0[2], sa[2], sq[2];
#pragma unroll
      for (int i = 0; i < 2; ++i) {
        c0[i] = bflo(__builtin_amdgcn_readfirstlane(x0[i].x));
        const f32x2 d[4] = {bf2(x0[i].x) - sp2(c0[i]), bf2(x0[i].y) - sp2(c0[i]), bf2(x0[i].z) - sp2(c0[i]), bf2(x0[i].w) - sp2(c0[i])};
        const f32x2 s2 = (d[0] + d[1]) + (d[2] + d[3]), q2 = fma2(d[0], d[0], d[1] * d[1]) + fma2(d[2], d[2], d[3] * d[3]);
        sa[i] = s2.x + s2.y; sq[i] = q2.x + q2.y;
      }
#define RED4(C) do { sa[0] = dpp_add<C>(sa[0]); sq[0] = dpp_add<C>(sq[0]); sa[1] = dpp_add<C>(sa[1]); sq[1] = dpp_add<C>(sq[1]); SB(); } while (0)
      SB(); RED4(0xB1); RED4(0x4E); RED4(0x141); RED4(0x140); RED4(0x142); RED4(0x143);
      const bool odd = l & 1;
      const float md = sel(odd, sa[1], sa[0]) * (1.f / D_), mean = sel(odd, c0[1], c0[0]) + md, var = fmaxf(sel(odd, sq[1], sq[0]) * (1.f / D_) - md * md, 0.f);
      const float m1 = rb(mean), sd1 = rb(__builtin_amdgcn_sqrtf(rb(rb(var) + 1e-5f)));
      const float i1 = __builtin_amdgcn_rcpf(sd1);
      const float m2 = (mean - m1) * i1, sd2 = __builtin_amdgcn_sqrtf(rb(rb(var * i1 * i1) + 1e-5f));
      if (l >= 62) st128(u32x4{pk2(m1, sd1), (u32)tag, pk2(m2, sd2), (u32)(tag >> 32)}, rs_ws, (l - 62) * 16, OFF_ST + srow * 16);
    }


      __builtin_amdgcn_s_setprio(1);
      if (!mainwg) {
        waitv<1>();
        SB();
#pragma unroll
        for (int p = 0; p < 4; ++p) {
          const int hp = gw * 4 + p;
          u32x4 wt[4];
#pragma unroll
          for (int nt = 0; nt < 4; ++nt) { const u32 a = hp * 4096 + tr0 + ((nt * 32) ^ sx32); wt[nt] = lds_tr2(a, a + 2048); }
#pragma unroll
          for (int nt = 0; nt < 4; ++nt)
            st128(wt[nt], rs_ws, l * 16, hp < 14 ? OFF_W2T + ((((sp & 3) * 2 + (hp & 1)) * 56 + (sp >> 2) * 7 + (hp >> 1)) * 4 + nt) * 1024 : OOR);
        }
        waitv<0>();
        arrive_flag(ws, C_W, 4, OFF_FW + sp * FS, tag, l);
      }
    }
    if (mainwg) {
      const int th = gw >> 1, fh = gw & 1;
      f32x4 acc[4][4];
#pragma unroll
      for (int i = 0; i < 16; ++i) acc[i >> 2][i & 3] = f32x4{0, 0, 0, 0};
      const u32 xa = th * 16384 + n16 * 128 + ((g4 ^ (n16 & 7)) << 4);
      const u32 wa = g4 * 4096 + 2048 + (n16 >> 2) * 256 + (((4 * fh + 2 * th) ^ ((n16 >> 2) | ((g4 & 1) << 2))) << 5) + (l & 3) * 8;
      u32 ad[2][6];
#pragma unroll
      for (int h = 0; h < 2; ++h) {
#pragma unroll
        for (int e = 0; e < 4; ++e) ad[h][e] = (wa ^ (e * 32)) + h * 65536;
        ad[h][4] = xa + h * 65536; ad[h][5] = (xa ^ 64) + h * 65536;
        asm volatile("" : "+v"(ad[h][0]), "+v"(ad[h][1]), "+v"(ad[h][2]), "+v"(ad[h][3]), "+v"(ad[h][4]), "+v"(ad[h][5]));
      }
      u32x4 xf[2][4], wf[2][4];
      f32x4 cf[4], bf[4]; f32x2 rc[4];
      auto rd = [&](int hs, int p) AI {
        const int s = hs >> 1, ks = hs & 1, hi = (s >> 1) & 1, e = p >> 1;
        const u32 so = (s & 1) * SLOT;
        if (hs > 15) {
          if (p < 4) bf[p] = lds_ld<f32x4>(L_CC + fh * 256 + ((p * 64) ^ (th * 128)) + g4 * 16);
          else rc[p - 4] = lds_ld<f32x2>(L_RC + (th * 64 + (p - 4) * 16 + n16) * 8);
        } else if (!(p & 1)) { const u32 a = ad[hi][e] + so + ks * 16384; wf[ks][e] = lds_tr2(a, a + 1024); }
        else xf[ks][e] = lds_ld<u32x4>(ad[hi][4 + ks] + so + e * 4096);
      };
      u32 fullv;
      u32x4 ones = {0x3f803f80u, 0x3f803f80u, 0x3f803f80u, 0x3f803f80u};
      asm volatile("" : "+v"(ones));
#pragma unroll
      for (int i = 0; i < 4; ++i) cf[i] = f32x4{0, 0, 0, 0};
      auto half = [&](int hs) AI {
        const int ks = hs & 1;
        sfor<0, 16>([&](auto J) AI {
          constexpr int j = J.v, o = j < 4 ? (j & 1) * 4 + (j >> 1) : j < 9 ? (j == 4 ? 8 : j == 5 ? 9 : j == 6 ? 2 : j == 7 ? 6 : 10) : (j < 12 ? 12 + (j - 9) : (j - 12) * 4 + 3);
          acc[o >> 2][o & 3] = mfma(wf[ks][o >> 2], xf[ks][o & 3], acc[o >> 2][o & 3]);
          SB();
          if (j < 12 && j % 3 != 1) rd(hs + 1, (j / 3) * 2 + (j % 3) / 2);
          if (ks == 0 && j == 12) cnt_add(C_FREE + 2 * hs);
          if (ks == 0 && j == 13 && hs < 14) fullv = cnt_ld(C_FULL + 2 * hs + 4);
          SB();
        });
        cf[0] = mfma(wf[ks][0], ones, cf[0]); cf[1] = mfma(wf[ks][1], ones, cf[1]);
        SB();
      };
      waitv<5>(); cnt_add(C_FULL);
      cnt_wait(C_FULL, 8);
#pragma unroll
      for (int p = 0; p < 8; ++p) rd(0, p);
      SB();
      sfor<0, 8>([&](auto S) AI {
        constexpr int s = S.v;
        half(2 * s);
        constexpr u32 need = s == 0 ? 8 : s < 3 ? 4 : 2;
        if (s == 0) { waitv<1>(); cnt_add(C_FULL + 4); }
        if (s < 7 && (u32)__builtin_amdgcn_readfirstlane(fullv) < need) cnt_wait(C_FULL + 4 * s + 4, need);
        CFENCE(); SB();
        half(2 * s + 1);
      });
      __builtin_amdgcn_s_setprio(2);
      if (n16 == 0) { lds_st<f32x4>(L_CSX + gw * 128 + g4 * 16, cf[0]); lds_st<f32x4>(L_CSX + gw * 128 + 64 + g4 * 16, cf[1]); }
      cnt_add(C_SA + fh * 4);
      const float c1 = -2.f * 0.7978845608028654f * 1.4426950408889634f, c2 = c1 * 0.044715f;
      const u32 hso = OFF_H + ((rp * 8 + th * 4) * 56 + cc * 4 + fh * 2) * 1024;
#pragma unroll
      for (int u = 0; u < 8; ++u) {
        const int tt = u & 3, kp = u >> 2;
        if (u == 3) { cnt_wait(C_SA + fh * 4, 2); cf[2] = lds_ld<f32x4>(L_CSX + (gw ^ 2) * 128 + g4 * 16); cf[3] = lds_ld<f32x4>(L_CSX + (gw ^ 2) * 128 + 64 + g4 * 16); SB(); }
        f32x2 x[4], p[4]; u32 hp[4];
        EACH4(x[j] = fma2(HALF(cf[kp * 2 + (j >> 1)], j), sp2(rc[tt].y), HALF(bf[kp * 2 + (j >> 1)], j)));
        EACH4(x[j] = fma2(HALF(acc[kp * 2 + (j >> 1)][tt], j), sp2(rc[tt].x), x[j]));
        EACH4(p[j] = x[j] * x[j]);
        EACH4(p[j] = fma2(p[j], sp2(c2), sp2(c1)));
        EACH4(p[j] = p[j] * x[j]);
        EACH4(p[j] = exp2x2(p[j]));
        EACH4(p[j] = p[j] + sp2(1.f));
        EACH4(p[j] = rcpx2(p[j]));
        EACH4(p[j] = p[j] * x[j]);
        EACH4(hp[j] = pk2(p[j].x, p[j].y));
        st128(u32x4{hp[0], hp[1], hp[2], hp[3]}, rs_ws, l * 16, hso + tt * 56 * 1024 + ((kp * 1024) ^ (th * 1024)));
        SB();
      }
      cnt_add(C_S2);
    }
    if (!mainwg) __builtin_amdgcn_s_sleep(32);
    poll_flags(ws, fw_off, tag);
    waitv<0>();
    if (mainwg) arrive_flag(ws, C_H, 4, OFF_FH + (rp * 16 + cc) * FS, tag, l);
    __builtin_amdgcn_s_setprio(0);
#pragma unroll
    for (int j = 0; j < 20; ++j) load_w2(j);
    const u64 fhv = flag_ld(ws, mainwg ? fh_off : OFF_E + 64);
#pragma unroll
    for (int j = 20; j < 28; ++j) load_w2(j);
    if (!mainwg) {
      cnt_wait(C_S2, 4);
      if (b == 255) {
        poll_flags(ws, OFF_FU + (gw * 64 + l) * FS, tag);
        arrive_flag(ws, C_EP, 4, OFF_E, tag, l);
      }
      cnt_wait(C_S3, 4);
    }
    if (mainwg) flag_chk(fhv, ws, fh_off, tag); else poll_flags(ws, fh_off, tag);
  }
  u32x4 hf[28];
  auto load_h = [&](int n) AI { if (n < 28) { hf[n] = ld128(rs_ws, l * 16, OFF_H + ((rt * 4 + n / 7) * 56 + 7 * w + n % 7) * 1024); SB(); } };
  if (w >= 4) __builtin_amdgcn_s_setprio(1);
  const int tok = tid >> 3, cg = tid & 7;
  const u32x4 qv = *(const GAS u32x4*)(q + (rt * 64 + tok) * D_ + ct * 64 + cg * 8), b2v = *(const GAS u32x4*)(b2 + ct * 64 + cg * 8);
  const u32 stf = __builtin_amdgcn_raw_buffer_load_b32(rs_ws, tok * 16, OFF_ST + rt * 1024, 16);
  CFENCE(); SB();
#pragma unroll
  for (int n = 0; n < HLA; ++n) load_h(n);
  f32x2 xn[4], xq[4], bq[4];
  {
    const f32x2 m = sp2(bflo(stf)), iv = sp2(__builtin_amdgcn_rcpf(bfhi(stf)));
    EACH4((xq[j] = bf2(qv[j]), bq[j] = bf2(b2v[j])));
    EACH4(xn[j] = rb2(rb2(xq[j] - m) * iv));
    asm volatile("" : "+v"(xn[0]), "+v"(xn[1]), "+v"(xn[2]), "+v"(xn[3]), "+v"(xq[0]), "+v"(xq[1]), "+v"(xq[2]), "+v"(xq[3]));
    asm volatile("" : "+v"(bq[0]), "+v"(bq[1]), "+v"(bq[2]), "+v"(bq[3]));
    SB();
  }
  const u32 fy_off = OFF_FY + (ct * 32 + (l & 31)) * FS;
  u64 fyv; u32 yv[4];
  f32x4 a2[2][4];
  const u32 wa2 = (w < 4 ? 65536 : 0) + gw * 4096 + n16 * 256;
  auto pwrite = [&](int t, int nt) AI { lds_st<f32x4>(t * 16384 + wa2 + (((4 * nt + g4) ^ (n16 & 7)) << 4), a2[t & 1][nt]); SB(); };
#pragma unroll
  for (int t = 0; t < 4; ++t) {
#pragma unroll
    for (int i = 0; i < 7; ++i) {
      if (t == 0 && i == 0 && w >= 4) { waitv<0>(); arrive_flag(ws, C_Y, 4, OFF_FY + b * FS, tag, l); SB(); }
      if (t == 1 && i == 0 && (w >= 4 || mainwg)) cnt_wait(w < 4 ? C_FREE + 28 : C_S1, 4);
#pragma unroll
      for (int nt = 0; nt < 4; ++nt) a2[t & 1][nt] = mfma(w2f[i * 4 + nt], hf[t * 7 + i], i ? a2[t & 1][nt] : f32x4{0, 0, 0, 0});
      SB();
      if (t > 0 && i < 4) pwrite(t - 1, i);
      if (t > 0 && i == 4) cnt_add(C_T + 4 * (t - 1));
      load_h(t * 7 + i + HLA);
      if (t * 7 + i == 24) { fyv = flag_ld(ws, fy_off); SB(); }
    }
  }
#pragma unroll
  for (int nt = 0; nt < 4; ++nt) pwrite(3, nt);
  cnt_add(C_T + 12);
  u32 ra0 = (tok >> 4) * 16384 + (tok & 15) * 256 + (((2 * cg) ^ (tok & 7)) << 4), ra1 = ra0 ^ 16, ra2 = ra0 + 65536, ra3 = ra1 + 65536;
  asm volatile("" : "+v"(ra0), "+v"(ra1), "+v"(ra2), "+v"(ra3));
  auto load_y = [&](bool go) AI { EACH4(yv[j] = __builtin_amdgcn_raw_buffer_load_b32(rs_ws, (cg * 4 + j) * 256 + tok * 4, go ? OFF_Y + rt * 65536 + ct * 8192 : OOR, 16)); };
  const bool yok = __all(fyv == tag);
  CFENCE();
  load_y(yok);
  cnt_wait(C_T + 4 * (w >> 1), 8);
  {
    f32x4 pr[16];
#pragma unroll
    for (int i = 0; i < 4; ++i) { pr[i] = lds_ld<f32x4>(ra0 + i * 4096); pr[4 + i] = lds_ld<f32x4>(ra2 + i * 4096); }
#pragma unroll
    for (int i = 0; i < 4; ++i) { pr[8 + i] = lds_ld<f32x4>(ra1 + i * 4096); pr[12 + i] = lds_ld<f32x4>(ra3 + i * 4096); }
    SB();
    const f32x4 s0 = ((pr[0] + pr[4]) + (pr[1] + pr[5])) + ((pr[2] + pr[6]) + (pr[3] + pr[7]));
    const f32x4 s1 = ((pr[8] + pr[12]) + (pr[9] + pr[13])) + ((pr[10] + pr[14]) + (pr[11] + pr[15]));
    f32x2 z[4] = {s0.lo, s0.hi, s1.lo, s1.hi}, p[4];
    EACH4(z[j] += bq[j]);
    EACH4(p[j] = exp2x2(z[j] * sp2(-1.4426950408889634f)));
    EACH4(p[j] += sp2(1.f));
    EACH4(p[j] = rcpx2(p[j]));
    EACH4(z[j] *= p[j]);
    asm volatile("" : "+v"(z[0]), "+v"(z[1]), "+v"(z[2]), "+v"(z[3]));
    if (!yok) { poll_flags(ws, fy_off, tag); load_y(true); }
    EACH4(p[j] = fma2(rb2(xn[j] * bf2(yv[j])), z[j], xq[j]));
    *(GAS u32x4*)(out + (rt * 64 + tok) * D_ + ct * 64 + cg * 8) = u32x4{pk2(p[0].x, p[0].y), pk2(p[1].x, p[1].y), pk2(p[2].x, p[2].y), pk2(p[3].x, p[3].y)};
  }
}

extern "C" void kernel_launch(void* const* d_in, const int* in_sizes, int n_in, void* d_out, int out_size, void* d_ws, size_t ws_size,
                              hipStream_t stream) {
  fused_kernel<<<256, 512, 0, stream>>>((const u16*)d_in[0], (const u16*)d_in[1], (const u16*)d_in[2], (const u16*)d_in[3], (const u16*)d_in[4],
                                        (const u16*)d_in[5], (const u16*)d_in[7], (const u16*)d_in[8], (const u16*)d_in[9], (const u16*)d_in[10],
                                        (u16*)d_out, (char*)d_ws);
}
```

```cpp
#include <hip/hip_runtime.h>
typedef unsigned short u16;
typedef unsigned u32;
typedef unsigned long long u64;
typedef __bf16 bf16x8 __attribute__((ext_vector_type(8)));
typedef __bf16 bf16x2 __attribute__((ext_vector_type(2)));
typedef short s16x4 __attribute__((ext_vector_type(4)));
typedef float f32x4 __attribute__((ext_vector_type(4)));
typedef float f32x2 __attribute__((ext_vector_type(2)));
typedef unsigned u32x4 __attribute__((ext_vector_type(4)));
typedef unsigned u32x2 __attribute__((ext_vector_type(2)));
#define LAS __attribute__((address_space(3)))
#define GAS __attribute__((address_space(1)))
#define DI __device__ __forceinline__
#pragma clang diagnostic ignored "-Wint-to-pointer-cast"

constexpr int S_ = 2048, D_ = 512, F_ = 1792;
constexpr u32 OFF_E = 0, OFF_ST = 32768, FS = 256, OFF_FU = 65536, OFF_FW = OFF_FU + 256 * FS, OFF_FH = OFF_FW + 256 * FS, OFF_FY = OFF_FH + 256 * FS;
constexpr u32 OFF_U = 1u << 20, OFF_Y = 3u << 20, OFF_W2T = 5u << 20, OFF_H = 7u << 20, WS_BYTES = 14u << 20;
constexpr u32 OOR = 0x7f000000u;
constexpr u32 SLOT = 32768, L_PK = 131072, L_PV = 140288, L_PW = 148480, L_SK = 156928, L_B1 = 157440, L_CNT = 157696,
              L_RC = 157824  , L_CC = 158848  , L_CSX = 159872, L_DUM = 162816, L_EP = 163072,
              L_HC0 = 131072, L_HC1 = 0, HCS = 4160, L_UR = 147712, URS = 4576, L_CP = 32768;
constexpr u32 C_FULL = L_CNT  , C_TG = L_CNT + 32, C_SA = L_CNT + 36  , C_KV = L_CNT + 44, C_FREE = L_CNT + 48  ,
              C_H = L_CNT + 80, C_W = L_CNT + 84, C_U = L_CNT + 88, C_Y = L_CNT + 92, C_S1 = L_CNT + 96, C_S2 = L_CNT + 100, C_S3 = L_CNT + 104,
              C_EP = L_CNT + 108, C_T = L_CNT + 112  ;
constexpr u32 CODE_BYTES = 31872;
constexpr int HLA = 7;

template <int V> struct IC { static constexpr int v = V; };
template <int I, int N, class F> DI void sfor(F&& f) { if constexpr (I < N) { f(IC<I>{}); sfor<I + 1, N>(f); } }
DI u32x4 mk_srd(const void* p, u32 bytes) { u64 a = (u64)p; return u32x4{(u32)a, (u32)(a >> 32) & 0xffffu, bytes, 0x00020000u}; }
template <int I> DI void dma16g(u32x4 srd, u32 voff, u32 soff, u32 lds) {
  if (I == 0) asm volatile("s_mov_b32 m0, %3\n\ts_nop 0\n\tbuffer_load_dwordx4 %0, %1, %2 offen lds" ::"v"(voff), "s"(srd), "s"(soff), "s"(lds) : "memory");
  else asm volatile("buffer_load_dwordx4 %0, %1, %2 offen offset:%3 lds" ::"v"(voff), "s"(srd), "s"(soff), "i"(I * 1024) : "memory");
}
DI void dma16m(u32x4 srd, u32 voff, u32 soff, u32 lds, u64 mask) {
  asm volatile("s_mov_b32 m0, %3\n\ts_mov_b64 exec, %4\n\tbuffer_load_dwordx4 %0, %1, %2 offen lds\n\ts_mov_b64 exec, -1"
               ::"v"(voff), "s"(srd), "s"(soff), "s"(lds), "s"(mask) : "memory");
}
template <int N> DI void waitv() { asm volatile("s_waitcnt vmcnt(%0)" ::"i"(N) : "memory"); }
#define CFENCE() asm volatile("" ::: "memory")
#define SB() __builtin_amdgcn_sched_barrier(0)
DI void bar() { SB(); CFENCE(); __builtin_amdgcn_s_waitcnt(0xC07F); __builtin_amdgcn_s_barrier(); CFENCE(); SB(); }
DI float bflo(u32 x) { return __uint_as_float(x << 16); }
DI float bfhi(u32 x) { return __uint_as_float(x & 0xffff0000u); }
DI u32 pk2(float a, float b) { f32x2 p = {a, b}; return __builtin_bit_cast(u32, __builtin_convertvector(p, bf16x2)); }
DI float rb(float a) { return bflo(pk2(a, a)); }
DI f32x2 bf2(u32 x) { return f32x2{bflo(x), bfhi(x)}; }
DI f32x2 rb2(f32x2 v) { return bf2(pk2(v.x, v.y)); }
DI f32x2 sp2(float v) { return f32x2{v, v}; }
DI f32x2 exp2x2(f32x2 v) { return f32x2{__builtin_amdgcn_exp2f(v.x), __builtin_amdgcn_exp2f(v.y)}; }
DI f32x2 rcpx2(f32x2 v) { return f32x2{__builtin_amdgcn_rcpf(v.x), __builtin_amdgcn_rcpf(v.y)}; }
DI f32x2 fma2(f32x2 a, f32x2 b, f32x2 c) { return __builtin_elementwise_fma(a, b, c); }
DI float sfma(float a, float b, float c) { float d; asm("v_fma_f32 %0, %1, %2, %3" : "=v"(d) : "v"(a), "v"(b), "v"(c)); return d; }
DI float smul(float a, float b) { float d; asm("v_mul_f32 %0, %1, %2" : "=v"(d) : "v"(a), "v"(b)); return d; }
DI f32x2 fma2s(f32x2 a, f32x2 b, f32x2 c) { return f32x2{sfma(a.x, b.x, c.x), sfma(a.y, b.y, c.y)}; }
DI f32x2 mul2s(f32x2 a, f32x2 b) { return f32x2{smul(a.x, b.x), smul(a.y, b.y)}; }
template <class T> DI T lds_ld(u32 off) { return *(const LAS T*)(off); }
template <class T> DI void lds_st(u32 off, T v) { *(LAS T*)(off) = v; }
DI u32x2 lds_tr(u32 off) { return __builtin_bit_cast(u32x2, __builtin_amdgcn_ds_read_tr16_b64_v4i16((LAS s16x4*)(off))); }
DI u32x4 lds_tr2(u32 o0, u32 o1) { u32x2 a = lds_tr(o0), b = lds_tr(o1); return u32x4{a.x, a.y, b.x, b.y}; }
DI u32x4 lds_2x64(u32 o) { u32x2 a = *(const volatile LAS u32x2*)(o), b = *(const volatile LAS u32x2*)(o + 8); return u32x4{a.x, a.y, b.x, b.y}; }
DI f32x4 mfma(u32x4 a, u32x4 b, f32x4 c) {
  return __builtin_amdgcn_mfma_f32_16x16x32_bf16(__builtin_bit_cast(bf16x8, a), __builtin_bit_cast(bf16x8, b), c, 0, 0, 0);
}
DI void st128(u32x4 v, __amdgpu_buffer_rsrc_t rs, u32 voff, u32 soff) {
  __builtin_amdgcn_raw_buffer_store_b128(v, rs, voff, soff, 16);
  asm volatile("s_nop 1" ::"v"(v) : "memory");
}
DI u32x4 ld128(__amdgpu_buffer_rsrc_t rs, u32 voff, u32 soff) { return __builtin_amdgcn_raw_buffer_load_b128(rs, voff, soff, 16); }
template <int C> DI float dpp_add(float v) {
  return v + __builtin_bit_cast(float, __builtin_amdgcn_update_dpp(0, __builtin_bit_cast(int, v), C, 0xf, 0xf, true));
}
DI float sel(bool c, float a, float b) { return c ? a : b; }
constexpr int SPIN_CAP = 1 << 16;
DI u64 flag_ld(const char* ws, u32 off) { return __hip_atomic_load((const GAS u64*)(ws + off), __ATOMIC_RELAXED, __HIP_MEMORY_SCOPE_AGENT); }
DI void poll_flags(const char* ws, u32 off, u64 tag) {
#pragma unroll 1
  for (int it = 0; it < SPIN_CAP; ++it) {
    if (__all(flag_ld(ws, off) == tag)) break;
    __builtin_amdgcn_s_sleep(8);
  }
  CFENCE();
}
DI void flag_chk(u64 v, const char* ws, u32 off, u64 tag) { if (!__all(v == tag)) poll_flags(ws, off, tag); CFENCE(); }
DI void set_flag(char* ws, u32 off, u64 tag) { __hip_atomic_store((GAS u64*)(ws + off), tag, __ATOMIC_RELAXED, __HIP_MEMORY_SCOPE_AGENT); }
DI void arrive_flag(char* ws, u32 cnt, u32 n, u32 off, u64 tag, int l) {
  if (l == 0) {
    u32 old;
    asm volatile("ds_add_rtn_u32 %0, %1, %2\n\ts_waitcnt lgkmcnt(0)" : "=&v"(old) : "v"(cnt), "v"(1u) : "memory");
    if (old == n - 1) set_flag(ws, off, tag);
  }
  CFENCE();
}
DI void cnt_add(u32 cnt) { asm volatile("s_mov_b64 exec, 1\n\tds_add_u32 %0, %1\n\ts_mov_b64 exec, -1" ::"v"(cnt), "v"(1u) : "memory"); }
DI u32 cnt_ld(u32 cnt) { return *(const volatile LAS u32*)cnt; }
DI void cnt_wait(u32 cnt, u32 n) {
#pragma unroll 1
  for (int it = 0; it < (1 << 22); ++it) { if ((u32)__builtin_amdgcn_readfirstlane(cnt_ld(cnt)) >= n) break; __builtin_amdgcn_s_sleep(1); }
  CFENCE(); SB();
}
#define AI __attribute__((always_inline))
#define EACH4(stmt) do { _Pragma("unroll") for (int j = 0; j < 4; ++j) { stmt; } SB(); } while (0)
#define HALF(v, j) (((j) & 1) ? (v).hi : (v).lo)

__global__ __launch_bounds__(512, 2) void fused_kernel(const u16* __restrict__ q, const u16* __restrict__ kk, const u16* __restrict__ vv,
                                                       const u16* __restrict__ sk, const u16* __restrict__ ssk, const u16* __restrict__ sD,
                                                       const u16* __restrict__ w1, const u16* __restrict__ b1, const u16* __restrict__ w2,
                                                       const u16* __restrict__ b2, u16* __restrict__ out, char* __restrict__ ws) {
  __shared__ __attribute__((aligned(16))) char sm[163840];
  asm volatile("" ::"v"(sm) : "memory");
  const int tid = threadIdx.x, l = tid & 63;
  const int w = __builtin_amdgcn_readfirstlane(tid >> 6);
  const int b = blockIdx.x, xcd = b & 7, r = b >> 3;
  const bool mainwg = r < 28;
  const int rp = (xcd & 3) * 4 + (r & 3), cc = (xcd >> 2) * 7 + (r >> 2);
  const int rt = (xcd & 3) * 8 + (xcd >> 2) * 4 + (r & 3), ct = r >> 2;
  const int gw = w & 3;
  const int n16 = l & 15, g4 = l >> 4;
  {
    u64 pc;
    asm volatile("s_getpc_b64 %0" : "=s"(pc));
    asm volatile("s_mov_b32 m0, %3\n\ts_mov_b64 exec, 0xff\n\tbuffer_load_dword %0, %1, %2 offen lds\n\tds_write_b128 %4, %5\n\ts_mov_b64 exec, -1"
                 ::"v"(l * 128), "s"(mk_srd((const void*)pc, CODE_BYTES)), "s"((r & 3) * 8192 + w * 1024), "s"(L_DUM), "v"(L_CNT + l * 16), "v"(u32x4{0, 0, 0, 0}) : "memory");
  }
  u32 vx0 = (l >> 3) * 1024 + (((l & 7) ^ (l >> 3)) << 4), vx1 = vx0 + 8192 - 1024;
  const u32 vwb = (g4 + 1) * (F_ * 2) - 2048, vws = (n16 ^ (g4 << 1)) << 4;
  u32 vo0 = vwb + (vws ^ ((w & 1) << 7)), vo1 = vo0 + 4 * (F_ * 2) - 1024;
  u32 sxw = rp * 131072 + w * 16384, sww = w * 8 * (F_ * 2) + cc * 256;
  asm volatile("" ::"s"(sxw), "s"(sww), "v"(vx0), "v"(vx1), "v"(vo0), "v"(vo1));
  SB();
  asm volatile("" ::"s"(q), "s"(kk), "s"(vv), "s"(sk), "s"(ssk), "s"(sD), "s"(w1), "s"(b1), "s"(w2), "s"(b2), "s"(out), "s"(ws));
  asm volatile("s_mov_b32 m0, %2\n\ts_mov_b64 exec, 3\n\tbuffer_load_dword %0, %1, 0 offen sc1 lds\n\ts_mov_b64 exec, -1" ::"v"(l * 4), "s"(mk_srd(ws, 8)), "s"(L_EP) : "memory");
  if (mainwg) {
    const u32x4 sq_ = mk_srd(q, S_ * D_ * 2), sw_ = mk_srd((const char*)w1 - F_ * 2, D_ * F_ * 2 + F_ * 2);
#pragma unroll
    for (int s = 0; s < 2; ++s) {
      dma16g<0>(sq_, vx0, sxw + s * 128, s * SLOT + w * 4096); dma16g<1>(sq_, vx1, sxw + s * 128, 0);
      dma16g<2>(sw_, vo0, sww + s * 64 * (F_ * 2), 0); dma16g<3>(sw_, vo1, sww + s * 64 * (F_ * 2), 0);
      if (s == 0) bar();
    }
  } else bar();
  SB();
  const u32 vr = (l >> 3) * 1024 + (l & 7) * 16;
  const u32x4 srd_q = mk_srd(q, S_ * D_ * 2);
  const u32x4 srd_w1m = mk_srd((const char*)w1 - F_ * 2, D_ * F_ * 2 + F_ * 2);
  u64 tag;
  auto get_tag = [&]() AI {
    const u32x2 ev = *(const volatile LAS u32x2*)L_EP;
    tag = (((u64)__builtin_amdgcn_readfirstlane(ev.y) << 32) | (u32)__builtin_amdgcn_readfirstlane(ev.x)) + 0x9E3779B97F4A7C15ull;
  };
  const __amdgpu_buffer_rsrc_t rs_ws = __builtin_amdgcn_make_buffer_rsrc(ws, 0, WS_BYTES, 0x00020000);
  u32x4 w2f[28];
  auto load_w2 = [&](int j) AI { w2f[j] = ld128(rs_ws, l * 16 + (j & 3) * 1024, OFF_W2T + (ct * 56 + 7 * w + (j >> 2)) * 4096); SB(); };
  const u32 fw_off = OFF_FW + (4 * w + (ct >> 1)) * FS;
  const u32 fh_off = OFF_FH + ((rt >> 1) * 16 + min(((7 * w) >> 2) + (l & 3), (7 * w + 6) >> 2)) * FS;

  const u32 sx32 = ((2 * g4 + (n16 >> 3)) & 3) << 5, tr0 = (4 * g4 + (n16 >> 2)) * 128 + (l & 3) * 8, vsw = ((l & 7) ^ ((l >> 3) & 6)) << 4;
  const int sp = (r - 28) * 8 + xcd;
  if (w >= 4) {
    u32x4 ha[2]; u32 hn[2], dd;
    auto load_taps = [&]() AI {
      asm volatile("buffer_load_dwordx4 %0, %4, %5, %6 offen nt\n\tbuffer_load_dwordx4 %1, %4, %5, %6 offen offset:1024 nt\n\t"
                   "buffer_load_dword %2, %4, %5, %6 offen offset:16\n\tbuffer_load_dword %3, %4, %5, %6 offen offset:1040"
                   : "=&v"(ha[0]), "=&v"(ha[1]), "=&v"(hn[0]), "=&v"(hn[1]) : "v"(l * 16), "s"(mk_srd(ssk, S_ * D_ * 2)), "s"(b * 8192 + gw * 2048) : "memory");
      asm volatile("buffer_load_ushort %0, off, %1, %2" : "=v"(dd) : "s"(mk_srd(sD, D_ * 2)), "s"(b * 4 + (gw >> 1) * 2) : "memory");
    };
    __builtin_amdgcn_s_setprio(2);
    const u32 vw[4] = {vwb + vws, vwb + vws + 4 * (F_ * 2) - 1024, vwb + (vws ^ 128), vwb + (vws ^ 128) + 4 * (F_ * 2) - 1024};
    auto regions = [&](int s, int r0, auto N) AI {
      const u32 sx = rp * 131072 + r0 * 16384 + s * 128, sw = (r0 * 8 + s * 64) * (F_ * 2) + cc * 256, dst = (s & 3) * SLOT + r0 * 4096;
#pragma unroll
      for (int i = 0; i < N.v; ++i) {
        dma16g<0>(srd_q, vx0, sx + i * 16384, dst + i * 4096); dma16g<1>(srd_q, vx1, sx + i * 16384, 0);
        dma16g<2>(srd_w1m, vw[(i & 1) * 2], sw + i * 8 * (F_ * 2), 0); dma16g<3>(srd_w1m, vw[(i & 1) * 2 + 1], sw + i * 8 * (F_ * 2), 0);
      }
    };
    auto stage = [&](int s) AI { regions(s, gw * 4, IC<4>{}); };
    if (mainwg) {
      regions(2, gw * 2, IC<2>{}); waitv<12>(); cnt_add(C_FULL);
      regions(3, gw * 2, IC<2>{}); waitv<16>(); cnt_add(C_FULL + 4);
    }
    if (gw < 2) {
      if (mainwg) { waitv<8>(); cnt_add(C_FULL + 8); } else waitv<0>();
      cnt_add(C_TG);
      if (mainwg) {
        u32x4 g;
#pragma unroll 1
        for (int s = 4; s < 8; ++s) {
          cnt_wait(C_FREE + 4 * (s - 4), 4);
          if (s == 7) {
            asm volatile("buffer_load_dwordx4 %0, %1, %2, %3 offen sc1" : "=v"(g) : "v"(l * 16), "s"(mk_srd(ws, WS_BYTES)), "s"(OFF_ST + rp * 2048 + gw * 1024) : "memory");
            dma16m(mk_srd(b1, F_ * 2), l * 16, cc * 256, L_B1, 0xffffull);
          }
          stage(s);
          waitv<16>(); cnt_add(C_FULL + 4 * (s - 1));
        }
        get_tag();
        SB();
        asm volatile("" : "+v"(g));
        load_taps();
        auto g_ok = [&]() AI { return __all((g.y == (u32)tag) & (g.w == (u32)(tag >> 32))); };
        if (!g_ok()) {
#pragma unroll 1
          for (int it = 0; it < SPIN_CAP; ++it) { g = ld128(rs_ws, l * 16, OFF_ST + rp * 2048 + gw * 1024); if (g_ok()) break; __builtin_amdgcn_s_sleep(4); CFENCE(); }
        }
        const float i2 = __builtin_amdgcn_rcpf(bfhi(g.z));
        float ra = __builtin_amdgcn_rcpf(bfhi(g.x)) * i2;
        asm volatile("" : "+v"(ra));
        lds_st<f32x2>(L_RC + (gw * 64 + l) * 8, f32x2{ra, -bflo(g.x) * ra - bflo(g.z) * i2});
        lds_st<u32>(L_CC + (gw * 64 + l) * 4, (u32)lds_ld<u16>(L_B1 + (gw * 64 + l) * 2) << 16);
        waitv<5>();
        cnt_add(C_FULL + 28);
      } else { load_taps(); waitv<0>(); get_tag(); }
      cnt_add(C_KV);
    } else {
      waitv<8>(); cnt_add(C_FULL + 8);
      const int pw = gw & 1;
      const u32 so = (rt * 64 + pw * 32) * 1024 + ct * 128;
      const bool top = rt == 0 && pw == 0;
      sfor<0, 4>([&](auto I) AI { dma16g<I.v>(mk_srd(kk, S_ * D_ * 2), vr, so + I.v * 7168, L_PK + 1024 + pw * 4096); });
      dma16m(mk_srd(kk, top ? 0 : S_ * D_ * 2), vr, top ? 0 : so - 8192, L_PK + pw * 4096, ~0ull << 40);
      dma16m(mk_srd(sk, D_ * 8), l * 16, ct * 512, L_SK, 0xffffffffull);
      sfor<0, 4>([&](auto I) AI { dma16g<I.v>(mk_srd(vv, S_ * D_ * 2), vr, so + I.v * 7168, L_PV + pw * 4096); });
      waitv<10>(); cnt_add(C_FULL + 12);
      waitv<0>();
      get_tag();
      load_taps();
      const int cpl = l & 31, hh = l >> 5;
      const u32x4 sv = lds_ld<u32x4>(L_SK + cpl * 16);
      const f32x2 kT[4] = {f32x2{bflo(sv.x), bflo(sv.z)}, f32x2{bfhi(sv.x), bfhi(sv.z)}, f32x2{bflo(sv.y), bflo(sv.w)}, f32x2{bfhi(sv.y), bfhi(sv.w)}};
      u32 a = L_PK + (pw * 32 + hh * 16) * 128 + cpl * 4;
      asm volatile("" : "+v"(a));
      u32 kr[19], vq[16];
#pragma unroll
      for (int i = 0; i < 19; ++i) kr[i] = lds_ld<u32>(a + 5 * 128 + i * 128);
#pragma unroll
      for (int i = 0; i < 16; ++i) vq[i] = lds_ld<u32>(a + (L_PV - L_PK) + i * 128);
      SB();
      f32x2 kK[19];
#pragma unroll
      for (int i = 0; i < 3; ++i) kK[i] = bf2(kr[i]);
      u32x4 uo[4];
#pragma unroll
      for (int u = 0; u < 4; ++u) {
        f32x2 ka[4];
        EACH4(kK[4 * u + 3 + j] = bf2(kr[4 * u + 3 + j]));
        EACH4(ka[j] = mul2s(kK[4 * u + j], kT[3])); EACH4(ka[j] = fma2s(kK[4 * u + j + 1], kT[2], ka[j]));
        EACH4(ka[j] = fma2s(kK[4 * u + j + 2], kT[1], ka[j])); EACH4(ka[j] = fma2s(kK[4 * u + j + 3], kT[0], ka[j]));
        EACH4(ka[j] = rb2(ka[j])); EACH4(ka[j] = mul2s(ka[j], bf2(vq[4 * u + j])));
        uo[u] = u32x4{pk2(ka[3].x, ka[2].x), pk2(ka[1].x, ka[0].x), pk2(ka[3].y, ka[2].y), pk2(ka[1].y, ka[0].y)};
        SB();
      }
#pragma unroll
      for (int u = 0; u < 4; ++u) lds_st<u32x4>(L_PV + pw * 4096 + cpl * 128 + (((hh * 4 + u) ^ (cpl & 7)) << 4), uo[u]);
#pragma unroll
      for (int i = 0; i < 4; ++i) uo[i] = lds_ld<u32x4>(L_PV + pw * 4096 + i * 1024 + (l >> 3) * 128 + (((l & 7) ^ (l >> 3)) << 4));
#pragma unroll
      for (int i = 0; i < 4; ++i) st128(uo[i], rs_ws, (l >> 3) * 8192 + (l & 7) * 16, OFF_U + ((ct * 32 + 8 * i) * 512 + rt * 16 + pw * 8) * 16);
      cnt_add(C_KV);
      waitv<0>();
      cnt_wait(C_TG, 6);
      arrive_flag(ws, C_U, 2, OFF_FU + (ct * 32 + rt) * FS, tag, l);
    }
    if (!mainwg) cnt_wait(C_W, 4);
    poll_flags(ws, fw_off, tag);
#pragma unroll
    for (int j = 0; j < 12; ++j) load_w2(j);
    __builtin_amdgcn_s_setprio(3);
    cnt_wait(C_KV, 4);
    if (mainwg) cnt_wait(C_FREE + 16, 4);
    asm volatile("" : "+v"(ha[0]), "+v"(ha[1]), "+v"(hn[0]), "+v"(hn[1]), "+v"(dd));
    {
      const u32 hb = (gw >> 1) ? L_HC1 : L_HC0;
      const u32 h0 = pk2(0.f, bflo(ha[0].x) + bflo(dd));
      if ((tid & 127) == 0) ha[0].x = __builtin_amdgcn_perm(ha[0].x, h0, 0x07060302u);
      const u32 h0u = __builtin_amdgcn_readfirstlane(h0);
      if (gw & 1) { if (l < 60) lds_st<u32x2>(L_UR + (gw >> 1) * URS + 4096 + l * 8, u32x2{0, 0}); }
      else if (l < 31 && l != 23) lds_st<u32>(hb + (l >> 3) * HCS + (l & 7) * 4, (l == 15 || l == 30) ? h0u : 0u);
#pragma unroll
      for (int i = 0; i < 2; ++i) {
        const u32x4 x = ha[i];
        const u32x4 o = {__builtin_amdgcn_alignbit(x.y, x.x, 16), __builtin_amdgcn_alignbit(x.z, x.y, 16), __builtin_amdgcn_alignbit(x.w, x.z, 16), __builtin_amdgcn_alignbit(hn[i], x.w, 16)};
        const u32 d = hb + 32 + (gw & 1) * 2048 + i * 1024 + l * 16, d2 = d + 2 * HCS - 4, d3 = d + 3 * HCS - 4;
        lds_st<u32x4>(d, x); lds_st<u32x4>(d + HCS, o);
        asm volatile("ds_write2_b32 %0, %2, %3 offset1:1\n\tds_write2_b32 %0, %4, %5 offset0:2 offset1:3\n\tds_write2_b32 %1, %6, %7 offset1:1\n\tds_write2_b32 %1, %8, %9 offset0:2 offset1:3"
                     ::"v"(d2), "v"(d3), "v"(x.x), "v"(x.y), "v"(x.z), "v"(x.w), "v"(o.x), "v"(o.y), "v"(o.z), "v"(o.w) : "memory");
      }
    }
    if (!mainwg) __builtin_amdgcn_s_sleep(32);
    const u32 fu_off = OFF_FU + ((b >> 5) * 32 + 8 * gw + (l & 7)) * FS;
#pragma unroll
    for (int j = 12; j < 20; ++j) load_w2(j);
    poll_flags(ws, fu_off, tag);
    u32x4 uu[2];
#pragma unroll
    for (int i = 0; i < 2; ++i) uu[i] = ld128(rs_ws, l * 16 + i * 1024, OFF_U + b * 8192 + gw * 2048);
    SB();
#pragma unroll
    for (int j = 20; j < 28; ++j) load_w2(j);
#pragma unroll
    for (int i = 0; i < 2; ++i) {
      const u32 a = L_UR + (2044 - 4 * (gw * 128 + i * 64 + l)) * 2;
      lds_st<u32x2>(a, u32x2{uu[i].x, uu[i].y}); lds_st<u32x2>(a + URS, u32x2{uu[i].z, uu[i].w});
    }
    cnt_add(C_S2);
    cnt_wait(C_S2, mainwg ? 8 : 4);
    {
      const int ch = gw & 1, kh = gw >> 1;
      f32x4 ca[8];
#pragma unroll
      for (int i = 0; i < 8; ++i) ca[i] = f32x4{0, 0, 0, 0};
      const u32 ab = (ch ? L_HC1 : L_HC0) + ((n16 + 1) & 3) * HCS + (((n16 + 1) & ~3) + 8 * g4) * 2 + kh * 256;
      const u32 bb = L_UR + ch * URS + (2032 - 1792 - 16 * n16 + 8 * g4) * 2 + kh * 256;
      const u32 pa = L_CP + gw * 8192 + (4 * n16 + (g4 ^ ((n16 >> 1) & 3))) * 16;
      u32x4 AL[2][4], BL[2][4], AH[4], BH[4];
      auto rdp = [&](u32x4& a, u32x4& bq, int k0, int i) AI { SB(); a = lds_2x64(ab + k0 * 64 + i * 512); bq = lds_ld<u32x4>(bb + k0 * 64 + (7 - i) * 512); SB(); };
#pragma unroll
      for (int i = 0; i < 4; ++i) rdp(AL[0][i], BL[0][i], 0, i);
      sfor<0, 4>([&](auto K) AI {
        constexpr int k0 = K.v;
        u32x4 (&A)[4] = AL[k0 & 1]; u32x4 (&B)[4] = BL[k0 & 1];
#pragma unroll
        for (int n = 0; n < 16; ++n) {
          constexpr int oi[16] = {0, 0, 1, 0, 1, 2, 0, 1, 2, 3, 1, 2, 3, 2, 3, 3}, ot[16] = {0, 1, 0, 2, 1, 0, 3, 2, 1, 0, 3, 2, 1, 3, 2, 3};
          ca[oi[n] + ot[n]] = mfma(A[oi[n]], B[ot[n]], ca[oi[n] + ot[n]]);
          if (n < 8 && (n & 1)) rdp(AH[n >> 1], BH[n >> 1], k0, 4 + (n >> 1));
          if (k0 == 3 && n >= 12) { SB(); lds_st<f32x4>(pa + (n - 12) * 1024, ca[n - 12]); SB(); }
        }
        int cn = 0;
#pragma unroll
        for (int T = 0; T < 4; ++T)
#pragma unroll
          for (int i = 0; i <= T; ++i) {
            ca[4 + T] = mfma(A[i], BH[T - i], ca[4 + T]); ca[4 + T] = mfma(AH[T - i], B[i], ca[4 + T]);
            if (k0 < 3 && cn >= 2 && cn < 6) rdp(AL[(k0 + 1) & 1][cn - 2], BL[(k0 + 1) & 1][cn - 2], k0 + 1, cn - 2);
            if (k0 == 3 && T > 0 && i == 1) { SB(); lds_st<f32x4>(pa + (3 + T) * 1024, ca[3 + T]); SB(); }
            ++cn;
          }
        SB();
      });
      lds_st<f32x4>(pa + 7 * 1024, ca[7]);
    }
    cnt_add(C_S3);
    __builtin_amdgcn_s_setprio(0);
    const u64 fhv = flag_ld(ws, fh_off);
    cnt_wait(C_S3, 4);
    {
      const int t2 = tid & 255;
      f32x4 pr[2][4];
#pragma unroll
      for (int i = 0; i < 2; ++i)
#pragma unroll
        for (int p = 0; p < 4; ++p) pr[i][p] = lds_ld<f32x4>(L_CP + ((t2 & ~3) | ((t2 & 3) ^ ((t2 >> 3) & 3))) * 16 + i * 4096 + p * 8192);
      cnt_add(C_S1);
      SB();
#pragma unroll
      for (int i = 0; i < 2; ++i) {
        const f32x4 y0 = pr[i][0] + pr[i][2], y1 = pr[i][1] + pr[i][3];
        st128(u32x4{pk2(y0[0], y1[0]), pk2(y0[1], y1[1]), pk2(y0[2], y1[2]), pk2(y0[3], y1[3])}, rs_ws, (t2 >> 4) * 65536 + (t2 & 15) * 16, OFF_Y + b * 256 + i * 16 * 65536);
      }
    }
    flag_chk(fhv, ws, fh_off, tag);
  } else {
    const int srow = rt * 64 + ct * 8 + gw * 2;
    dma16g<0>(srd_q, l * 16, srow * 1024, L_PW + gw * 2048); dma16g<1>(srd_q, l * 16, srow * 1024, 0);
    auto stats = [&]() AI
    {
      const u32x4 x0[2] = {lds_ld<u32x4>(L_PW + gw * 2048 + l * 16), lds_ld<u32x4>(L_PW + gw * 2048 + 1024 + l * 16)};
      float c0[2], sa[2], sq[2];
#pragma unroll
      for (int i = 0; i < 2; ++i) {
        c0[i] = bflo(__builtin_amdgcn_readfirstlane(x0[i].x));
        const f32x2 d[4] = {bf2(x0[i].x) - sp2(c0[i]), bf2(x0[i].y) - sp2(c0[i]), bf2(x0[i].z) - sp2(c0[i]), bf2(x0[i].w) - sp2(c0[i])};
        const f32x2 s2 = (d[0] + d[1]) + (d[2] + d[3]), q2 = fma2(d[0], d[0], d[1] * d[1]) + fma2(d[2], d[2], d[3] * d[3]);
        sa[i] = s2.x + s2.y; sq[i] = q2.x + q2.y;
      }
#define RED4(C) do { sa[0] = dpp_add<C>(sa[0]); sq[0] = dpp_add<C>(sq[0]); sa[1] = dpp_add<C>(sa[1]); sq[1] = dpp_add<C>(sq[1]); SB(); } while (0)
      SB(); RED4(0xB1); RED4(0x4E); RED4(0x141); RED4(0x140); RED4(0x142); RED4(0x143);
      const bool odd = l & 1;
      const float md = sel(odd, sa[1], sa[0]) * (1.f / D_), mean = sel(odd, c0[1], c0[0]) + md, var = fmaxf(sel(odd, sq[1], sq[0]) * (1.f / D_) - md * md, 0.f);
      const float m1 = rb(mean), sd1 = rb(__builtin_amdgcn_sqrtf(rb(rb(var) + 1e-5f)));
      const float i1 = __builtin_amdgcn_rcpf(sd1);
      const float m2 = (mean - m1) * i1, sd2 = __builtin_amdgcn_sqrtf(rb(rb(var * i1 * i1) + 1e-5f));
      if (l >= 62) st128(u32x4{pk2(m1, sd1), (u32)tag, pk2(m2, sd2), (u32)(tag >> 32)}, rs_ws, (l - 62) * 16, OFF_ST + srow * 16);
    };
    {
      __builtin_amdgcn_s_setprio(3);
      if (!mainwg) {
#pragma unroll
        for (int p = 0; p < 4; ++p) {
          const int hp = gw * 4 + p;
          sfor<0, 4>([&](auto I) AI { dma16g<I.v>(mk_srd(w2, hp < 14 ? D_ * F_ * 2 : 0), (l >> 3) * 1024 + vsw,
                                                       ((sp >> 2) * 7 + (hp >> 1)) * 32768 + (sp & 3) * 256 + (hp & 1) * 128 + I.v * 7168, hp * 4096); });
        }
      }
      if (mainwg) waitv<10>(); else waitv<16>();
      get_tag(); cnt_add(C_TG);
      if (!mainwg) stats();
      __builtin_amdgcn_s_setprio(1);
      if (!mainwg) {
        waitv<1>();
        SB();
#pragma unroll
        for (int p = 0; p < 4; ++p) {
          const int hp = gw * 4 + p;
          u32x4 wt[4];
#pragma unroll
          for (int nt = 0; nt < 4; ++nt) { const u32 a = hp * 4096 + tr0 + ((nt * 32) ^ sx32); wt[nt] = lds_tr2(a, a + 2048); }
#pragma unroll
          for (int nt = 0; nt < 4; ++nt)
            st128(wt[nt], rs_ws, l * 16, hp < 14 ? OFF_W2T + ((((sp & 3) * 2 + (hp & 1)) * 56 + (sp >> 2) * 7 + (hp >> 1)) * 4 + nt) * 1024 : OOR);
        }
        waitv<0>();
        arrive_flag(ws, C_W, 4, OFF_FW + sp * FS, tag, l);
      }
    }
    if (mainwg) {
      const int th = gw >> 1, fh = gw & 1;
      f32x4 acc[4][4];
#pragma unroll
      for (int i = 0; i < 16; ++i) acc[i >> 2][i & 3] = f32x4{0, 0, 0, 0};
      const u32 xa = th * 16384 + n16 * 128 + ((g4 ^ (n16 & 7)) << 4);
      const u32 wa = g4 * 4096 + 2048 + (n16 >> 2) * 256 + (((4 * fh + 2 * th) ^ ((n16 >> 2) | ((g4 & 1) << 2))) << 5) + (l & 3) * 8;
      u32 ad[2][6];
#pragma unroll
      for (int h = 0; h < 2; ++h) {
#pragma unroll
        for (int e = 0; e < 4; ++e) ad[h][e] = (wa ^ (e * 32)) + h * 65536;
        ad[h][4] = xa + h * 65536; ad[h][5] = (xa ^ 64) + h * 65536;
        asm volatile("" : "+v"(ad[h][0]), "+v"(ad[h][1]), "+v"(ad[h][2]), "+v"(ad[h][3]), "+v"(ad[h][4]), "+v"(ad[h][5]));
      }
      u32x4 xf[2][4], wf[2][4];
      f32x4 cf[4], bf[4]; f32x2 rc[4];
      auto rd = [&](int hs, int p) AI {
        const int s = hs >> 1, ks = hs & 1, hi = (s >> 1) & 1, e = p >> 1;
        const u32 so = (s & 1) * SLOT;
        if (hs > 15) {
          if (p < 4) bf[p] = lds_ld<f32x4>(L_CC + fh * 256 + ((p * 64) ^ (th * 128)) + g4 * 16);
          else rc[p - 4] = lds_ld<f32x2>(L_RC + (th * 64 + (p - 4) * 16 + n16) * 8);
        } else if (!(p & 1)) { const u32 a = ad[hi][e] + so + ks * 16384; wf[ks][e] = lds_tr2(a, a + 1024); }
        else xf[ks][e] = lds_ld<u32x4>(ad[hi][4 + ks] + so + e * 4096);
      };
      u32 fullv;
      u32x4 ones = {0x3f803f80u, 0x3f803f80u, 0x3f803f80u, 0x3f803f80u};
      asm volatile("" : "+v"(ones));
#pragma unroll
      for (int i = 0; i < 4; ++i) cf[i] = f32x4{0, 0, 0, 0};
      auto half = [&](int hs) AI {
        const int ks = hs & 1;
        sfor<0, 16>([&](auto J) AI {
          constexpr int j = J.v, o = j < 4 ? (j & 1) * 4 + (j >> 1) : j < 9 ? (j == 4 ? 8 : j == 5 ? 9 : j == 6 ? 2 : j == 7 ? 6 : 10) : (j < 12 ? 12 + (j - 9) : (j - 12) * 4 + 3);
          acc[o >> 2][o & 3] = mfma(wf[ks][o >> 2], xf[ks][o & 3], acc[o >> 2][o & 3]);
          SB();
          if (j < 12 && j % 3 != 1) rd(hs + 1, (j / 3) * 2 + (j % 3) / 2);
          if (ks == 0 && j == 12) cnt_add(C_FREE + 2 * hs);
          if (ks == 0 && j == 13 && hs < 14) fullv = cnt_ld(C_FULL + 2 * hs + 4);
          SB();
        });
        cf[0] = mfma(wf[ks][0], ones, cf[0]); cf[1] = mfma(wf[ks][1], ones, cf[1]);
        SB();
      };
      waitv<6>(); cnt_add(C_FULL);
      cnt_wait(C_FULL, 8);
#pragma unroll
      for (int p = 0; p < 8; ++p) rd(0, p);
      SB();
      sfor<0, 8>([&](auto S) AI {
        constexpr int s = S.v;
        half(2 * s);
        constexpr u32 need = s == 0 ? 8 : s < 3 ? 4 : 2;
        if (s == 0) { waitv<2>(); cnt_add(C_FULL + 4); }
        if (s == 1) { waitv<0>(); SB(); stats(); fullv = cnt_ld(C_FULL + 4 * s + 4); }
        if (s < 7 && (u32)__builtin_amdgcn_readfirstlane(fullv) < need) cnt_wait(C_FULL + 4 * s + 4, need);
        CFENCE(); SB();
        half(2 * s + 1);
      });
      __builtin_amdgcn_s_setprio(2);
      if (n16 == 0) { lds_st<f32x4>(L_CSX + gw * 128 + g4 * 16, cf[0]); lds_st<f32x4>(L_CSX + gw * 128 + 64 + g4 * 16, cf[1]); }
      cnt_add(C_SA + fh * 4);
      const float c1 = -2.f * 0.7978845608028654f * 1.4426950408889634f, c2 = c1 * 0.044715f;
      const u32 hso = OFF_H + ((rp * 8 + th * 4) * 56 + cc * 4 + fh * 2) * 1024;
#pragma unroll
      for (int u = 0; u < 8; ++u) {
        const int tt = u & 3, kp = u >> 2;
        if (u == 3) { cnt_wait(C_SA + fh * 4, 2); cf[2] = lds_ld<f32x4>(L_CSX + (gw ^ 2) * 128 + g4 * 16); cf[3] = lds_ld<f32x4>(L_CSX + (gw ^ 2) * 128 + 64 + g4 * 16); SB(); }
        f32x2 x[4], p[4]; u32 hp[4];
        EACH4(x[j] = fma2(HALF(cf[kp * 2 + (j >> 1)], j), sp2(rc[tt].y), HALF(bf[kp * 2 + (j >> 1)], j)));
        EACH4(x[j] = fma2(HALF(acc[kp * 2 + (j >> 1)][tt], j), sp2(rc[tt].x), x[j]));
        EACH4(p[j] = x[j] * x[j]);
        EACH4(p[j] = fma2(p[j], sp2(c2), sp2(c1)));
        EACH4(p[j] = p[j] * x[j]);
        EACH4(p[j] = exp2x2(p[j]));
        EACH4(p[j] = p[j] + sp2(1.f));
        EACH4(p[j] = rcpx2(p[j]));
        EACH4(p[j] = p[j] * x[j]);
        EACH4(hp[j] = pk2(p[j].x, p[j].y));
        st128(u32x4{hp[0], hp[1], hp[2], hp[3]}, rs_ws, l * 16, hso + tt * 56 * 1024 + ((kp * 1024) ^ (th * 1024)));
        SB();
      }
      cnt_add(C_S2);
    }
    if (!mainwg) __builtin_amdgcn_s_sleep(32);
    poll_flags(ws, fw_off, tag);
    waitv<0>();
    if (mainwg) arrive_flag(ws, C_H, 4, OFF_FH + (rp * 16 + cc) * FS, tag, l);
    __builtin_amdgcn_s_setprio(0);
#pragma unroll
    for (int j = 0; j < 20; ++j) load_w2(j);
    const u64 fhv = flag_ld(ws, mainwg ? fh_off : OFF_E + 64);
#pragma unroll
    for (int j = 20; j < 28; ++j) load_w2(j);
    if (!mainwg) {
      cnt_wait(C_S2, 4);
      if (b == 255) {
        poll_flags(ws, OFF_FU + (gw * 64 + l) * FS, tag);
        arrive_flag(ws, C_EP, 4, OFF_E, tag, l);
      }
      cnt_wait(C_S3, 4);
    }
    if (mainwg) flag_chk(fhv, ws, fh_off, tag); else poll_flags(ws, fh_off, tag);
  }
  u32x4 hf[28];
  auto load_h = [&](int n) AI { if (n < 28) { hf[n] = ld128(rs_ws, l * 16, OFF_H + ((rt * 4 + n / 7) * 56 + 7 * w + n % 7) * 1024); SB(); } };
  if (w >= 4) __builtin_amdgcn_s_setprio(1);
  const int tok = tid >> 3, cg = tid & 7;
  const u32x4 qv = *(const GAS u32x4*)(q + (rt * 64 + tok) * D_ + ct * 64 + cg * 8), b2v = *(const GAS u32x4*)(b2 + ct * 64 + cg * 8);
  u32x2 stf = __builtin_amdgcn_raw_buffer_load_b64(rs_ws, tok * 16, OFF_ST + rt * 1024, 16);
  CFENCE(); SB();
#pragma unroll
  for (int n = 0; n < HLA; ++n) load_h(n);
  f32x2 xn[4], xq[4], bq[4];
  if (!__all(stf.y == (u32)tag)) {
#pragma unroll 1
    for (int it = 0; it < SPIN_CAP; ++it) {
      CFENCE();
      stf = __builtin_amdgcn_raw_buffer_load_b64(rs_ws, tok * 16, OFF_ST + rt * 1024, 16);
      if (__all(stf.y == (u32)tag)) break;
    }
  }
  {
    const f32x2 m = sp2(bflo(stf.x)), iv = sp2(__builtin_amdgcn_rcpf(bfhi(stf.x)));
    EACH4((xq[j] = bf2(qv[j]), bq[j] = bf2(b2v[j])));
    EACH4(xn[j] = rb2(rb2(xq[j] - m) * iv));
    asm volatile("" : "+v"(xn[0]), "+v"(xn[1]), "+v"(xn[2]), "+v"(xn[3]), "+v"(xq[0]), "+v"(xq[1]), "+v"(xq[2]), "+v"(xq[3]));
    asm volatile("" : "+v"(bq[0]), "+v"(bq[1]), "+v"(bq[2]), "+v"(bq[3]));
    SB();
  }
  const u32 fy_off = OFF_FY + (ct * 32 + (l & 31)) * FS;
  u64 fyv; u32 yv[4];
  f32x4 a2[2][4];
  const u32 wa2 = (w < 4 ? 65536 : 0) + gw * 4096 + n16 * 256;
  auto pwrite = [&](int t, int nt) AI { lds_st<f32x4>(t * 16384 + wa2 + (((4 * nt + g4) ^ (n16 & 7)) << 4), a2[t & 1][nt]); SB(); };
#pragma unroll
  for (int t = 0; t < 4; ++t) {
#pragma unroll
    for (int i = 0; i < 7; ++i) {
      if (t == 0 && i == 0 && w >= 4) { waitv<0>(); arrive_flag(ws, C_Y, 4, OFF_FY + b * FS, tag, l); SB(); }
      if (t == 1 && i == 0 && (w >= 4 || mainwg)) cnt_wait(w < 4 ? C_FREE + 28 : C_S1, 4);
#pragma unroll
      for (int nt = 0; nt < 4; ++nt) a2[t & 1][nt] = mfma(w2f[i * 4 + nt], hf[t * 7 + i], i ? a2[t & 1][nt] : f32x4{0, 0, 0, 0});
      SB();
      if (t > 0 && i < 4) pwrite(t - 1, i);
      if (t > 0 && i == 4) cnt_add(C_T + 4 * (t - 1));
      load_h(t * 7 + i + HLA);
      if (t * 7 + i == 24) { fyv = flag_ld(ws, fy_off); SB(); }
    }
  }
#pragma unroll
  for (int nt = 0; nt < 4; ++nt) pwrite(3, nt);
  cnt_add(C_T + 12);
  u32 ra0 = (tok >> 4) * 16384 + (tok & 15) * 256 + (((2 * cg) ^ (tok & 7)) << 4), ra1 = ra0 ^ 16, ra2 = ra0 + 65536, ra3 = ra1 + 65536;
  asm volatile("" : "+v"(ra0), "+v"(ra1), "+v"(ra2), "+v"(ra3));
  auto load_y = [&](bool go) AI { EACH4(yv[j] = __builtin_amdgcn_raw_buffer_load_b32(rs_ws, (cg * 4 + j) * 256 + tok * 4, go ? OFF_Y + rt * 65536 + ct * 8192 : OOR, 16)); };
  const bool yok = __all(fyv == tag);
  CFENCE();
  load_y(yok);
  cnt_wait(C_T + 4 * (w >> 1), 8);
  {
    f32x4 pr[16];
#pragma unroll
    for (int i = 0; i < 4; ++i) { pr[i] = lds_ld<f32x4>(ra0 + i * 4096); pr[4 + i] = lds_ld<f32x4>(ra2 + i * 4096); }
#pragma unroll
    for (int i = 0; i < 4; ++i) { pr[8 + i] = lds_ld<f32x4>(ra1 + i * 4096); pr[12 + i] = lds_ld<f32x4>(ra3 + i * 4096); }
    SB();
    const f32x4 s0 = ((pr[0] + pr[4]) + (pr[1] + pr[5])) + ((pr[2] + pr[6]) + (pr[3] + pr[7]));
    const f32x4 s1 = ((pr[8] + pr[12]) + (pr[9] + pr[13])) + ((pr[10] + pr[14]) + (pr[11] + pr[15]));
    f32x2 z[4] = {s0.lo, s0.hi, s1.lo, s1.hi}, p[4];
    EACH4(z[j] += bq[j]);
    EACH4(p[j] = exp2x2(z[j] * sp2(-1.4426950408889634f)));
    EACH4(p[j] += sp2(1.f));
    EACH4(p[j] = rcpx2(p[j]));
    EACH4(z[j] *= p[j]);
    asm volatile("" : "+v"(z[0]), "+v"(z[1]), "+v"(z[2]), "+v"(z[3]));
    if (!yok) { poll_flags(ws, fy_off, tag); load_y(true); }
    EACH4(p[j] = fma2(rb2(xn[j] * bf2(yv[j])), z[j], xq[j]));
    __builtin_amdgcn_raw_buffer_store_b128(u32x4{pk2(p[0].x, p[0].y), pk2(p[1].x, p[1].y), pk2(p[2].x, p[2].y), pk2(p[3].x, p[3].y)},
        __builtin_amdgcn_make_buffer_rsrc(out, 0, S_ * D_ * 2, 0x00020000), tok * 1024 + cg * 16, rt * 65536 + ct * 128, 16);
  }
}

extern "C" void kernel_launch(void* const* d_in, const int* in_sizes, int n_in, void* d_out, int out_size, void* d_ws, size_t ws_size,
                              hipStream_t stream) {
  fused_kernel<<<256, 512, 0, stream>>>((const u16*)d_in[0], (const u16*)d_in[1], (const u16*)d_in[2], (const u16*)d_in[3], (const u16*)d_in[4],
                                        (const u16*)d_in[5], (const u16*)d_in[7], (const u16*)d_in[8], (const u16*)d_in[9], (const u16*)d_in[10],
                                        (u16*)d_out, (char*)d_ws);
}
```
